```python
import torch

def _softmax(x, axis=-1):
    return torch.softmax(x, dim=axis)
def _gelu(x):
    return torch.nn.functional.gelu(x, approximate="tanh")
def _silu(x):
    return torch.nn.functional.silu(x)
def _sigmoid(x):
    return torch.sigmoid(x)
def _layernorm(x, axis=-1, eps=1e-5):
    m = x.mean(dim=axis, keepdim=True)
    v = x.var(dim=axis, keepdim=True, unbiased=False)
    return (x - m) / torch.sqrt(v + eps)
def _scatter_add(tgt, idx, src, axis=0):
    return tgt.clone().index_add_(axis, idx, src)
def _segment_sum(src, seg, n):
    out = torch.zeros((n,) + src.shape[1:], dtype=src.dtype)
    return out.index_add_(0, seg, src)
def _pad(x, pw):
    flat = tuple(v for pair in reversed(pw) for v in pair)
    return torch.nn.functional.pad(x, flat)

kernel_name = 'interleave_layers__mha_block_cached__glu_gated_reglu__decode'

def setup_inputs():

    _w = (lambda a: (lambda t: t.to(torch.bfloat16) if t.is_floating_point() else t)(torch.as_tensor(a)))
    def _A_setup():
        B, Sq, D = 4, 1, 448
        H, Sk, Dh = 16, 4096, 256
        return {
            "x": _w((lambda *s: torch.randn(*s, dtype=torch.bfloat16) * 0.05)(B, Sq, D)),
            "k_cache": _w((lambda *s: torch.randn(*s, dtype=torch.bfloat16) * 0.8)(B, H, Sk, Dh)),
            "v_cache": _w((lambda *s: torch.randn(*s, dtype=torch.bfloat16) * 0.8)(B, H, Sk, Dh)),
            "wq": _w((lambda *s: torch.randn(*s, dtype=torch.bfloat16) * 0.8)(D, H * Dh) * (D ** -0.5)),
            "wo": _w((lambda *s: torch.randn(*s, dtype=torch.bfloat16) * 0.8)(H * Dh, D) * ((H * Dh) ** -0.5)),
        }
    _A_in = _A_setup()

    def _B_setup():
        B, S, D, F = 4, 1, 448, 1792
        return {
            "x": _w((lambda *s: torch.randn(*s, dtype=torch.bfloat16) * 0.05)(B, S, D)),
            "W_gate": _w((lambda *s: torch.randn(*s, dtype=torch.bfloat16) * 0.2)(D, F) * (2.0 / D) ** 0.5),
            "W_up": _w((lambda *s: torch.randn(*s, dtype=torch.bfloat16) * 0.2)(D, F) * (2.0 / D) ** 0.5),
            "W_down": _w((lambda *s: torch.randn(*s, dtype=torch.bfloat16) * 0.2)(F, D) * (1.0 / F) ** 0.5),
        }
    _B_in = _B_setup()

    return {
        "A_x": _A_in["x"],
        "A_k_cache": _A_in["k_cache"],
        "A_v_cache": _A_in["v_cache"],
        "A_wq": _A_in["wq"],
        "A_wo": _A_in["wo"],
        "B_x": _B_in["x"],
        "B_W_gate": _B_in["W_gate"],
        "B_W_up": _B_in["W_up"],
        "B_W_down": _B_in["W_down"],
    }

def reference(A_x, A_k_cache, A_v_cache, A_wq, A_wo, B_x, B_W_gate, B_W_up, B_W_down):
    def _A_ref(x, k_cache, v_cache, wq, wo):
        B, Sq, D = x.shape
        H, Dh = 16, 256
        q = (lambda x, *d: x.permute(*d))((x @ wq).reshape(B, Sq, H, Dh), 0, 2, 1, 3)
        scores = torch.einsum("bhqd,bhkd->bhqk", q, k_cache) * (Dh ** -0.5)
        p = _softmax(scores, axis=-1)
        o = torch.einsum("bhqk,bhkd->bhqd", p, v_cache)
        return (lambda x, *d: x.permute(*d))(o, 0, 2, 1, 3).reshape(B, Sq, H * Dh) @ wo

    def _B_ref(x, W_gate, W_up, W_down):
        gate = torch.einsum("bsd,df->bsf", x, W_gate)
        up = torch.einsum("bsd,df->bsf", x, W_up)
        gate = gate * (lambda x, dt: x.to(dt))(gate > 0, gate.dtype)
        h = gate * up
        return torch.einsum("bsf,fd->bsd", h, W_down)

    x = A_x
    x = x + _A_ref(_layernorm(x, axis=-1), A_k_cache, A_v_cache, A_wq, A_wo)
    x = x + _B_ref(_layernorm(x, axis=-1), B_W_gate, B_W_up, B_W_down)
    x = x + _A_ref(_layernorm(x, axis=-1), A_k_cache, A_v_cache, A_wq, A_wo)
    x = x + _B_ref(_layernorm(x, axis=-1), B_W_gate, B_W_up, B_W_down)
    return x
```

```cpp
#include <hip/hip_runtime.h>
#include <hip/hip_bf16.h>
#include <stdint.h>

#define MIMIC 1

typedef unsigned short u16;
typedef unsigned u32x4 __attribute__((ext_vector_type(4)));
constexpr int B=4, D=448, H=16, SK=4096, DH=256, F=1792, NS=16;

__device__ __forceinline__ float bf2f(u16 v){ return __uint_as_float(((unsigned)v)<<16); }
__device__ __forceinline__ u16 f2bf(float f){ __hip_bfloat16 h = __float2bfloat16(f); return *(u16*)&h; }
__device__ __forceinline__ float rb(float f){
  return bf2f(f2bf(f));
}
__device__ __forceinline__ float wsum(float v){ for(int o=32;o;o>>=1) v+=__shfl_xor(v,o); return v; }
__device__ __forceinline__ float wmax(float v){ for(int o=32;o;o>>=1) v=fmaxf(v,__shfl_xor(v,o)); return v; }

__device__ void ln_row(const float* x, float* z){
  int t=threadIdx.x;
  if(t<64){
    float v[7]; float s=0;
    for(int i=0;i<7;i++){ v[i]=x[t+64*i]; s+=v[i]; }
    s=wsum(s); float mean=s/D; float q=0;
    for(int i=0;i<7;i++){ float d=v[i]-mean; q+=d*d; }
    q=wsum(q); float var=rb(q/D); float m=rb(mean);
    float sd=rb(sqrtf(rb(var+1e-5f)));
    for(int i=0;i<7;i++) z[t+64*i]=rb(rb(v[i]-m)/sd);
  }
  __syncthreads();
}
__global__ void k_init(const u16* x, float* xf){ int i=blockIdx.x*256+threadIdx.x; if(i<B*D) xf[i]=bf2f(x[i]); }
__global__ void k_q(const float* xf, const u16* wq, float* q){
  __shared__ float z[D];
  int b=blockIdx.y; ln_row(xf+b*D,z);
  int c=blockIdx.x*256+threadIdx.x; float a=0;
  for(int k=0;k<D;k++) a+=z[k]*bf2f(wq[(size_t)k*4096+c]);
  q[b*4096+c]=rb(a);
}
__global__ void k_attn(const float* q, const u16* K, const u16* V, float* po, float* pml){
  __shared__ float sc[256]; __shared__ float red[4][256]; __shared__ float wm[4];
  int s=blockIdx.x,h=blockIdx.y,b=blockIdx.z; int t=threadIdx.x,l=t&63,w=t>>6;
  size_t base=((size_t)(b*H+h)*SK+s*256)*DH;
  int c=l&31, r=l>>5; float qv[8];
  for(int i=0;i<8;i++) qv[i]=q[b*4096+h*256+c*8+i];
  #pragma unroll 8
  for(int i=0;i<32;i++){ int key=w*64+i*2+r;
    u32x4 kv=*(const u32x4*)(K+base+(size_t)key*DH+c*8); float a=0;
    for(int j=0;j<4;j++){ a+=qv[2*j]*__uint_as_float(kv[j]<<16)+qv[2*j+1]*__uint_as_float(kv[j]&0xffff0000u);}
    for(int o=16;o;o>>=1) a+=__shfl_xor(a,o);
    if(c==0) sc[key]=rb(rb(a)*0.0625f);
  }
  __syncthreads();
  float v=sc[t]; float m=wmax(v); if(l==0) wm[w]=m; __syncthreads();
  m=fmaxf(fmaxf(wm[0],wm[1]),fmaxf(wm[2],wm[3])); __syncthreads();
  float p=__expf(v-m); sc[t]=p; float ls=wsum(p); if(l==0) wm[w]=ls; __syncthreads();
  ls=wm[0]+wm[1]+wm[2]+wm[3];
  float acc[8]={0,0,0,0,0,0,0,0};
  #pragma unroll 8
  for(int i=0;i<32;i++){ int key=w*64+i*2+r;
    u32x4 vv=*(const u32x4*)(V+base+(size_t)key*DH+c*8); float pp=sc[key];
    for(int j=0;j<4;j++){ acc[2*j]+=pp*__uint_as_float(vv[j]<<16); acc[2*j+1]+=pp*__uint_as_float(vv[j]&0xffff0000u);}
  }
  for(int i=0;i<8;i++){ acc[i]+=__shfl_xor(acc[i],32); }
  if(r==0) for(int i=0;i<8;i++) red[w][c*8+i]=acc[i];
  __syncthreads();
  int slot=(b*H+h)*NS+s;
  po[(size_t)slot*256+t]=red[0][t]+red[1][t]+red[2][t]+red[3][t];
  if(t==0){ pml[2*slot]=m; pml[2*slot+1]=ls; }
}
__global__ void k_comb(const float* po, const float* pml, float* o){
  int h=blockIdx.x,b=blockIdx.y,t=threadIdx.x; int s0=(b*H+h)*NS; float M=-INFINITY;
  for(int s=0;s<NS;s++) M=fmaxf(M,pml[2*(s0+s)]);
  float L=0,a=0; for(int s=0;s<NS;s++){ float w=__expf(pml[2*(s0+s)]-M); L+=w*pml[2*(s0+s)+1]; a+=w*po[(size_t)(s0+s)*256+t]; }
  o[b*4096+h*256+t]=rb(a/L);
}
template<int KK>
__global__ void k_proj(const float* a, const u16* W, float* xf, u16* out){
  __shared__ float red[16][64];
  int b=blockIdx.y, c=blockIdx.x*64+(threadIdx.x&63), g=threadIdx.x>>6; float s=0;
  for(int k=g;k<KK;k+=16) s+=a[b*KK+k]*bf2f(W[(size_t)k*D+c]);
  red[g][threadIdx.x&63]=s; __syncthreads();
  if(g==0){ float t=0; for(int i=0;i<16;i++) t+=red[i][threadIdx.x]; float nx=rb(xf[b*D+c]+rb(t)); xf[b*D+c]=nx; if(out) out[b*D+c]=f2bf(nx);}
}
__global__ void k_gu(const float* xf, const u16* wg, const u16* wu, float* hh){
  __shared__ float z[D];
  int b=blockIdx.y; ln_row(xf+b*D,z);
  int c=blockIdx.x*256+threadIdx.x; float g=0,u=0;
  for(int k=0;k<D;k++){ g+=z[k]*bf2f(wg[(size_t)k*F+c]); u+=z[k]*bf2f(wu[(size_t)k*F+c]); }
  g=rb(g); u=rb(u); g=g>0?g:0.f*g;
  hh[b*F+c]=rb(g*u);
}

extern "C" void kernel_launch(void* const* d_in, const int* in_sizes, int n_in, void* d_out, int out_size, void* d_ws, size_t ws_size, hipStream_t stream){
  const u16* x=(const u16*)d_in[0]; const u16* K=(const u16*)d_in[1]; const u16* V=(const u16*)d_in[2];
  const u16* wq=(const u16*)d_in[3]; const u16* wo=(const u16*)d_in[4];
  const u16* wg=(const u16*)d_in[6]; const u16* wu=(const u16*)d_in[7]; const u16* wd=(const u16*)d_in[8];
  float* ws=(float*)d_ws; float* xf=ws; float* q=ws+2048; float* o=q+B*4096; float* hh=o+B*4096; float* pml=hh+B*F; float* po=pml+2*B*H*NS;
  k_init<<<7,256,0,stream>>>(x,xf);
  for(int l=0;l<2;l++){
    k_q<<<dim3(16,B),256,0,stream>>>(xf,wq,q);
    k_attn<<<dim3(NS,H,B),256,0,stream>>>(q,K,V,po,pml);
    k_comb<<<dim3(H,B),256,0,stream>>>(po,pml,o);
    k_proj<4096><<<dim3(7,B),1024,0,stream>>>(o,wo,xf,nullptr);
    k_gu<<<dim3(7,B),256,0,stream>>>(xf,wg,wu,hh);
    k_proj<F><<<dim3(7,B),1024,0,stream>>>(hh,wd,xf,l==1?(u16*)d_out:nullptr);
  }
}
```

```cpp
#include <hip/hip_runtime.h>
#include <hip/hip_bf16.h>
#include <stdint.h>

typedef unsigned short u16;
typedef unsigned u32x4 __attribute__((ext_vector_type(4)));
typedef unsigned u32x2 __attribute__((ext_vector_type(2)));
typedef float f32x4 __attribute__((ext_vector_type(4)));
typedef float f32x2 __attribute__((ext_vector_type(2)));
typedef __bf16 bf16x8 __attribute__((ext_vector_type(8)));
typedef __bf16 bf16x2 __attribute__((ext_vector_type(2)));
typedef short s16x4 __attribute__((ext_vector_type(4)));
#define LAS __attribute__((address_space(3)))
#define GAS __attribute__((address_space(1)))

constexpr int DM = 448, FF = 1792, NSP = 14;
constexpr int REC = 640, NID = 9;
constexpr int WS_TAG = 256;
constexpr int WS_PART = 4096;
constexpr int WS_Y = WS_PART + NID * 16 * NSP * REC;
constexpr int WS_DN = WS_Y + NID * 8 * 1024;
constexpr int WS_Q = WS_DN + NID * 28 * 1024;
constexpr int WS_END = WS_Q + NID * 16384;
constexpr int WS_DBG = 4 << 20;
constexpr int LDS_BYTES = 160 * 1024;
constexpr unsigned SPIN_CAP = 1u << 20;
#define KERNEL_ASM \
  "s_memrealtime s[30:31]\n" \
  "s_load_dwordx16 s[4:19], %0, 0x0\n" \
  "s_load_dwordx4 s[20:23], %0, 0x40\n" \
  "v_readfirstlane_b32 s26, %2\n" \
  "v_and_b32 v2, 63, %2\n" \
  "s_mov_b32 s2, %1\n" \
  "s_and_b32 s24, s2, 7\n" \
  "s_lshr_b32 s25, s2, 3\n" \
  "s_lshr_b32 s26, s26, 6\n" \
  "s_cmp_lt_u32 s2, 32\n" \
  "s_cbranch_scc1 .LS_START\n" \
  ".LA_START:\n" \
  "s_mov_b32 s60, s26\n" \
  "s_sub_u32 s25, s25, 4\n" \
  "s_cmp_ge_u32 s25, 14\n" \
  "s_cselect_b32 s27, 14, 0\n" \
  "s_cselect_b32 s28, 1, 0\n" \
  "s_sub_u32 s27, s25, s27\n" \
  "s_lshl_b32 s56, s24, 1\n" \
  "s_add_u32 s56, s56, s28\n" \
  "s_lshl_b32 s25, s25, 2\n" \
  "s_add_u32 s25, s25, s26\n" \
  "v_and_b32 v4, 15, v2\n" \
  "v_lshrrev_b32 v5, 4, v2\n" \
  "v_and_b32 v6, 31, v2\n" \
  "s_lshl_b32 s59, s25, 5\n" \
  "v_add_u32 v6, s59, v6\n" \
  "v_lshrrev_b32 v7, 3, v6\n" \
  "v_lshlrev_b32 v7, 13, v7\n" \
  "v_and_b32 v8, 7, v6\n" \
  "v_lshl_add_u32 v7, v8, 7, v7\n" \
  "s_lshl_b32 s59, s24, 10\n" \
  "v_add_u32 v7, s59, v7\n" \
  "s_mul_i32 s59, s24, 458752\n" \
  "v_lshl_add_u32 v8, v6, 7, s59\n" \
  "v_lshrrev_b32 v6, 2, v4\n" \
  "v_and_b32 v9, 3, v2\n" \
  "v_lshlrev_b32 v0, 9, v6\n" \
  "v_lshl_add_u32 v0, v9, 4, v0\n" \
  "v_lshl_add_u32 v0, v5, 6, v0\n" \
  "s_and_b32 s59, s26, 1\n" \
  "s_mul_i32 s59, s59, 14\n" \
  "s_add_u32 s59, s59, s27\n" \
  "s_add_u32 s58, s59, 1008\n" \
  "s_lshl_b32 s58, s58, 11\n" \
  "s_cmp_lt_u32 s59, 16\n" \
  "s_cselect_b32 s59, 0x200000, 0\n" \
  "s_mul_i32 s67, s26, 36864\n" \
  "s_add_u32 s57, s67, 32768\n" \
  "s_cmp_lt_u32 s26, 2\n" \
  "s_cselect_b32 s61, s59, 0\n" \
  "s_cselect_b32 s62, 0, s59\n" \
  "s_cselect_b32 s65, s57, 147456\n" \
  "s_cselect_b32 s66, 147456, s57\n" \
  "s_mul_i32 s0, s27, 147456\n" \
  "s_mul_i32 s59, s26, 36864\n" \
  "s_add_u32 s0, s0, s59\n" \
  "s_mul_i32 s34, s27, 5\n" \
  "s_mul_i32 s35, s56, 7\n" \
  "s_add_u32 s34, s34, s35\n" \
  "s_mul_i32 s35, s26, 3\n" \
  "s_add_u32 s34, s34, s35\n" \
  "s_mul_i32 s35, s34, 3641\n" \
  "s_lshr_b32 s35, s35, 16\n" \
  "s_mul_i32 s35, s35, 18\n" \
  "s_sub_u32 s34, s34, s35\n" \
  "s_lshl_b32 s34, s34, 11\n" \
  "s_mov_b32 s35, s0\n" \
  "s_add_u32 s0, s0, s34\n" \
  "s_lshl_b32 s57, s56, 21\n" \
  "s_mov_b32 s38, 0x200000\n" \
  "s_mov_b32 s39, 0x20000\n" \
  "s_mov_b32 s47, 0x20000\n" \
  "s_waitcnt lgkmcnt(0)\n" \
  "s_load_dwordx2 s[90:91], s[22:23], 0x0\n" \
  "s_mov_b32 exec_hi, 0\n" \
  "global_load_dword v63, v7, s[10:11]\n" \
  "global_load_dword v63, v8, s[12:13]\n" \
  "s_mov_b64 exec, -1\n" \
  "s_add_u32 s36, s6, s57\n" \
  "s_addc_u32 s37, s7, 0\n" \
  "s_mov_b64 s[44:45], s[36:37]\n" \
  "s_mov_b32 s46, s61\n" \
  "s_add_u32 m0, s65, 0\n" \
  "s_nop 0\n" \
  "buffer_load_dwordx4 v0, s[44:47], s58 offen nt lds\n" \
  "s_add_u32 m0, s65, 768\n" \
  "s_nop 0\n" \
  "buffer_load_dwordx4 v0, s[44:47], s58 offen offset:256 nt lds\n" \
  "buffer_load_dwordx4 a[0:3], v0, s[36:39], s0 offen nt\n" \
  "buffer_load_dwordx4 a[4:7], v0, s[36:39], s0 offen offset:256 nt\n" \
  "s_mov_b64 s[52:53], s[22:23]\n" \
  "s_mov_b32 s54, 1773568\n" \
  "s_mov_b32 s55, 0x20000\n" \
  "s_mul_i32 s77, s56, 14\n" \
  "s_add_u32 s77, s77, s27\n" \
  "s_mul_i32 s77, s77, 640\n" \
  "s_add_u32 s77, s77, 4096\n" \
  "v_mov_b32 v44, -1\n" \
  "v_mov_b32 v45, -1\n" \
  "v_mov_b32 v46, -1\n" \
  "v_mov_b32 v47, -1\n" \
  "v_lshlrev_b32 v48, 4, v2\n" \
  "s_mul_i32 s59, s26, 286720\n" \
  "s_add_u32 s59, s59, s77\n" \
  "s_mov_b32 exec_hi, 255\n" \
  "buffer_store_dwordx4 v[44:47], v48, s[52:55], s59 offen sc1\n" \
  "s_add_u32 s59, s59, 143360\n" \
  "buffer_store_dwordx4 v[44:47], v48, s[52:55], s59 offen sc1\n" \
  "s_mov_b64 exec, -1\n" \
  "s_sub_u32 s57, s57, 0x2000000\n" \
  "s_cselect_b32 s59, -1, 0\n" \
  "s_add_u32 s40, s8, s57\n" \
  "s_addc_u32 s41, s9, s59\n" \
  "s_mov_b32 s42, 0x200000\n" \
  "s_mov_b32 s43, 0x20000\n" \
  "s_mov_b64 s[48:49], s[22:23]\n" \
  "s_mov_b32 s50, 0\n" \
  "s_mov_b32 s51, 0x20000\n" \
  "s_lshl_b32 s82, s2, 3\n" \
  "s_mov_b64 s[20:21], s[14:15]\n" \
  "s_mov_b64 s[22:23], s[16:17]\n" \
  "s_lshl_b32 s59, s25, 2\n" \
  "v_add_u32 v6, s59, v6\n" \
  "s_lshl_b32 s59, s24, 2\n" \
  "v_add_u32 v9, s59, v9\n" \
  "v_cmp_gt_u32 s[92:93], 28, v9\n" \
  "s_nop 0\n" \
  "s_and_b64 s[92:93], s[92:93], 0xffff\n" \
  "v_mul_u32_u24 v60, 3584, v6\n" \
  "v_lshl_add_u32 v60, v9, 7, v60\n" \
  "v_mul_u32_u24 v61, 57344, v9\n" \
  "v_lshl_add_u32 v61, v6, 7, v61\n" \
  "s_mov_b32 s57, s0\n" \
  "s_add_u32 s34, s34, 2048\n" \
  "s_sub_u32 s59, s34, 36864\n" \
  "s_min_u32 s34, s34, s59\n" \
  "s_add_u32 s1, s35, s34\n" \
  "s_add_u32 s34, s34, 2048\n" \
  "s_sub_u32 s59, s34, 36864\n" \
  "s_min_u32 s34, s34, s59\n" \
  "s_add_u32 s2, s35, s34\n" \
  "s_add_u32 s34, s34, 2048\n" \
  "s_sub_u32 s59, s34, 36864\n" \
  "s_min_u32 s34, s34, s59\n" \
  "s_add_u32 s3, s35, s34\n" \
  "s_add_u32 s34, s34, 2048\n" \
  "s_sub_u32 s59, s34, 36864\n" \
  "s_min_u32 s34, s34, s59\n" \
  "s_add_u32 s4, s35, s34\n" \
  "s_add_u32 s34, s34, 2048\n" \
  "s_sub_u32 s59, s34, 36864\n" \
  "s_min_u32 s34, s34, s59\n" \
  "s_add_u32 s5, s35, s34\n" \
  "s_add_u32 s34, s34, 2048\n" \
  "s_sub_u32 s59, s34, 36864\n" \
  "s_min_u32 s34, s34, s59\n" \
  "s_add_u32 s6, s35, s34\n" \
  "s_add_u32 s34, s34, 2048\n" \
  "s_sub_u32 s59, s34, 36864\n" \
  "s_min_u32 s34, s34, s59\n" \
  "s_add_u32 s7, s35, s34\n" \
  "s_add_u32 s34, s34, 2048\n" \
  "s_sub_u32 s59, s34, 36864\n" \
  "s_min_u32 s34, s34, s59\n" \
  "s_add_u32 s8, s35, s34\n" \
  "s_add_u32 s34, s34, 2048\n" \
  "s_sub_u32 s59, s34, 36864\n" \
  "s_min_u32 s34, s34, s59\n" \
  "s_add_u32 s9, s35, s34\n" \
  "s_add_u32 s34, s34, 2048\n" \
  "s_sub_u32 s59, s34, 36864\n" \
  "s_min_u32 s34, s34, s59\n" \
  "s_add_u32 s10, s35, s34\n" \
  "s_add_u32 s34, s34, 2048\n" \
  "s_sub_u32 s59, s34, 36864\n" \
  "s_min_u32 s34, s34, s59\n" \
  "s_add_u32 s11, s35, s34\n" \
  "s_add_u32 s34, s34, 2048\n" \
  "s_sub_u32 s59, s34, 36864\n" \
  "s_min_u32 s34, s34, s59\n" \
  "s_add_u32 s12, s35, s34\n" \
  "s_add_u32 s34, s34, 2048\n" \
  "s_sub_u32 s59, s34, 36864\n" \
  "s_min_u32 s34, s34, s59\n" \
  "s_add_u32 s13, s35, s34\n" \
  "s_add_u32 s34, s34, 2048\n" \
  "s_sub_u32 s59, s34, 36864\n" \
  "s_min_u32 s34, s34, s59\n" \
  "s_add_u32 s14, s35, s34\n" \
  "s_add_u32 s34, s34, 2048\n" \
  "s_sub_u32 s59, s34, 36864\n" \
  "s_min_u32 s34, s34, s59\n" \
  "s_add_u32 s15, s35, s34\n" \
  "s_add_u32 s34, s34, 2048\n" \
  "s_sub_u32 s59, s34, 36864\n" \
  "s_min_u32 s34, s34, s59\n" \
  "s_add_u32 s16, s35, s34\n" \
  "s_add_u32 s34, s34, 2048\n" \
  "s_sub_u32 s59, s34, 36864\n" \
  "s_min_u32 s34, s34, s59\n" \
  "s_add_u32 s17, s35, s34\n" \
  "s_sub_u32 s78, s65, s67\n" \
  "s_sub_u32 s80, s66, s67\n" \
  "v_lshlrev_b32 v1, 9, v5\n" \
  "v_lshl_add_u32 v1, v4, 4, v1\n" \
  "v_lshrrev_b32 v3, 1, v5\n" \
  "v_lshlrev_b32 v3, 9, v3\n" \
  "v_lshl_add_u32 v3, v4, 5, v3\n" \
  "v_and_b32 v6, 1, v5\n" \
  "v_lshl_add_u32 v3, v6, 4, v3\n" \
  "s_lshl_b32 s59, s26, 10\n" \
  "v_add_u32 v3, s59, v3\n" \
  "s_lshl_b32 s87, s26, 4\n" \
  "v_and_b32 v42, 1, v2\n" \
  "v_lshlrev_b32 v42, 4, v42\n" \
  "v_mov_b32 v43, 0xffff\n" \
  "v_lshlrev_b32 v43, v42, v43\n" \
  "v_bfe_u32 v42, v2, 1, 2\n" \
  "v_cmp_eq_u32 vcc, 0, v42\n" \
  "s_nop 1\n" \
  "v_cndmask_b32 v4, 0, v43, vcc\n" \
  "v_cmp_eq_u32 vcc, 1, v42\n" \
  "s_nop 1\n" \
  "v_cndmask_b32 v5, 0, v43, vcc\n" \
  "v_cmp_eq_u32 vcc, 2, v42\n" \
  "s_nop 1\n" \
  "v_cndmask_b32 v6, 0, v43, vcc\n" \
  "v_cmp_eq_u32 vcc, 3, v42\n" \
  "s_nop 1\n" \
  "v_cndmask_b32 v7, 0, v43, vcc\n" \
  "v_lshlrev_b32 v2, 4, v2\n" \
  "v_add_u32 v2, s67, v2\n" \
  "s_cmp_eq_u32 s61, 0\n" \
  "s_cselect_b32 s63, 0xff800000, 0\n" \
  "s_cmp_eq_u32 s62, 0\n" \
  "s_cselect_b32 s64, 0xff800000, 0\n" \
  "s_mov_b32 s88, 0\n" \
  "s_mov_b32 s89, -1\n" \
  "s_mov_b32 s72, 0\n" \
  "s_mov_b32 s74, 0x10000\n" \
  "s_add_u32 s79, s77, 573440\n" \
  "s_lshl_b32 s75, s56, 10\n" \
  "s_add_u32 s75, s75, 1626112\n" \
  "s_add_u32 s76, s75, 65536\n" \
  "v_mov_b32 v42, 155712\n" \
  "v_mov_b32 v44, 0\n" \
  "v_mov_b32 v45, 0\n" \
  "ds_write_b64 v42, v[44:45]\n" \
  "ds_write_b64 v42, v[44:45] offset:4224\n" \
  "s_lshl_b32 s59, s28, 4\n" \
  "s_add_u32 s81, s24, s59\n" \
  "s_lshl_b32 s81, s81, 3\n" \
  "s_min_u32 s83, s24, 6\n" \
  "s_lshl_b32 s83, s83, 12\n" \
  "s_add_u32 s83, s83, 1454080\n" \
  "s_lshl_b32 s33, s30, 6\n" \
  "s_add_u32 s33, s33, 7040\n" \
  "s_mov_b32 s57, s30\n" \
  "s_mov_b32 s27, 1660\n" \
  "s_call_b64 s[34:35], .LA_GATE\n" \
  "buffer_load_dwordx4 a[8:11], v0, s[36:39], s1 offen nt\n" \
  "buffer_load_dwordx4 a[12:15], v0, s[36:39], s1 offen offset:256 nt\n" \
  "s_call_b64 s[34:35], .LA_GATE\n" \
  "buffer_load_dwordx4 a[16:19], v0, s[36:39], s2 offen nt\n" \
  "buffer_load_dwordx4 a[20:23], v0, s[36:39], s2 offen offset:256 nt\n" \
  "s_call_b64 s[34:35], .LA_GATE\n" \
  "buffer_load_dwordx4 a[24:27], v0, s[36:39], s3 offen nt\n" \
  "buffer_load_dwordx4 a[28:31], v0, s[36:39], s3 offen offset:256 nt\n" \
  "s_call_b64 s[34:35], .LA_GATE\n" \
  "buffer_load_dwordx4 a[32:35], v0, s[36:39], s4 offen nt\n" \
  "buffer_load_dwordx4 a[36:39], v0, s[36:39], s4 offen offset:256 nt\n" \
  "s_waitcnt vmcnt(11)\n" \
  "s_call_b64 s[34:35], .LA_GATE\n" \
  "buffer_load_dwordx4 a[40:43], v0, s[36:39], s5 offen nt\n" \
  "buffer_store_dword v42, off, s[48:51], 0\n" \
  "buffer_store_dword v42, off, s[48:51], 0\n" \
  "buffer_store_dword v42, off, s[48:51], 0\n" \
  "buffer_store_dword v42, off, s[48:51], 0\n" \
  ".LA_LOOP:\n" \
  "s_cmp_eq_u32 s72, 0\n" \
  "s_mov_b32 s27, 1660\n" \
  "s_cselect_b32 s27, 1660, s27\n" \
  "s_waitcnt vmcnt(15)\n" \
  "buffer_load_dwordx4 a[44:47], v0, s[36:39], s5 offen offset:256 nt\n" \
  "s_waitcnt vmcnt(15)\n" \
  "s_call_b64 s[34:35], .LA_GATE\n" \
  "buffer_load_dwordx4 a[48:51], v0, s[36:39], s6 offen nt\n" \
  "s_waitcnt vmcnt(15)\n" \
  "buffer_load_dwordx4 a[52:55], v0, s[36:39], s6 offen offset:256 nt\n" \
  "s_waitcnt vmcnt(15)\n" \
  "s_call_b64 s[34:35], .LA_GATE\n" \
  "buffer_load_dwordx4 a[56:59], v0, s[36:39], s7 offen nt\n" \
  "s_cmp_lg_u32 s72, 0\n" \
  "s_cbranch_scc1 .LA_NOTAG\n" \
  "s_waitcnt lgkmcnt(0)\n" \
  "s_barrier\n" \
  "s_add_u32 s90, s90, 1\n" \
  "s_addc_u32 s91, s91, 0\n" \
  "v_mov_b32 v44, s90\n" \
  "v_mov_b32 v45, s91\n" \
  "s_mov_b64 exec, 1\n" \
  "buffer_store_dwordx2 v[44:45], off, s[52:55], s82 offset:256 sc1\n" \
  "s_mov_b64 exec, -1\n" \
  ".LA_NOTAG:\n" \
  "s_waitcnt vmcnt(15)\n" \
  "buffer_load_dwordx4 a[60:63], v0, s[36:39], s7 offen offset:256 nt\n" \
  "s_cmp_lg_u32 s72, 0\n" \
  "s_cbranch_scc1 .LA_NOT2\n" \
  "s_cmp_eq_u64 s[92:93], 0\n" \
  "s_cbranch_scc1 .LA_NOT2B\n" \
  "s_mov_b64 exec, s[92:93]\n" \
  "global_load_dword v63, v60, s[20:21]\n" \
  "global_load_dword v63, v60, s[22:23]\n" \
  "global_load_dword v63, v61, s[18:19]\n" \
  "s_mov_b64 exec, -1\n" \
  ".LA_NOT2B:\n" \
  "s_add_u32 s33, s33, 7680\n" \
  ".LA_NOT2:\n" \
  "s_waitcnt vmcnt(15)\n" \
  "s_call_b64 s[34:35], .LA_GATE\n" \
  "buffer_load_dwordx4 a[64:67], v0, s[36:39], s8 offen nt\n" \
  "s_waitcnt vmcnt(13)\n" \
  "buffer_load_dwordx4 a[68:71], v0, s[36:39], s8 offen offset:256 nt\n" \
  "s_waitcnt vmcnt(13)\n" \
  "s_call_b64 s[34:35], .LA_GATE\n" \
  "buffer_load_dwordx4 a[72:75], v0, s[36:39], s9 offen nt\n" \
  "s_waitcnt vmcnt(13)\n" \
  "buffer_load_dwordx4 a[76:79], v0, s[36:39], s9 offen offset:256 nt\n" \
  "s_waitcnt vmcnt(13)\n" \
  "s_call_b64 s[34:35], .LA_GATE\n" \
  "buffer_load_dwordx4 a[80:83], v0, s[36:39], s10 offen nt\n" \
  "s_cmp_lg_u32 s72, 0\n" \
  "s_cbranch_scc1 .LA_NOTG1\n" \
  "buffer_load_dwordx2 v[44:45], off, s[52:55], s81 offset:256 sc1\n" \
  "buffer_load_dwordx2 v[46:47], off, s[52:55], s81 offset:320 sc1\n" \
  "v_and_b32 v42, 0x70, v1\n" \
  "buffer_load_dwordx4 v[48:51], v42, s[52:55], 0 offen offset:128 sc1\n" \
  "s_nop 0\n" \
  ".LA_NOTG1:\n" \
  "s_waitcnt vmcnt(13)\n" \
  "buffer_load_dwordx4 a[84:87], v0, s[36:39], s10 offen offset:256 nt\n" \
  "s_waitcnt vmcnt(13)\n" \
  "s_call_b64 s[34:35], .LA_GATE\n" \
  "buffer_load_dwordx4 a[88:91], v0, s[36:39], s11 offen nt\n" \
  "s_waitcnt vmcnt(11)\n" \
  "buffer_load_dwordx4 a[92:95], v0, s[36:39], s11 offen offset:256 nt\n" \
  "s_waitcnt vmcnt(11)\n" \
  "s_call_b64 s[34:35], .LA_GATE\n" \
  "buffer_load_dwordx4 a[96:99], v0, s[36:39], s12 offen nt\n" \
  "s_waitcnt vmcnt(11)\n" \
  "buffer_load_dwordx4 a[100:103], v0, s[36:39], s12 offen offset:256 nt\n" \
  "s_waitcnt vmcnt(11)\n" \
  "s_call_b64 s[34:35], .LA_GATE\n" \
  "buffer_load_dwordx4 a[104:107], v0, s[36:39], s13 offen nt\n" \
  "s_waitcnt vmcnt(11)\n" \
  "buffer_load_dwordx4 a[108:111], v0, s[36:39], s13 offen offset:256 nt\n" \
  "s_waitcnt vmcnt(11)\n" \
  "s_call_b64 s[34:35], .LA_GATE\n" \
  "buffer_load_dwordx4 a[112:115], v0, s[36:39], s14 offen nt\n" \
  "s_waitcnt vmcnt(11)\n" \
  "buffer_load_dwordx4 a[116:119], v0, s[36:39], s14 offen offset:256 nt\n" \
  "s_waitcnt vmcnt(11)\n" \
  "s_call_b64 s[34:35], .LA_GATE\n" \
  "buffer_load_dwordx4 a[120:123], v0, s[36:39], s15 offen nt\n" \
  "s_waitcnt vmcnt(11)\n" \
  "buffer_load_dwordx4 a[124:127], v0, s[36:39], s15 offen offset:256 nt\n" \
  "s_waitcnt vmcnt(11)\n" \
  "s_call_b64 s[34:35], .LA_GATE\n" \
  "buffer_load_dwordx4 a[128:131], v0, s[36:39], s16 offen nt\n" \
  "s_waitcnt vmcnt(11)\n" \
  "buffer_load_dwordx4 a[132:135], v0, s[36:39], s16 offen offset:256 nt\n" \
  "s_cmp_lg_u32 s72, 0\n" \
  "s_cbranch_scc1 .LA_NOTG2\n" \
  "v_cmp_eq_u64 vcc, s[90:91], v[48:49]\n" \
  "v_subrev_u32 v50, s57, v50\n" \
  "s_nop 0\n" \
  "v_cndmask_b32 v50, 0, v50, vcc\n" \
  "s_nop 1\n" \
  "v_min_i32_dpp v50, v50, v50 quad_perm:[1,0,3,2] row_mask:0xf bank_mask:0xf\n" \
  "s_nop 1\n" \
  "v_min_i32_dpp v50, v50, v50 quad_perm:[2,3,0,1] row_mask:0xf bank_mask:0xf\n" \
  "s_nop 1\n" \
  "v_min_i32_dpp v50, v50, v50 row_ror:4 row_mask:0xf bank_mask:0xf\n" \
  "s_nop 0\n" \
  "v_readfirstlane_b32 s59, v50\n" \
  "s_nop 0\n" \
  "s_add_u32 s59, s59, 15\n" \
  "s_min_i32 s59, s59, 0\n" \
  "s_max_i32 s59, s59, -150\n" \
  "s_lshl_b32 s59, s59, 6\n" \
  "s_add_u32 s33, s33, s59\n" \
  ".LA_TG:\n" \
  "v_cmp_ne_u64 vcc, s[90:91], v[44:45]\n" \
  "v_cmp_ne_u64 s[92:93], s[90:91], v[46:47]\n" \
  "s_nop 0\n" \
  "s_or_b64 vcc, vcc, s[92:93]\n" \
  "s_nop 1\n" \
  "s_cbranch_vccz .LA_TGOK\n" \
  "s_sub_u32 s74, s74, 1\n" \
  "s_cbranch_scc1 .LA_TGX\n" \
  "buffer_load_dwordx2 v[44:45], off, s[52:55], s81 offset:256 sc1\n" \
  "buffer_load_dwordx2 v[46:47], off, s[52:55], s81 offset:320 sc1\n" \
  "s_waitcnt vmcnt(0)\n" \
  "s_branch .LA_TG\n" \
  ".LA_TGX:\n" \
  "s_mov_b32 s74, 0\n" \
  ".LA_TGOK:\n" \
  ".LA_NOTG2:\n" \
  "s_waitcnt vmcnt(11)\n" \
  "s_call_b64 s[34:35], .LA_GATE\n" \
  "buffer_load_dwordx4 a[136:139], v0, s[36:39], s17 offen nt\n" \
  "s_waitcnt vmcnt(11)\n" \
  "buffer_load_dwordx4 a[140:143], v0, s[36:39], s17 offen offset:256 nt\n" \
  "s_waitcnt vmcnt(11)\n" \
  "s_add_u32 s40, s40, 0x2000000\n" \
  "s_addc_u32 s41, s41, 0\n" \
  "s_mov_b64 s[44:45], s[40:41]\n" \
  "s_mov_b32 s46, s61\n" \
  "s_add_u32 m0, s65, 2048\n" \
  "s_nop 0\n" \
  "buffer_load_dwordx4 v1, s[44:47], s58 offen nt lds\n" \
  "s_waitcnt vmcnt(11)\n" \
  "s_add_u32 m0, s65, 2816\n" \
  "s_nop 0\n" \
  "buffer_load_dwordx4 v1, s[44:47], s58 offen offset:256 nt lds\n" \
  "s_waitcnt vmcnt(11)\n" \
  "s_call_b64 s[34:35], .LA_GATE\n" \
  "s_add_u32 m0, s67, 0\n" \
  "s_nop 0\n" \
  "buffer_load_dwordx4 v1, s[40:43], s0 offen nt lds\n" \
  "s_waitcnt vmcnt(11)\n" \
  "s_add_u32 m0, s67, 768\n" \
  "s_nop 0\n" \
  "buffer_load_dwordx4 v1, s[40:43], s0 offen offset:256 nt lds\n" \
  "v_and_b32 v42, 0xc0, v0\n" \
  "v_and_b32 v43, 0xf0, v0\n" \
  "v_add_u32 v42, v42, v43\n" \
  "buffer_load_dwordx4 v[8:11], v42, s[52:55], s75 offen sc1\n" \
  "buffer_load_dwordx4 v[12:15], v42, s[52:55], s75 offen offset:512 sc1\n" \
  "s_waitcnt vmcnt(13)\n" \
  "s_call_b64 s[34:35], .LA_GATE\n" \
  "s_add_u32 m0, s67, 2048\n" \
  "s_nop 0\n" \
  "buffer_load_dwordx4 v1, s[40:43], s1 offen nt lds\n" \
  "s_waitcnt vmcnt(13)\n" \
  "s_add_u32 m0, s67, 2816\n" \
  "s_nop 0\n" \
  "buffer_load_dwordx4 v1, s[40:43], s1 offen offset:256 nt lds\n" \
  "s_waitcnt vmcnt(13)\n" \
  "s_call_b64 s[34:35], .LA_GATE\n" \
  "s_add_u32 m0, s67, 4096\n" \
  "s_nop 0\n" \
  "buffer_load_dwordx4 v1, s[40:43], s2 offen nt lds\n" \
  "s_waitcnt vmcnt(13)\n" \
  "s_add_u32 m0, s67, 4864\n" \
  "s_nop 0\n" \
  "buffer_load_dwordx4 v1, s[40:43], s2 offen offset:256 nt lds\n" \
  "s_waitcnt vmcnt(13)\n" \
  "s_call_b64 s[34:35], .LA_GATE\n" \
  "s_add_u32 m0, s67, 6144\n" \
  "s_nop 0\n" \
  "buffer_load_dwordx4 v1, s[40:43], s3 offen nt lds\n" \
  "s_waitcnt vmcnt(13)\n" \
  "s_add_u32 m0, s67, 6912\n" \
  "s_nop 0\n" \
  "buffer_load_dwordx4 v1, s[40:43], s3 offen offset:256 nt lds\n" \
  "s_waitcnt vmcnt(13)\n" \
  "s_call_b64 s[34:35], .LA_GATE\n" \
  "s_add_u32 m0, s67, 8192\n" \
  "s_nop 0\n" \
  "buffer_load_dwordx4 v1, s[40:43], s4 offen nt lds\n" \
  "s_waitcnt vmcnt(13)\n" \
  "s_add_u32 m0, s67, 8960\n" \
  "s_nop 0\n" \
  "buffer_load_dwordx4 v1, s[40:43], s4 offen offset:256 nt lds\n" \
  "s_waitcnt vmcnt(8)\n" \
  "s_mov_b32 s86, s75\n" \
  "s_call_b64 s[94:95], .LA_QWAIT\n" \
  "s_add_u32 s75, s75, 16384\n" \
  "v_mfma_f32_16x16x32_bf16 v[44:47], a[0:3], v[8:11], 0\n" \
  "v_add_u32 v43, s78, v2\n" \
  "v_mov_b32 v36, 0xff800000\n" \
  "v_mfma_f32_16x16x32_bf16 v[44:47], a[4:7], v[12:15], v[44:47]\n" \
  "ds_read_b128 v[56:59], v43\n" \
  "ds_read_b128 v[60:63], v43 offset:1024\n" \
  "v_mov_b32 v42, s63\n" \
  "v_mfma_f32_16x16x32_bf16 v[48:51], a[8:11], v[8:11], 0\n" \
  "v_mfma_f32_16x16x32_bf16 v[48:51], a[12:15], v[12:15], v[48:51]\n" \
  "v_mov_b32_dpp v36, v42 quad_perm:[0,1,2,3] row_mask:0xf bank_mask:0x4\n" \
  "v_mfma_f32_16x16x32_bf16 v[52:55], a[16:19], v[8:11], 0\n" \
  "s_nop 0\n" \
  "v_mov_b32_dpp v32, v44 quad_perm:[0,0,0,0] row_mask:0xf bank_mask:0x1\n" \
  "v_mov_b32_dpp v42, v45 quad_perm:[1,1,1,1] row_mask:0xf bank_mask:0x1\n" \
  "v_mfma_f32_16x16x32_bf16 v[52:55], a[20:23], v[12:15], v[52:55]\n" \
  "v_add_f32_dpp v32, v46, v32 quad_perm:[2,2,2,2] row_mask:0xf bank_mask:0x1\n" \
  "v_add_f32_dpp v42, v47, v42 quad_perm:[3,3,3,3] row_mask:0xf bank_mask:0x1\n" \
  "v_mfma_f32_16x16x32_bf16 v[44:47], a[24:27], v[8:11], 0\n" \
  "s_waitcnt vmcnt(13)\n" \
  "s_add_u32 s33, s33, s27\n" \
  "s_add_u32 m0, s67, 10240\n" \
  "s_nop 0\n" \
  "buffer_load_dwordx4 v1, s[40:43], s5 offen nt lds\n" \
  "v_mov_b32_dpp v32, v48 quad_perm:[0,0,0,0] row_mask:0xf bank_mask:0x2\n" \
  "v_mov_b32_dpp v42, v49 quad_perm:[1,1,1,1] row_mask:0xf bank_mask:0x2\n" \
  "v_mfma_f32_16x16x32_bf16 v[44:47], a[28:31], v[12:15], v[44:47]\n" \
  "v_add_f32_dpp v32, v50, v32 quad_perm:[2,2,2,2] row_mask:0xf bank_mask:0x2\n" \
  "v_add_f32_dpp v42, v51, v42 quad_perm:[3,3,3,3] row_mask:0xf bank_mask:0x2\n" \
  "v_mfma_f32_16x16x32_bf16 v[48:51], a[32:35], v[8:11], 0\n" \
  "v_mov_b32_dpp v32, v52 quad_perm:[0,0,0,0] row_mask:0xf bank_mask:0x4\n" \
  "v_mov_b32_dpp v42, v53 quad_perm:[1,1,1,1] row_mask:0xf bank_mask:0x4\n" \
  "v_mfma_f32_16x16x32_bf16 v[48:51], a[36:39], v[12:15], v[48:51]\n" \
  "v_add_f32_dpp v32, v54, v32 quad_perm:[2,2,2,2] row_mask:0xf bank_mask:0x4\n" \
  "v_add_f32_dpp v42, v55, v42 quad_perm:[3,3,3,3] row_mask:0xf bank_mask:0x4\n" \
  "v_mfma_f32_16x16x32_bf16 v[52:55], a[40:43], v[8:11], 0\n" \
  "v_mov_b32_dpp v32, v44 quad_perm:[0,0,0,0] row_mask:0xf bank_mask:0x8\n" \
  "v_mov_b32_dpp v42, v45 quad_perm:[1,1,1,1] row_mask:0xf bank_mask:0x8\n" \
  "v_mfma_f32_16x16x32_bf16 v[52:55], a[44:47], v[12:15], v[52:55]\n" \
  "v_add_f32_dpp v32, v46, v32 quad_perm:[2,2,2,2] row_mask:0xf bank_mask:0x8\n" \
  "v_add_f32_dpp v42, v47, v42 quad_perm:[3,3,3,3] row_mask:0xf bank_mask:0x8\n" \
  "v_mfma_f32_16x16x32_bf16 v[44:47], a[48:51], v[8:11], 0\n" \
  "v_mov_b32_dpp v33, v48 quad_perm:[0,0,0,0] row_mask:0xf bank_mask:0x1\n" \
  "v_mov_b32_dpp v43, v49 quad_perm:[1,1,1,1] row_mask:0xf bank_mask:0x1\n" \
  "v_mfma_f32_16x16x32_bf16 v[44:47], a[52:55], v[12:15], v[44:47]\n" \
  "v_add_f32_dpp v33, v50, v33 quad_perm:[2,2,2,2] row_mask:0xf bank_mask:0x1\n" \
  "v_add_f32_dpp v43, v51, v43 quad_perm:[3,3,3,3] row_mask:0xf bank_mask:0x1\n" \
  "v_add_f32 v32, v32, v42\n" \
  "v_mfma_f32_16x16x32_bf16 v[48:51], a[56:59], v[8:11], 0\n" \
  "v_mov_b32_dpp v33, v52 quad_perm:[0,0,0,0] row_mask:0xf bank_mask:0x2\n" \
  "v_mov_b32_dpp v43, v53 quad_perm:[1,1,1,1] row_mask:0xf bank_mask:0x2\n" \
  "v_mfma_f32_16x16x32_bf16 v[48:51], a[60:63], v[12:15], v[48:51]\n" \
  "v_add_f32_dpp v33, v54, v33 quad_perm:[2,2,2,2] row_mask:0xf bank_mask:0x2\n" \
  "v_add_f32_dpp v43, v55, v43 quad_perm:[3,3,3,3] row_mask:0xf bank_mask:0x2\n" \
  "v_mfma_f32_16x16x32_bf16 v[52:55], a[64:67], v[8:11], 0\n" \
  "v_mov_b32_dpp v33, v44 quad_perm:[0,0,0,0] row_mask:0xf bank_mask:0x4\n" \
  "v_mov_b32_dpp v43, v45 quad_perm:[1,1,1,1] row_mask:0xf bank_mask:0x4\n" \
  "v_mfma_f32_16x16x32_bf16 v[52:55], a[68:71], v[12:15], v[52:55]\n" \
  "s_waitcnt vmcnt(13)\n" \
  "s_add_u32 m0, s67, 11008\n" \
  "s_nop 0\n" \
  "buffer_load_dwordx4 v1, s[40:43], s5 offen offset:256 nt lds\n" \
  "v_add_f32_dpp v33, v46, v33 quad_perm:[2,2,2,2] row_mask:0xf bank_mask:0x4\n" \
  "v_add_f32_dpp v43, v47, v43 quad_perm:[3,3,3,3] row_mask:0xf bank_mask:0x4\n" \
  "v_mfma_f32_16x16x32_bf16 v[44:47], a[72:75], v[8:11], 0\n" \
  "v_mov_b32_dpp v33, v48 quad_perm:[0,0,0,0] row_mask:0xf bank_mask:0x8\n" \
  "v_mov_b32_dpp v43, v49 quad_perm:[1,1,1,1] row_mask:0xf bank_mask:0x8\n" \
  "v_mfma_f32_16x16x32_bf16 v[44:47], a[76:79], v[12:15], v[44:47]\n" \
  "v_add_f32_dpp v33, v50, v33 quad_perm:[2,2,2,2] row_mask:0xf bank_mask:0x8\n" \
  "v_add_f32_dpp v43, v51, v43 quad_perm:[3,3,3,3] row_mask:0xf bank_mask:0x8\n" \
  "v_mfma_f32_16x16x32_bf16 v[48:51], a[80:83], v[8:11], 0\n" \
  "v_mov_b32_dpp v34, v52 quad_perm:[0,0,0,0] row_mask:0xf bank_mask:0x1\n" \
  "v_mov_b32_dpp v42, v53 quad_perm:[1,1,1,1] row_mask:0xf bank_mask:0x1\n" \
  "v_mfma_f32_16x16x32_bf16 v[48:51], a[84:87], v[12:15], v[48:51]\n" \
  "v_add_f32_dpp v34, v54, v34 quad_perm:[2,2,2,2] row_mask:0xf bank_mask:0x1\n" \
  "v_add_f32_dpp v42, v55, v42 quad_perm:[3,3,3,3] row_mask:0xf bank_mask:0x1\n" \
  "v_add_f32 v33, v33, v43\n" \
  "v_mfma_f32_16x16x32_bf16 v[52:55], a[88:91], v[8:11], 0\n" \
  "v_mov_b32_dpp v34, v44 quad_perm:[0,0,0,0] row_mask:0xf bank_mask:0x2\n" \
  "v_mov_b32_dpp v42, v45 quad_perm:[1,1,1,1] row_mask:0xf bank_mask:0x2\n" \
  "v_mfma_f32_16x16x32_bf16 v[52:55], a[92:95], v[12:15], v[52:55]\n" \
  "v_add_f32_dpp v34, v46, v34 quad_perm:[2,2,2,2] row_mask:0xf bank_mask:0x2\n" \
  "v_add_f32_dpp v42, v47, v42 quad_perm:[3,3,3,3] row_mask:0xf bank_mask:0x2\n" \
  "v_mfma_f32_16x16x32_bf16 v[44:47], a[96:99], v[8:11], 0\n" \
  "v_mov_b32_dpp v34, v48 quad_perm:[0,0,0,0] row_mask:0xf bank_mask:0x4\n" \
  "v_mov_b32_dpp v42, v49 quad_perm:[1,1,1,1] row_mask:0xf bank_mask:0x4\n" \
  "v_mfma_f32_16x16x32_bf16 v[44:47], a[100:103], v[12:15], v[44:47]\n" \
  "v_add_f32_dpp v34, v50, v34 quad_perm:[2,2,2,2] row_mask:0xf bank_mask:0x4\n" \
  "v_add_f32_dpp v42, v51, v42 quad_perm:[3,3,3,3] row_mask:0xf bank_mask:0x4\n" \
  "v_mfma_f32_16x16x32_bf16 v[48:51], a[104:107], v[8:11], 0\n" \
  "v_mov_b32_dpp v34, v52 quad_perm:[0,0,0,0] row_mask:0xf bank_mask:0x8\n" \
  "v_mov_b32_dpp v42, v53 quad_perm:[1,1,1,1] row_mask:0xf bank_mask:0x8\n" \
  "v_mfma_f32_16x16x32_bf16 v[48:51], a[108:111], v[12:15], v[48:51]\n" \
  "v_add_f32_dpp v34, v54, v34 quad_perm:[2,2,2,2] row_mask:0xf bank_mask:0x8\n" \
  "v_add_f32_dpp v42, v55, v42 quad_perm:[3,3,3,3] row_mask:0xf bank_mask:0x8\n" \
  "v_mfma_f32_16x16x32_bf16 v[52:55], a[112:115], v[8:11], 0\n" \
  "s_waitcnt vmcnt(13)\n" \
  "s_add_u32 s33, s33, s27\n" \
  "s_add_u32 m0, s67, 12288\n" \
  "s_nop 0\n" \
  "buffer_load_dwordx4 v1, s[40:43], s6 offen nt lds\n" \
  "v_mov_b32_dpp v35, v44 quad_perm:[0,0,0,0] row_mask:0xf bank_mask:0x1\n" \
  "v_mov_b32_dpp v43, v45 quad_perm:[1,1,1,1] row_mask:0xf bank_mask:0x1\n" \
  "v_mfma_f32_16x16x32_bf16 v[52:55], a[116:119], v[12:15], v[52:55]\n" \
  "v_add_f32_dpp v35, v46, v35 quad_perm:[2,2,2,2] row_mask:0xf bank_mask:0x1\n" \
  "v_add_f32_dpp v43, v47, v43 quad_perm:[3,3,3,3] row_mask:0xf bank_mask:0x1\n" \
  "v_add_f32 v34, v34, v42\n" \
  "v_mfma_f32_16x16x32_bf16 v[44:47], a[120:123], v[8:11], 0\n" \
  "v_mov_b32_dpp v35, v48 quad_perm:[0,0,0,0] row_mask:0xf bank_mask:0x2\n" \
  "v_mov_b32_dpp v43, v49 quad_perm:[1,1,1,1] row_mask:0xf bank_mask:0x2\n" \
  "v_mfma_f32_16x16x32_bf16 v[44:47], a[124:127], v[12:15], v[44:47]\n" \
  "v_add_f32_dpp v35, v50, v35 quad_perm:[2,2,2,2] row_mask:0xf bank_mask:0x2\n" \
  "v_add_f32_dpp v43, v51, v43 quad_perm:[3,3,3,3] row_mask:0xf bank_mask:0x2\n" \
  "v_mfma_f32_16x16x32_bf16 v[48:51], a[128:131], v[8:11], 0\n" \
  "v_mov_b32_dpp v35, v52 quad_perm:[0,0,0,0] row_mask:0xf bank_mask:0x4\n" \
  "v_mov_b32_dpp v43, v53 quad_perm:[1,1,1,1] row_mask:0xf bank_mask:0x4\n" \
  "v_mfma_f32_16x16x32_bf16 v[48:51], a[132:135], v[12:15], v[48:51]\n" \
  "v_add_f32_dpp v35, v54, v35 quad_perm:[2,2,2,2] row_mask:0xf bank_mask:0x4\n" \
  "v_add_f32_dpp v43, v55, v43 quad_perm:[3,3,3,3] row_mask:0xf bank_mask:0x4\n" \
  "v_mfma_f32_16x16x32_bf16 v[52:55], a[136:139], v[8:11], 0\n" \
  "v_mov_b32_dpp v35, v44 quad_perm:[0,0,0,0] row_mask:0xf bank_mask:0x8\n" \
  "v_mov_b32_dpp v43, v45 quad_perm:[1,1,1,1] row_mask:0xf bank_mask:0x8\n" \
  "v_mfma_f32_16x16x32_bf16 v[52:55], a[140:143], v[12:15], v[52:55]\n" \
  "v_add_f32_dpp v35, v46, v35 quad_perm:[2,2,2,2] row_mask:0xf bank_mask:0x8\n" \
  "v_add_f32_dpp v43, v47, v43 quad_perm:[3,3,3,3] row_mask:0xf bank_mask:0x8\n" \
  "s_waitcnt lgkmcnt(0)\n" \
  "v_mfma_f32_16x16x32_bf16 v[44:47], v[56:59], v[8:11], 0\n" \
  "v_mov_b32_dpp v36, v48 quad_perm:[0,0,0,0] row_mask:0xf bank_mask:0x1\n" \
  "v_mov_b32_dpp v42, v49 quad_perm:[1,1,1,1] row_mask:0xf bank_mask:0x1\n" \
  "v_mfma_f32_16x16x32_bf16 v[44:47], v[60:63], v[12:15], v[44:47]\n" \
  "v_add_f32_dpp v36, v50, v36 quad_perm:[2,2,2,2] row_mask:0xf bank_mask:0x1\n" \
  "v_add_f32_dpp v42, v51, v42 quad_perm:[3,3,3,3] row_mask:0xf bank_mask:0x1\n" \
  "v_add_f32 v35, v35, v43\n" \
  "v_mov_b32_dpp v36, v52 quad_perm:[0,0,0,0] row_mask:0xf bank_mask:0x2\n" \
  "v_mov_b32_dpp v42, v53 quad_perm:[1,1,1,1] row_mask:0xf bank_mask:0x2\n" \
  "s_nop 0\n" \
  "v_add_f32_dpp v36, v54, v36 quad_perm:[2,2,2,2] row_mask:0xf bank_mask:0x2\n" \
  "v_add_f32_dpp v42, v55, v42 quad_perm:[3,3,3,3] row_mask:0xf bank_mask:0x2\n" \
  "s_nop 0\n" \
  "v_add_f32_dpp v36, v44, v36 quad_perm:[0,0,0,0] row_mask:0xf bank_mask:0x4\n" \
  "v_mov_b32_dpp v42, v45 quad_perm:[1,1,1,1] row_mask:0xf bank_mask:0x4\n" \
  "s_nop 0\n" \
  "v_add_f32_dpp v36, v46, v36 quad_perm:[2,2,2,2] row_mask:0xf bank_mask:0x4\n" \
  "v_add_f32_dpp v42, v47, v42 quad_perm:[3,3,3,3] row_mask:0xf bank_mask:0x4\n" \
  "v_add_f32 v36, v36, v42\n" \
  "s_waitcnt vmcnt(13)\n" \
  "s_add_u32 m0, s67, 13056\n" \
  "s_nop 0\n" \
  "buffer_load_dwordx4 v1, s[40:43], s6 offen offset:256 nt lds\n" \
  "v_max3_f32 v42, v32, v33, v34\n" \
  "v_max3_f32 v42, v42, v35, v36\n" \
  "s_nop 1\n" \
  "v_max_f32_dpp v42, v42, v42 row_ror:4 row_mask:0xf bank_mask:0xf\n" \
  "s_nop 1\n" \
  "v_max_f32_dpp v42, v42, v42 row_ror:8 row_mask:0xf bank_mask:0xf\n" \
  "s_nop 1\n" \
  "v_max_f32_dpp v42, v42, v42 row_bcast:15 row_mask:0xa bank_mask:0xf\n" \
  "s_nop 1\n" \
  "v_max_f32_dpp v42, v42, v42 row_bcast:31 row_mask:0xc bank_mask:0xf\n" \
  "s_nop 0\n" \
  "v_readlane_b32 s68, v42, 63\n" \
  "s_nop 1\n" \
  "v_subrev_f32 v32, s68, v32\n" \
  "v_subrev_f32 v33, s68, v33\n" \
  "v_subrev_f32 v34, s68, v34\n" \
  "v_subrev_f32 v35, s68, v35\n" \
  "v_subrev_f32 v36, s68, v36\n" \
  "v_exp_f32 v32, v32\n" \
  "v_exp_f32 v33, v33\n" \
  "v_exp_f32 v34, v34\n" \
  "v_exp_f32 v35, v35\n" \
  "v_exp_f32 v36, v36\n" \
  "v_add_f32 v8, v32, v33\n" \
  "v_add_f32 v9, v34, v35\n" \
  "v_cvt_pk_bf16_f32 v32, v32, v32\n" \
  "v_cvt_pk_bf16_f32 v33, v33, v33\n" \
  "v_cvt_pk_bf16_f32 v34, v34, v34\n" \
  "v_cvt_pk_bf16_f32 v35, v35, v35\n" \
  "v_add_f32 v9, v9, v36\n" \
  "v_add_f32 v8, v8, v9\n" \
  "v_cvt_pk_bf16_f32 v36, v36, v36\n" \
  "s_waitcnt vmcnt(11)\n" \
  "s_call_b64 s[34:35], .LA_GATE\n" \
  "s_add_u32 m0, s67, 14336\n" \
  "s_nop 0\n" \
  "buffer_load_dwordx4 v1, s[40:43], s7 offen nt lds\n" \
  "v_add_f32_dpp v8, v8, v8 row_ror:4 row_mask:0xf bank_mask:0xf\n" \
  "s_nop 1\n" \
  "v_add_f32_dpp v8, v8, v8 row_ror:8 row_mask:0xf bank_mask:0xf\n" \
  "s_nop 1\n" \
  "v_add_f32_dpp v8, v8, v8 row_bcast:15 row_mask:0xa bank_mask:0xf\n" \
  "s_nop 1\n" \
  "v_add_f32_dpp v8, v8, v8 row_bcast:31 row_mask:0xc bank_mask:0xf\n" \
  "s_nop 0\n" \
  "v_readlane_b32 s70, v8, 63\n" \
  "v_and_b32_dpp v48, v36, v4 row_newbcast:8 row_mask:0xf bank_mask:0xf\n" \
  "v_and_b32_dpp v49, v36, v5 row_newbcast:8 row_mask:0xf bank_mask:0xf\n" \
  "v_and_b32_dpp v50, v36, v6 row_newbcast:8 row_mask:0xf bank_mask:0xf\n" \
  "v_and_b32_dpp v51, v36, v7 row_newbcast:8 row_mask:0xf bank_mask:0xf\n" \
  "v_add_u32 v43, s78, v2\n" \
  "ds_read_b128 v[56:59], v43 offset:2048\n" \
  "ds_read_b128 v[60:63], v43 offset:3072\n" \
  "s_waitcnt lgkmcnt(1)\n" \
  "v_mfma_f32_16x16x32_bf16 v[16:19], v[48:51], v[56:59], 0\n" \
  "s_waitcnt lgkmcnt(0)\n" \
  "v_mfma_f32_16x16x32_bf16 v[20:23], v[48:51], v[60:63], 0\n" \
  "v_and_b32_dpp v44, v32, v4 row_newbcast:0 row_mask:0xf bank_mask:0xf\n" \
  "v_and_b32_dpp v45, v32, v5 row_newbcast:0 row_mask:0xf bank_mask:0xf\n" \
  "v_and_b32_dpp v46, v32, v6 row_newbcast:0 row_mask:0xf bank_mask:0xf\n" \
  "v_and_b32_dpp v47, v32, v7 row_newbcast:0 row_mask:0xf bank_mask:0xf\n" \
  "ds_read_b128 v[56:59], v2 offset:0\n" \
  "ds_read_b128 v[60:63], v2 offset:1024\n" \
  "s_waitcnt lgkmcnt(1)\n" \
  "v_mfma_f32_16x16x32_bf16 v[16:19], v[44:47], v[56:59], v[16:19]\n" \
  "s_waitcnt lgkmcnt(0)\n" \
  "v_mfma_f32_16x16x32_bf16 v[20:23], v[44:47], v[60:63], v[20:23]\n" \
  "v_and_b32_dpp v48, v32, v4 row_newbcast:4 row_mask:0xf bank_mask:0xf\n" \
  "v_and_b32_dpp v49, v32, v5 row_newbcast:4 row_mask:0xf bank_mask:0xf\n" \
  "v_and_b32_dpp v50, v32, v6 row_newbcast:4 row_mask:0xf bank_mask:0xf\n" \
  "v_and_b32_dpp v51, v32, v7 row_newbcast:4 row_mask:0xf bank_mask:0xf\n" \
  "s_waitcnt vmcnt(11)\n" \
  "s_add_u32 m0, s67, 15104\n" \
  "s_nop 0\n" \
  "buffer_load_dwordx4 v1, s[40:43], s7 offen offset:256 nt lds\n" \
  "ds_read_b128 v[56:59], v2 offset:2048\n" \
  "ds_read_b128 v[60:63], v2 offset:3072\n" \
  "s_waitcnt lgkmcnt(1)\n" \
  "v_mfma_f32_16x16x32_bf16 v[16:19], v[48:51], v[56:59], v[16:19]\n" \
  "s_waitcnt lgkmcnt(0)\n" \
  "v_mfma_f32_16x16x32_bf16 v[20:23], v[48:51], v[60:63], v[20:23]\n" \
  "v_and_b32_dpp v44, v32, v4 row_newbcast:8 row_mask:0xf bank_mask:0xf\n" \
  "v_and_b32_dpp v45, v32, v5 row_newbcast:8 row_mask:0xf bank_mask:0xf\n" \
  "v_and_b32_dpp v46, v32, v6 row_newbcast:8 row_mask:0xf bank_mask:0xf\n" \
  "v_and_b32_dpp v47, v32, v7 row_newbcast:8 row_mask:0xf bank_mask:0xf\n" \
  "s_waitcnt vmcnt(11)\n" \
  "s_nop 1\n" \
  "s_call_b64 s[34:35], .LA_GATE\n" \
  "buffer_load_dwordx4 a[144:147], v1, s[40:43], s8 offen nt\n" \
  "s_waitcnt vmcnt(11)\n" \
  "buffer_load_dwordx4 a[148:151], v1, s[40:43], s8 offen offset:256 nt\n" \
  "ds_read_b128 v[56:59], v2 offset:4096\n" \
  "ds_read_b128 v[60:63], v2 offset:5120\n" \
  "s_waitcnt lgkmcnt(1)\n" \
  "v_mfma_f32_16x16x32_bf16 v[16:19], v[44:47], v[56:59], v[16:19]\n" \
  "s_waitcnt lgkmcnt(0)\n" \
  "v_mfma_f32_16x16x32_bf16 v[20:23], v[44:47], v[60:63], v[20:23]\n" \
  "v_and_b32_dpp v48, v32, v4 row_newbcast:12 row_mask:0xf bank_mask:0xf\n" \
  "v_and_b32_dpp v49, v32, v5 row_newbcast:12 row_mask:0xf bank_mask:0xf\n" \
  "v_and_b32_dpp v50, v32, v6 row_newbcast:12 row_mask:0xf bank_mask:0xf\n" \
  "v_and_b32_dpp v51, v32, v7 row_newbcast:12 row_mask:0xf bank_mask:0xf\n" \
  "s_waitcnt vmcnt(11)\n" \
  "s_nop 1\n" \
  "s_call_b64 s[34:35], .LA_GATE\n" \
  "buffer_load_dwordx4 a[152:155], v1, s[40:43], s9 offen nt\n" \
  "s_waitcnt vmcnt(11)\n" \
  "buffer_load_dwordx4 a[156:159], v1, s[40:43], s9 offen offset:256 nt\n" \
  "ds_read_b128 v[56:59], v2 offset:6144\n" \
  "ds_read_b128 v[60:63], v2 offset:7168\n" \
  "s_waitcnt lgkmcnt(1)\n" \
  "v_mfma_f32_16x16x32_bf16 v[16:19], v[48:51], v[56:59], v[16:19]\n" \
  "s_waitcnt lgkmcnt(0)\n" \
  "v_mfma_f32_16x16x32_bf16 v[20:23], v[48:51], v[60:63], v[20:23]\n" \
  "v_and_b32_dpp v44, v33, v4 row_newbcast:0 row_mask:0xf bank_mask:0xf\n" \
  "v_and_b32_dpp v45, v33, v5 row_newbcast:0 row_mask:0xf bank_mask:0xf\n" \
  "v_and_b32_dpp v46, v33, v6 row_newbcast:0 row_mask:0xf bank_mask:0xf\n" \
  "v_and_b32_dpp v47, v33, v7 row_newbcast:0 row_mask:0xf bank_mask:0xf\n" \
  "s_waitcnt vmcnt(11)\n" \
  "s_nop 1\n" \
  "s_call_b64 s[34:35], .LA_GATE\n" \
  "buffer_load_dwordx4 a[160:163], v1, s[40:43], s10 offen nt\n" \
  "s_waitcnt vmcnt(11)\n" \
  "buffer_load_dwordx4 a[164:167], v1, s[40:43], s10 offen offset:256 nt\n" \
  "ds_read_b128 v[56:59], v2 offset:8192\n" \
  "ds_read_b128 v[60:63], v2 offset:9216\n" \
  "s_waitcnt lgkmcnt(1)\n" \
  "v_mfma_f32_16x16x32_bf16 v[16:19], v[44:47], v[56:59], v[16:19]\n" \
  "s_waitcnt lgkmcnt(0)\n" \
  "v_mfma_f32_16x16x32_bf16 v[20:23], v[44:47], v[60:63], v[20:23]\n" \
  "v_and_b32_dpp v48, v33, v4 row_newbcast:4 row_mask:0xf bank_mask:0xf\n" \
  "v_and_b32_dpp v49, v33, v5 row_newbcast:4 row_mask:0xf bank_mask:0xf\n" \
  "v_and_b32_dpp v50, v33, v6 row_newbcast:4 row_mask:0xf bank_mask:0xf\n" \
  "v_and_b32_dpp v51, v33, v7 row_newbcast:4 row_mask:0xf bank_mask:0xf\n" \
  "s_waitcnt vmcnt(11)\n" \
  "s_nop 1\n" \
  "s_call_b64 s[34:35], .LA_GATE\n" \
  "buffer_load_dwordx4 a[168:171], v1, s[40:43], s11 offen nt\n" \
  "s_waitcnt vmcnt(11)\n" \
  "buffer_load_dwordx4 a[172:175], v1, s[40:43], s11 offen offset:256 nt\n" \
  "ds_read_b128 v[56:59], v2 offset:10240\n" \
  "ds_read_b128 v[60:63], v2 offset:11264\n" \
  "s_waitcnt lgkmcnt(1)\n" \
  "v_mfma_f32_16x16x32_bf16 v[16:19], v[48:51], v[56:59], v[16:19]\n" \
  "s_waitcnt lgkmcnt(0)\n" \
  "v_mfma_f32_16x16x32_bf16 v[20:23], v[48:51], v[60:63], v[20:23]\n" \
  "v_and_b32_dpp v44, v33, v4 row_newbcast:8 row_mask:0xf bank_mask:0xf\n" \
  "v_and_b32_dpp v45, v33, v5 row_newbcast:8 row_mask:0xf bank_mask:0xf\n" \
  "v_and_b32_dpp v46, v33, v6 row_newbcast:8 row_mask:0xf bank_mask:0xf\n" \
  "v_and_b32_dpp v47, v33, v7 row_newbcast:8 row_mask:0xf bank_mask:0xf\n" \
  "s_waitcnt vmcnt(11)\n" \
  "s_nop 1\n" \
  "s_call_b64 s[34:35], .LA_GATE\n" \
  "buffer_load_dwordx4 a[176:179], v1, s[40:43], s12 offen nt\n" \
  "s_waitcnt vmcnt(11)\n" \
  "buffer_load_dwordx4 a[180:183], v1, s[40:43], s12 offen offset:256 nt\n" \
  "ds_read_b128 v[56:59], v2 offset:12288\n" \
  "ds_read_b128 v[60:63], v2 offset:13312\n" \
  "s_waitcnt lgkmcnt(1)\n" \
  "v_mfma_f32_16x16x32_bf16 v[16:19], v[44:47], v[56:59], v[16:19]\n" \
  "s_waitcnt lgkmcnt(0)\n" \
  "v_mfma_f32_16x16x32_bf16 v[20:23], v[44:47], v[60:63], v[20:23]\n" \
  "v_and_b32_dpp v48, v33, v4 row_newbcast:12 row_mask:0xf bank_mask:0xf\n" \
  "v_and_b32_dpp v49, v33, v5 row_newbcast:12 row_mask:0xf bank_mask:0xf\n" \
  "v_and_b32_dpp v50, v33, v6 row_newbcast:12 row_mask:0xf bank_mask:0xf\n" \
  "v_and_b32_dpp v51, v33, v7 row_newbcast:12 row_mask:0xf bank_mask:0xf\n" \
  "s_waitcnt vmcnt(11)\n" \
  "s_nop 1\n" \
  "s_call_b64 s[34:35], .LA_GATE\n" \
  "buffer_load_dwordx4 a[184:187], v1, s[40:43], s13 offen nt\n" \
  "s_waitcnt vmcnt(11)\n" \
  "buffer_load_dwordx4 a[188:191], v1, s[40:43], s13 offen offset:256 nt\n" \
  "ds_read_b128 v[56:59], v2 offset:14336\n" \
  "ds_read_b128 v[60:63], v2 offset:15360\n" \
  "s_waitcnt lgkmcnt(1)\n" \
  "v_mfma_f32_16x16x32_bf16 v[16:19], v[48:51], v[56:59], v[16:19]\n" \
  "s_waitcnt lgkmcnt(0)\n" \
  "v_mfma_f32_16x16x32_bf16 v[20:23], v[48:51], v[60:63], v[20:23]\n" \
  "v_and_b32_dpp v44, v34, v4 row_newbcast:0 row_mask:0xf bank_mask:0xf\n" \
  "v_and_b32_dpp v45, v34, v5 row_newbcast:0 row_mask:0xf bank_mask:0xf\n" \
  "v_and_b32_dpp v46, v34, v6 row_newbcast:0 row_mask:0xf bank_mask:0xf\n" \
  "v_and_b32_dpp v47, v34, v7 row_newbcast:0 row_mask:0xf bank_mask:0xf\n" \
  "s_waitcnt vmcnt(11)\n" \
  "s_nop 0\n" \
  "v_mfma_f32_16x16x32_bf16 v[16:19], v[44:47], a[144:147], v[16:19]\n" \
  "s_nop 6\n" \
  "s_call_b64 s[34:35], .LA_GATE\n" \
  "buffer_load_dwordx4 a[192:195], v1, s[40:43], s14 offen nt\n" \
  "s_waitcnt vmcnt(11)\n" \
  "v_mfma_f32_16x16x32_bf16 v[20:23], v[44:47], a[148:151], v[20:23]\n" \
  "buffer_load_dwordx4 a[196:199], v1, s[40:43], s14 offen offset:256 nt\n" \
  "s_sub_u32 s59, s75, 16384\n" \
  "s_cmp_eq_u32 s72, 0\n" \
  "s_cselect_b32 s59, s59, s76\n" \
  "v_and_b32 v42, 0xc0, v0\n" \
  "v_and_b32 v43, 0xf0, v0\n" \
  "v_add_u32 v42, v42, v43\n" \
  "buffer_load_dwordx4 v[8:11], v42, s[52:55], s59 offen sc1\n" \
  "buffer_load_dwordx4 v[12:15], v42, s[52:55], s59 offen offset:512 sc1\n" \
  "v_and_b32_dpp v48, v34, v4 row_newbcast:4 row_mask:0xf bank_mask:0xf\n" \
  "v_and_b32_dpp v49, v34, v5 row_newbcast:4 row_mask:0xf bank_mask:0xf\n" \
  "v_and_b32_dpp v50, v34, v6 row_newbcast:4 row_mask:0xf bank_mask:0xf\n" \
  "v_and_b32_dpp v51, v34, v7 row_newbcast:4 row_mask:0xf bank_mask:0xf\n" \
  "s_waitcnt vmcnt(13)\n" \
  "s_nop 0\n" \
  "v_mfma_f32_16x16x32_bf16 v[16:19], v[48:51], a[152:155], v[16:19]\n" \
  "s_nop 6\n" \
  "s_call_b64 s[34:35], .LA_GATE\n" \
  "buffer_load_dwordx4 a[200:203], v1, s[40:43], s15 offen nt\n" \
  "s_waitcnt vmcnt(13)\n" \
  "v_mfma_f32_16x16x32_bf16 v[20:23], v[48:51], a[156:159], v[20:23]\n" \
  "buffer_load_dwordx4 a[204:207], v1, s[40:43], s15 offen offset:256 nt\n" \
  "v_and_b32_dpp v44, v34, v4 row_newbcast:8 row_mask:0xf bank_mask:0xf\n" \
  "v_and_b32_dpp v45, v34, v5 row_newbcast:8 row_mask:0xf bank_mask:0xf\n" \
  "v_and_b32_dpp v46, v34, v6 row_newbcast:8 row_mask:0xf bank_mask:0xf\n" \
  "v_and_b32_dpp v47, v34, v7 row_newbcast:8 row_mask:0xf bank_mask:0xf\n" \
  "s_waitcnt vmcnt(13)\n" \
  "s_nop 0\n" \
  "v_mfma_f32_16x16x32_bf16 v[16:19], v[44:47], a[160:163], v[16:19]\n" \
  "s_nop 6\n" \
  "s_call_b64 s[34:35], .LA_GATE\n" \
  "buffer_load_dwordx4 a[208:211], v1, s[40:43], s16 offen nt\n" \
  "s_waitcnt vmcnt(13)\n" \
  "v_mfma_f32_16x16x32_bf16 v[20:23], v[44:47], a[164:167], v[20:23]\n" \
  "buffer_load_dwordx4 a[212:215], v1, s[40:43], s16 offen offset:256 nt\n" \
  "v_and_b32_dpp v48, v34, v4 row_newbcast:12 row_mask:0xf bank_mask:0xf\n" \
  "v_and_b32_dpp v49, v34, v5 row_newbcast:12 row_mask:0xf bank_mask:0xf\n" \
  "v_and_b32_dpp v50, v34, v6 row_newbcast:12 row_mask:0xf bank_mask:0xf\n" \
  "v_and_b32_dpp v51, v34, v7 row_newbcast:12 row_mask:0xf bank_mask:0xf\n" \
  "s_waitcnt vmcnt(13)\n" \
  "s_nop 0\n" \
  "v_mfma_f32_16x16x32_bf16 v[16:19], v[48:51], a[168:171], v[16:19]\n" \
  "s_nop 6\n" \
  "s_call_b64 s[34:35], .LA_GATE\n" \
  "buffer_load_dwordx4 a[216:219], v1, s[40:43], s17 offen nt\n" \
  "s_waitcnt vmcnt(13)\n" \
  "v_mfma_f32_16x16x32_bf16 v[20:23], v[48:51], a[172:175], v[20:23]\n" \
  "buffer_load_dwordx4 a[220:223], v1, s[40:43], s17 offen offset:256 nt\n" \
  "s_lshl_b32 s84, s72, 1\n" \
  "s_nop 4\n" \
  "s_call_b64 s[94:95], .LA_GUARD\n" \
  "s_mov_b32 s73, 1\n" \
  "s_cmp_lg_u32 s72, 0\n" \
  "s_cbranch_scc1 .LA_CHK_A\n" \
  "s_waitcnt vmcnt(13)\n" \
  "s_add_u32 s36, s36, 0x2000000\n" \
  "s_addc_u32 s37, s37, 0\n" \
  "s_mov_b64 s[44:45], s[36:37]\n" \
  "s_mov_b32 s46, s62\n" \
  "s_add_u32 m0, s66, 0\n" \
  "s_nop 0\n" \
  "buffer_load_dwordx4 v0, s[44:47], s58 offen nt lds\n" \
  "s_waitcnt vmcnt(13)\n" \
  "s_add_u32 m0, s66, 768\n" \
  "s_nop 0\n" \
  "buffer_load_dwordx4 v0, s[44:47], s58 offen offset:256 nt lds\n" \
  "s_waitcnt vmcnt(13)\n" \
  "s_call_b64 s[34:35], .LA_GATE\n" \
  "buffer_load_dwordx4 a[224:227], v0, s[36:39], s0 offen nt\n" \
  "s_waitcnt vmcnt(13)\n" \
  "buffer_load_dwordx4 a[228:231], v0, s[36:39], s0 offen offset:256 nt\n" \
  "s_waitcnt vmcnt(13)\n" \
  "s_call_b64 s[34:35], .LA_GATE\n" \
  "buffer_load_dwordx4 a[232:235], v0, s[36:39], s1 offen nt\n" \
  "s_waitcnt vmcnt(13)\n" \
  "buffer_load_dwordx4 a[236:239], v0, s[36:39], s1 offen offset:256 nt\n" \
  "s_branch .LA_FAST_A\n" \
  ".LA_CHK_A:\n" \
  "s_waitcnt vmcnt(6)\n" \
  "v_max3_u32 v42, v8, v9, v10\n" \
  "v_max3_u32 v43, v11, v12, v13\n" \
  "v_max3_u32 v42, v42, v14, v15\n" \
  "v_max_u32 v42, v42, v43\n" \
  "v_cmp_eq_u32 vcc, -1, v42\n" \
  "s_nop 0\n" \
  "s_cbranch_vccnz .LA_DEFER_A\n" \
  "s_add_u32 s76, s76, 16384\n" \
  "v_mfma_f32_16x16x32_bf16 v[44:47], a[224:227], v[8:11], 0\n" \
  "v_add_u32 v43, s80, v2\n" \
  "v_mov_b32 v41, 0xff800000\n" \
  "v_mfma_f32_16x16x32_bf16 v[44:47], a[228:231], v[12:15], v[44:47]\n" \
  "ds_read_b128 v[56:59], v43\n" \
  "ds_read_b128 v[60:63], v43 offset:1024\n" \
  "v_mov_b32 v42, s64\n" \
  "v_mfma_f32_16x16x32_bf16 v[48:51], a[232:235], v[8:11], 0\n" \
  "v_mfma_f32_16x16x32_bf16 v[48:51], a[236:239], v[12:15], v[48:51]\n" \
  "v_mov_b32_dpp v41, v42 quad_perm:[0,1,2,3] row_mask:0xf bank_mask:0x4\n" \
  "v_mfma_f32_16x16x32_bf16 v[52:55], a[240:243], v[8:11], 0\n" \
  "s_nop 0\n" \
  "v_mov_b32_dpp v37, v44 quad_perm:[0,0,0,0] row_mask:0xf bank_mask:0x1\n" \
  "v_mov_b32_dpp v42, v45 quad_perm:[1,1,1,1] row_mask:0xf bank_mask:0x1\n" \
  "v_mfma_f32_16x16x32_bf16 v[52:55], a[244:247], v[12:15], v[52:55]\n" \
  "v_add_f32_dpp v37, v46, v37 quad_perm:[2,2,2,2] row_mask:0xf bank_mask:0x1\n" \
  "v_add_f32_dpp v42, v47, v42 quad_perm:[3,3,3,3] row_mask:0xf bank_mask:0x1\n" \
  "v_mfma_f32_16x16x32_bf16 v[44:47], a[248:251], v[8:11], 0\n" \
  "v_mov_b32_dpp v37, v48 quad_perm:[0,0,0,0] row_mask:0xf bank_mask:0x2\n" \
  "v_mov_b32_dpp v42, v49 quad_perm:[1,1,1,1] row_mask:0xf bank_mask:0x2\n" \
  "v_mfma_f32_16x16x32_bf16 v[44:47], a[252:255], v[12:15], v[44:47]\n" \
  "v_add_f32_dpp v37, v50, v37 quad_perm:[2,2,2,2] row_mask:0xf bank_mask:0x2\n" \
  "v_add_f32_dpp v42, v51, v42 quad_perm:[3,3,3,3] row_mask:0xf bank_mask:0x2\n" \
  "s_waitcnt lgkmcnt(0)\n" \
  "v_mfma_f32_16x16x32_bf16 v[48:51], v[64:67], v[8:11], 0\n" \
  "s_waitcnt vmcnt(13)\n" \
  "s_add_u32 s36, s36, 0x2000000\n" \
  "s_addc_u32 s37, s37, 0\n" \
  "s_mov_b64 s[44:45], s[36:37]\n" \
  "s_mov_b32 s46, s62\n" \
  "s_add_u32 m0, s66, 0\n" \
  "s_nop 0\n" \
  "buffer_load_dwordx4 v0, s[44:47], s58 offen nt lds\n" \
  "s_waitcnt vmcnt(13)\n" \
  "s_add_u32 m0, s66, 768\n" \
  "s_nop 0\n" \
  "buffer_load_dwordx4 v0, s[44:47], s58 offen offset:256 nt lds\n" \
  "s_waitcnt vmcnt(13)\n" \
  "s_add_u32 s33, s33, s27\n" \
  "buffer_load_dwordx4 a[224:227], v0, s[36:39], s0 offen nt\n" \
  "v_mov_b32_dpp v37, v52 quad_perm:[0,0,0,0] row_mask:0xf bank_mask:0x4\n" \
  "v_mov_b32_dpp v42, v53 quad_perm:[1,1,1,1] row_mask:0xf bank_mask:0x4\n" \
  "v_mfma_f32_16x16x32_bf16 v[48:51], v[68:71], v[12:15], v[48:51]\n" \
  "v_add_f32_dpp v37, v54, v37 quad_perm:[2,2,2,2] row_mask:0xf bank_mask:0x4\n" \
  "v_add_f32_dpp v42, v55, v42 quad_perm:[3,3,3,3] row_mask:0xf bank_mask:0x4\n" \
  "v_mfma_f32_16x16x32_bf16 v[52:55], v[72:75], v[8:11], 0\n" \
  "v_mov_b32_dpp v37, v44 quad_perm:[0,0,0,0] row_mask:0xf bank_mask:0x8\n" \
  "v_mov_b32_dpp v42, v45 quad_perm:[1,1,1,1] row_mask:0xf bank_mask:0x8\n" \
  "v_mfma_f32_16x16x32_bf16 v[52:55], v[76:79], v[12:15], v[52:55]\n" \
  "v_add_f32_dpp v37, v46, v37 quad_perm:[2,2,2,2] row_mask:0xf bank_mask:0x8\n" \
  "v_add_f32_dpp v42, v47, v42 quad_perm:[3,3,3,3] row_mask:0xf bank_mask:0x8\n" \
  "v_mfma_f32_16x16x32_bf16 v[44:47], v[80:83], v[8:11], 0\n" \
  "v_mov_b32_dpp v38, v48 quad_perm:[0,0,0,0] row_mask:0xf bank_mask:0x1\n" \
  "v_mov_b32_dpp v43, v49 quad_perm:[1,1,1,1] row_mask:0xf bank_mask:0x1\n" \
  "v_mfma_f32_16x16x32_bf16 v[44:47], v[84:87], v[12:15], v[44:47]\n" \
  "v_add_f32_dpp v38, v50, v38 quad_perm:[2,2,2,2] row_mask:0xf bank_mask:0x1\n" \
  "v_add_f32_dpp v43, v51, v43 quad_perm:[3,3,3,3] row_mask:0xf bank_mask:0x1\n" \
  "v_add_f32 v37, v37, v42\n" \
  "v_mfma_f32_16x16x32_bf16 v[48:51], v[88:91], v[8:11], 0\n" \
  "v_mov_b32_dpp v38, v52 quad_perm:[0,0,0,0] row_mask:0xf bank_mask:0x2\n" \
  "v_mov_b32_dpp v43, v53 quad_perm:[1,1,1,1] row_mask:0xf bank_mask:0x2\n" \
  "v_mfma_f32_16x16x32_bf16 v[48:51], v[92:95], v[12:15], v[48:51]\n" \
  "v_add_f32_dpp v38, v54, v38 quad_perm:[2,2,2,2] row_mask:0xf bank_mask:0x2\n" \
  "v_add_f32_dpp v43, v55, v43 quad_perm:[3,3,3,3] row_mask:0xf bank_mask:0x2\n" \
  "v_mfma_f32_16x16x32_bf16 v[52:55], v[96:99], v[8:11], 0\n" \
  "v_mov_b32_dpp v38, v44 quad_perm:[0,0,0,0] row_mask:0xf bank_mask:0x4\n" \
  "v_mov_b32_dpp v43, v45 quad_perm:[1,1,1,1] row_mask:0xf bank_mask:0x4\n" \
  "v_mfma_f32_16x16x32_bf16 v[52:55], v[100:103], v[12:15], v[52:55]\n" \
  "v_add_f32_dpp v38, v46, v38 quad_perm:[2,2,2,2] row_mask:0xf bank_mask:0x4\n" \
  "v_add_f32_dpp v43, v47, v43 quad_perm:[3,3,3,3] row_mask:0xf bank_mask:0x4\n" \
  "v_mfma_f32_16x16x32_bf16 v[44:47], v[104:107], v[8:11], 0\n" \
  "v_mov_b32_dpp v38, v48 quad_perm:[0,0,0,0] row_mask:0xf bank_mask:0x8\n" \
  "v_mov_b32_dpp v43, v49 quad_perm:[1,1,1,1] row_mask:0xf bank_mask:0x8\n" \
  "v_mfma_f32_16x16x32_bf16 v[44:47], v[108:111], v[12:15], v[44:47]\n" \
  "s_waitcnt vmcnt(13)\n" \
  "buffer_load_dwordx4 a[228:231], v0, s[36:39], s0 offen offset:256 nt\n" \
  "v_add_f32_dpp v38, v50, v38 quad_perm:[2,2,2,2] row_mask:0xf bank_mask:0x8\n" \
  "v_add_f32_dpp v43, v51, v43 quad_perm:[3,3,3,3] row_mask:0xf bank_mask:0x8\n" \
  "v_mfma_f32_16x16x32_bf16 v[48:51], v[112:115], v[8:11], 0\n" \
  "v_mov_b32_dpp v39, v52 quad_perm:[0,0,0,0] row_mask:0xf bank_mask:0x1\n" \
  "v_mov_b32_dpp v42, v53 quad_perm:[1,1,1,1] row_mask:0xf bank_mask:0x1\n" \
  "v_mfma_f32_16x16x32_bf16 v[48:51], v[116:119], v[12:15], v[48:51]\n" \
  "v_add_f32_dpp v39, v54, v39 quad_perm:[2,2,2,2] row_mask:0xf bank_mask:0x1\n" \
  "v_add_f32_dpp v42, v55, v42 quad_perm:[3,3,3,3] row_mask:0xf bank_mask:0x1\n" \
  "v_add_f32 v38, v38, v43\n" \
  "v_mfma_f32_16x16x32_bf16 v[52:55], v[120:123], v[8:11], 0\n" \
  "v_mov_b32_dpp v39, v44 quad_perm:[0,0,0,0] row_mask:0xf bank_mask:0x2\n" \
  "v_mov_b32_dpp v42, v45 quad_perm:[1,1,1,1] row_mask:0xf bank_mask:0x2\n" \
  "v_mfma_f32_16x16x32_bf16 v[52:55], v[124:127], v[12:15], v[52:55]\n" \
  "v_add_f32_dpp v39, v46, v39 quad_perm:[2,2,2,2] row_mask:0xf bank_mask:0x2\n" \
  "v_add_f32_dpp v42, v47, v42 quad_perm:[3,3,3,3] row_mask:0xf bank_mask:0x2\n" \
  "v_mfma_f32_16x16x32_bf16 v[44:47], v[128:131], v[8:11], 0\n" \
  "v_mov_b32_dpp v39, v48 quad_perm:[0,0,0,0] row_mask:0xf bank_mask:0x4\n" \
  "v_mov_b32_dpp v42, v49 quad_perm:[1,1,1,1] row_mask:0xf bank_mask:0x4\n" \
  "v_mfma_f32_16x16x32_bf16 v[44:47], v[132:135], v[12:15], v[44:47]\n" \
  "v_add_f32_dpp v39, v50, v39 quad_perm:[2,2,2,2] row_mask:0xf bank_mask:0x4\n" \
  "v_add_f32_dpp v42, v51, v42 quad_perm:[3,3,3,3] row_mask:0xf bank_mask:0x4\n" \
  "v_mfma_f32_16x16x32_bf16 v[48:51], v[136:139], v[8:11], 0\n" \
  "v_mov_b32_dpp v39, v52 quad_perm:[0,0,0,0] row_mask:0xf bank_mask:0x8\n" \
  "v_mov_b32_dpp v42, v53 quad_perm:[1,1,1,1] row_mask:0xf bank_mask:0x8\n" \
  "v_mfma_f32_16x16x32_bf16 v[48:51], v[140:143], v[12:15], v[48:51]\n" \
  "v_add_f32_dpp v39, v54, v39 quad_perm:[2,2,2,2] row_mask:0xf bank_mask:0x8\n" \
  "v_add_f32_dpp v42, v55, v42 quad_perm:[3,3,3,3] row_mask:0xf bank_mask:0x8\n" \
  "v_mfma_f32_16x16x32_bf16 v[52:55], v[144:147], v[8:11], 0\n" \
  "v_mov_b32_dpp v40, v44 quad_perm:[0,0,0,0] row_mask:0xf bank_mask:0x1\n" \
  "v_mov_b32_dpp v43, v45 quad_perm:[1,1,1,1] row_mask:0xf bank_mask:0x1\n" \
  "v_mfma_f32_16x16x32_bf16 v[52:55], v[148:151], v[12:15], v[52:55]\n" \
  "v_add_f32_dpp v40, v46, v40 quad_perm:[2,2,2,2] row_mask:0xf bank_mask:0x1\n" \
  "v_add_f32_dpp v43, v47, v43 quad_perm:[3,3,3,3] row_mask:0xf bank_mask:0x1\n" \
  "v_add_f32 v39, v39, v42\n" \
  "v_mfma_f32_16x16x32_bf16 v[44:47], v[152:155], v[8:11], 0\n" \
  "s_waitcnt vmcnt(13)\n" \
  "s_add_u32 s33, s33, s27\n" \
  "buffer_load_dwordx4 a[232:235], v0, s[36:39], s1 offen nt\n" \
  "v_mov_b32_dpp v40, v48 quad_perm:[0,0,0,0] row_mask:0xf bank_mask:0x2\n" \
  "v_mov_b32_dpp v43, v49 quad_perm:[1,1,1,1] row_mask:0xf bank_mask:0x2\n" \
  "v_mfma_f32_16x16x32_bf16 v[44:47], v[156:159], v[12:15], v[44:47]\n" \
  "v_add_f32_dpp v40, v50, v40 quad_perm:[2,2,2,2] row_mask:0xf bank_mask:0x2\n" \
  "v_add_f32_dpp v43, v51, v43 quad_perm:[3,3,3,3] row_mask:0xf bank_mask:0x2\n" \
  "v_mfma_f32_16x16x32_bf16 v[48:51], v[160:163], v[8:11], 0\n" \
  "v_mov_b32_dpp v40, v52 quad_perm:[0,0,0,0] row_mask:0xf bank_mask:0x4\n" \
  "v_mov_b32_dpp v43, v53 quad_perm:[1,1,1,1] row_mask:0xf bank_mask:0x4\n" \
  "v_mfma_f32_16x16x32_bf16 v[48:51], v[164:167], v[12:15], v[48:51]\n" \
  "v_add_f32_dpp v40, v54, v40 quad_perm:[2,2,2,2] row_mask:0xf bank_mask:0x4\n" \
  "v_add_f32_dpp v43, v55, v43 quad_perm:[3,3,3,3] row_mask:0xf bank_mask:0x4\n" \
  "v_mfma_f32_16x16x32_bf16 v[52:55], v[168:171], v[8:11], 0\n" \
  "v_mov_b32_dpp v40, v44 quad_perm:[0,0,0,0] row_mask:0xf bank_mask:0x8\n" \
  "v_mov_b32_dpp v43, v45 quad_perm:[1,1,1,1] row_mask:0xf bank_mask:0x8\n" \
  "v_mfma_f32_16x16x32_bf16 v[52:55], v[172:175], v[12:15], v[52:55]\n" \
  "v_add_f32_dpp v40, v46, v40 quad_perm:[2,2,2,2] row_mask:0xf bank_mask:0x8\n" \
  "v_add_f32_dpp v43, v47, v43 quad_perm:[3,3,3,3] row_mask:0xf bank_mask:0x8\n" \
  "v_mfma_f32_16x16x32_bf16 v[44:47], v[56:59], v[8:11], 0\n" \
  "v_mov_b32_dpp v41, v48 quad_perm:[0,0,0,0] row_mask:0xf bank_mask:0x1\n" \
  "v_mov_b32_dpp v42, v49 quad_perm:[1,1,1,1] row_mask:0xf bank_mask:0x1\n" \
  "v_mfma_f32_16x16x32_bf16 v[44:47], v[60:63], v[12:15], v[44:47]\n" \
  "v_add_f32_dpp v41, v50, v41 quad_perm:[2,2,2,2] row_mask:0xf bank_mask:0x1\n" \
  "v_add_f32_dpp v42, v51, v42 quad_perm:[3,3,3,3] row_mask:0xf bank_mask:0x1\n" \
  "v_add_f32 v40, v40, v43\n" \
  "v_mov_b32_dpp v41, v52 quad_perm:[0,0,0,0] row_mask:0xf bank_mask:0x2\n" \
  "v_mov_b32_dpp v42, v53 quad_perm:[1,1,1,1] row_mask:0xf bank_mask:0x2\n" \
  "s_nop 0\n" \
  "v_add_f32_dpp v41, v54, v41 quad_perm:[2,2,2,2] row_mask:0xf bank_mask:0x2\n" \
  "v_add_f32_dpp v42, v55, v42 quad_perm:[3,3,3,3] row_mask:0xf bank_mask:0x2\n" \
  "s_nop 0\n" \
  "v_add_f32_dpp v41, v44, v41 quad_perm:[0,0,0,0] row_mask:0xf bank_mask:0x4\n" \
  "v_mov_b32_dpp v42, v45 quad_perm:[1,1,1,1] row_mask:0xf bank_mask:0x4\n" \
  "s_nop 0\n" \
  "v_add_f32_dpp v41, v46, v41 quad_perm:[2,2,2,2] row_mask:0xf bank_mask:0x4\n" \
  "v_add_f32_dpp v42, v47, v42 quad_perm:[3,3,3,3] row_mask:0xf bank_mask:0x4\n" \
  "v_add_f32 v41, v41, v42\n" \
  "v_add_u32 v43, s80, v2\n" \
  "v_max3_f32 v42, v37, v38, v39\n" \
  "v_max3_f32 v42, v42, v40, v41\n" \
  "ds_read_b128 v[168:171], v43 offset:2048\n" \
  "ds_read_b128 v[172:175], v43 offset:3072\n" \
  "v_max_f32_dpp v42, v42, v42 row_ror:4 row_mask:0xf bank_mask:0xf\n" \
  "ds_read_b128 v[104:107], v2 offset:16384\n" \
  "ds_read_b128 v[108:111], v2 offset:17408\n" \
  "v_max_f32_dpp v42, v42, v42 row_ror:8 row_mask:0xf bank_mask:0xf\n" \
  "ds_read_b128 v[112:115], v2 offset:18432\n" \
  "ds_read_b128 v[116:119], v2 offset:19456\n" \
  "v_max_f32_dpp v42, v42, v42 row_bcast:15 row_mask:0xa bank_mask:0xf\n" \
  "ds_read_b128 v[120:123], v2 offset:20480\n" \
  "ds_read_b128 v[124:127], v2 offset:21504\n" \
  "v_max_f32_dpp v42, v42, v42 row_bcast:31 row_mask:0xc bank_mask:0xf\n" \
  "ds_read_b128 v[128:131], v2 offset:22528\n" \
  "ds_read_b128 v[132:135], v2 offset:23552\n" \
  "v_readlane_b32 s69, v42, 63\n" \
  "ds_read_b128 v[136:139], v2 offset:24576\n" \
  "ds_read_b128 v[140:143], v2 offset:25600\n" \
  "v_subrev_f32 v37, s69, v37\n" \
  "v_subrev_f32 v38, s69, v38\n" \
  "v_subrev_f32 v39, s69, v39\n" \
  "v_subrev_f32 v40, s69, v40\n" \
  "v_subrev_f32 v41, s69, v41\n" \
  "v_exp_f32 v37, v37\n" \
  "v_exp_f32 v38, v38\n" \
  "v_exp_f32 v39, v39\n" \
  "v_exp_f32 v40, v40\n" \
  "v_exp_f32 v41, v41\n" \
  "v_add_f32 v8, v37, v38\n" \
  "v_add_f32 v9, v39, v40\n" \
  "v_cvt_pk_bf16_f32 v37, v37, v37\n" \
  "v_cvt_pk_bf16_f32 v38, v38, v38\n" \
  "v_cvt_pk_bf16_f32 v39, v39, v39\n" \
  "v_cvt_pk_bf16_f32 v40, v40, v40\n" \
  "v_add_f32 v9, v9, v41\n" \
  "v_add_f32 v8, v8, v9\n" \
  "v_cvt_pk_bf16_f32 v41, v41, v41\n" \
  "ds_read_b128 v[144:147], v2 offset:26624\n" \
  "ds_read_b128 v[148:151], v2 offset:27648\n" \
  "ds_read_b128 v[152:155], v2 offset:28672\n" \
  "ds_read_b128 v[156:159], v2 offset:29696\n" \
  "ds_read_b128 v[160:163], v2 offset:30720\n" \
  "ds_read_b128 v[164:167], v2 offset:31744\n" \
  "s_waitcnt vmcnt(13)\n" \
  "buffer_load_dwordx4 a[236:239], v0, s[36:39], s1 offen offset:256 nt\n" \
  ".LA_FAST_A:\n" \
  "v_and_b32_dpp v44, v35, v4 row_newbcast:0 row_mask:0xf bank_mask:0xf\n" \
  "v_and_b32_dpp v45, v35, v5 row_newbcast:0 row_mask:0xf bank_mask:0xf\n" \
  "v_and_b32_dpp v46, v35, v6 row_newbcast:0 row_mask:0xf bank_mask:0xf\n" \
  "v_and_b32_dpp v47, v35, v7 row_newbcast:0 row_mask:0xf bank_mask:0xf\n" \
  "s_nop 1\n" \
  "v_mfma_f32_16x16x32_bf16 v[16:19], v[44:47], a[176:179], v[16:19]\n" \
  "v_mfma_f32_16x16x32_bf16 v[20:23], v[44:47], a[180:183], v[20:23]\n" \
  "v_and_b32_dpp v48, v35, v4 row_newbcast:4 row_mask:0xf bank_mask:0xf\n" \
  "v_and_b32_dpp v49, v35, v5 row_newbcast:4 row_mask:0xf bank_mask:0xf\n" \
  "v_and_b32_dpp v50, v35, v6 row_newbcast:4 row_mask:0xf bank_mask:0xf\n" \
  "v_and_b32_dpp v51, v35, v7 row_newbcast:4 row_mask:0xf bank_mask:0xf\n" \
  "s_nop 1\n" \
  "v_mfma_f32_16x16x32_bf16 v[16:19], v[48:51], a[184:187], v[16:19]\n" \
  "v_mfma_f32_16x16x32_bf16 v[20:23], v[48:51], a[188:191], v[20:23]\n" \
  "v_and_b32_dpp v44, v35, v4 row_newbcast:8 row_mask:0xf bank_mask:0xf\n" \
  "v_and_b32_dpp v45, v35, v5 row_newbcast:8 row_mask:0xf bank_mask:0xf\n" \
  "v_and_b32_dpp v46, v35, v6 row_newbcast:8 row_mask:0xf bank_mask:0xf\n" \
  "v_and_b32_dpp v47, v35, v7 row_newbcast:8 row_mask:0xf bank_mask:0xf\n" \
  "s_nop 1\n" \
  "v_mfma_f32_16x16x32_bf16 v[16:19], v[44:47], a[192:195], v[16:19]\n" \
  "v_mfma_f32_16x16x32_bf16 v[20:23], v[44:47], a[196:199], v[20:23]\n" \
  "v_and_b32_dpp v48, v35, v4 row_newbcast:12 row_mask:0xf bank_mask:0xf\n" \
  "v_and_b32_dpp v49, v35, v5 row_newbcast:12 row_mask:0xf bank_mask:0xf\n" \
  "v_and_b32_dpp v50, v35, v6 row_newbcast:12 row_mask:0xf bank_mask:0xf\n" \
  "v_and_b32_dpp v51, v35, v7 row_newbcast:12 row_mask:0xf bank_mask:0xf\n" \
  "s_waitcnt vmcnt(11)\n" \
  "s_nop 0\n" \
  "v_mfma_f32_16x16x32_bf16 v[16:19], v[48:51], a[200:203], v[16:19]\n" \
  "s_waitcnt vmcnt(10)\n" \
  "v_mfma_f32_16x16x32_bf16 v[20:23], v[48:51], a[204:207], v[20:23]\n" \
  "v_and_b32_dpp v44, v36, v4 row_newbcast:0 row_mask:0xf bank_mask:0xf\n" \
  "v_and_b32_dpp v45, v36, v5 row_newbcast:0 row_mask:0xf bank_mask:0xf\n" \
  "v_and_b32_dpp v46, v36, v6 row_newbcast:0 row_mask:0xf bank_mask:0xf\n" \
  "v_and_b32_dpp v47, v36, v7 row_newbcast:0 row_mask:0xf bank_mask:0xf\n" \
  "s_waitcnt vmcnt(9)\n" \
  "s_nop 0\n" \
  "v_mfma_f32_16x16x32_bf16 v[16:19], v[44:47], a[208:211], v[16:19]\n" \
  "s_waitcnt vmcnt(8)\n" \
  "v_mfma_f32_16x16x32_bf16 v[20:23], v[44:47], a[212:215], v[20:23]\n" \
  "s_add_u32 s33, s33, s27\n" \
  "buffer_load_dwordx4 a[240:243], v0, s[36:39], s2 offen nt\n" \
  "v_and_b32_dpp v48, v36, v4 row_newbcast:4 row_mask:0xf bank_mask:0xf\n" \
  "v_and_b32_dpp v49, v36, v5 row_newbcast:4 row_mask:0xf bank_mask:0xf\n" \
  "v_and_b32_dpp v50, v36, v6 row_newbcast:4 row_mask:0xf bank_mask:0xf\n" \
  "v_and_b32_dpp v51, v36, v7 row_newbcast:4 row_mask:0xf bank_mask:0xf\n" \
  "s_waitcnt vmcnt(8)\n" \
  "s_nop 0\n" \
  "v_mfma_f32_16x16x32_bf16 v[16:19], v[48:51], a[216:219], v[16:19]\n" \
  "s_waitcnt vmcnt(7)\n" \
  "v_mfma_f32_16x16x32_bf16 v[20:23], v[48:51], a[220:223], v[20:23]\n" \
  "s_mov_b32 s81, 151552\n" \
  "s_mov_b32 s82, s77\n" \
  "s_add_u32 s77, s77, 143360\n" \
  "s_add_u32 s59, s87, 151552\n" \
  "s_sub_u32 s85, 151552, s67\n" \
  "v_add_u32 v42, 151552, v3\n" \
  "v_and_b32 v43, 0x30, v2\n" \
  "v_add_u32 v63, s85, v2\n" \
  "v_mov_b32 v52, 1\n" \
  "v_add_u32 v43, 151552, v43\n" \
  "v_mov_b32 v48, s68\n" \
  "v_mov_b32 v49, s70\n" \
  "v_mov_b32 v50, s59\n" \
  "v_mov_b32 v51, s81\n" \
  "v_cndmask_b32 v44, v16, v20, s[88:89]\n" \
  "v_cndmask_b32 v45, v17, v21, s[88:89]\n" \
  "v_cndmask_b32 v46, v18, v22, s[88:89]\n" \
  "v_cndmask_b32 v47, v19, v23, s[88:89]\n" \
  "s_call_b64 s[94:95], .LA_MERGE\n" \
  "buffer_load_dwordx4 a[244:247], v0, s[36:39], s2 offen offset:256 nt\n" \
  "s_add_u32 s33, s33, s27\n" \
  "buffer_load_dwordx4 a[248:251], v0, s[36:39], s3 offen nt\n" \
  "s_cmp_lg_u32 s72, 0\n" \
  "s_cbranch_scc1 .LA_PV2_A\n" \
  "buffer_load_dwordx4 a[252:255], v0, s[36:39], s3 offen offset:256 nt\n" \
  "s_call_b64 s[34:35], .LA_GATE\n" \
  "buffer_load_dwordx4 v[64:67], v0, s[36:39], s4 offen nt\n" \
  "buffer_load_dwordx4 v[68:71], v0, s[36:39], s4 offen offset:256 nt\n" \
  "s_waitcnt vmcnt(13)\n" \
  "s_call_b64 s[34:35], .LA_GATE\n" \
  "buffer_load_dwordx4 v[72:75], v0, s[36:39], s5 offen nt\n" \
  "s_branch .LA_PAD_A\n" \
  ".LA_PV2_A:\n" \
  "v_and_b32_dpp v44, v39, v4 row_newbcast:0 row_mask:0xf bank_mask:0xf\n" \
  "v_and_b32_dpp v45, v39, v5 row_newbcast:0 row_mask:0xf bank_mask:0xf\n" \
  "v_and_b32_dpp v46, v39, v6 row_newbcast:0 row_mask:0xf bank_mask:0xf\n" \
  "v_and_b32_dpp v47, v39, v7 row_newbcast:0 row_mask:0xf bank_mask:0xf\n" \
  "v_and_b32_dpp v48, v39, v4 row_newbcast:4 row_mask:0xf bank_mask:0xf\n" \
  "v_and_b32_dpp v49, v39, v5 row_newbcast:4 row_mask:0xf bank_mask:0xf\n" \
  "v_mfma_f32_16x16x32_bf16 v[24:27], v[44:47], v[176:179], 0\n" \
  "v_and_b32_dpp v50, v39, v6 row_newbcast:4 row_mask:0xf bank_mask:0xf\n" \
  "v_and_b32_dpp v51, v39, v7 row_newbcast:4 row_mask:0xf bank_mask:0xf\n" \
  "v_add_f32_dpp v8, v8, v8 row_ror:4 row_mask:0xf bank_mask:0xf\n" \
  "v_mfma_f32_16x16x32_bf16 v[28:31], v[44:47], v[180:183], 0\n" \
  "v_and_b32_dpp v44, v39, v4 row_newbcast:8 row_mask:0xf bank_mask:0xf\n" \
  "v_and_b32_dpp v45, v39, v5 row_newbcast:8 row_mask:0xf bank_mask:0xf\n" \
  "v_mfma_f32_16x16x32_bf16 v[24:27], v[48:51], v[184:187], v[24:27]\n" \
  "v_and_b32_dpp v46, v39, v6 row_newbcast:8 row_mask:0xf bank_mask:0xf\n" \
  "v_and_b32_dpp v47, v39, v7 row_newbcast:8 row_mask:0xf bank_mask:0xf\n" \
  "v_add_f32_dpp v8, v8, v8 row_ror:8 row_mask:0xf bank_mask:0xf\n" \
  "v_mfma_f32_16x16x32_bf16 v[28:31], v[48:51], v[188:191], v[28:31]\n" \
  "buffer_load_dwordx4 a[252:255], v0, s[36:39], s3 offen offset:256 nt\n" \
  "v_and_b32_dpp v48, v39, v4 row_newbcast:12 row_mask:0xf bank_mask:0xf\n" \
  "v_and_b32_dpp v49, v39, v5 row_newbcast:12 row_mask:0xf bank_mask:0xf\n" \
  "v_mfma_f32_16x16x32_bf16 v[24:27], v[44:47], v[192:195], v[24:27]\n" \
  "v_and_b32_dpp v50, v39, v6 row_newbcast:12 row_mask:0xf bank_mask:0xf\n" \
  "v_and_b32_dpp v51, v39, v7 row_newbcast:12 row_mask:0xf bank_mask:0xf\n" \
  "v_add_f32_dpp v8, v8, v8 row_bcast:15 row_mask:0xa bank_mask:0xf\n" \
  "v_mfma_f32_16x16x32_bf16 v[28:31], v[44:47], v[196:199], v[28:31]\n" \
  "v_and_b32_dpp v44, v40, v4 row_newbcast:0 row_mask:0xf bank_mask:0xf\n" \
  "v_and_b32_dpp v45, v40, v5 row_newbcast:0 row_mask:0xf bank_mask:0xf\n" \
  "v_mfma_f32_16x16x32_bf16 v[24:27], v[48:51], v[200:203], v[24:27]\n" \
  "v_and_b32_dpp v46, v40, v6 row_newbcast:0 row_mask:0xf bank_mask:0xf\n" \
  "v_and_b32_dpp v47, v40, v7 row_newbcast:0 row_mask:0xf bank_mask:0xf\n" \
  "v_add_f32_dpp v8, v8, v8 row_bcast:31 row_mask:0xc bank_mask:0xf\n" \
  "v_mfma_f32_16x16x32_bf16 v[28:31], v[48:51], v[204:207], v[28:31]\n" \
  "v_and_b32_dpp v48, v40, v4 row_newbcast:4 row_mask:0xf bank_mask:0xf\n" \
  "v_and_b32_dpp v49, v40, v5 row_newbcast:4 row_mask:0xf bank_mask:0xf\n" \
  "v_mfma_f32_16x16x32_bf16 v[24:27], v[44:47], v[208:211], v[24:27]\n" \
  "v_and_b32_dpp v50, v40, v6 row_newbcast:4 row_mask:0xf bank_mask:0xf\n" \
  "v_and_b32_dpp v51, v40, v7 row_newbcast:4 row_mask:0xf bank_mask:0xf\n" \
  "v_readlane_b32 s71, v8, 63\n" \
  "v_mfma_f32_16x16x32_bf16 v[28:31], v[44:47], v[212:215], v[28:31]\n" \
  "v_and_b32_dpp v44, v40, v4 row_newbcast:8 row_mask:0xf bank_mask:0xf\n" \
  "v_and_b32_dpp v45, v40, v5 row_newbcast:8 row_mask:0xf bank_mask:0xf\n" \
  "v_mfma_f32_16x16x32_bf16 v[24:27], v[48:51], v[216:219], v[24:27]\n" \
  "v_and_b32_dpp v46, v40, v6 row_newbcast:8 row_mask:0xf bank_mask:0xf\n" \
  "v_and_b32_dpp v47, v40, v7 row_newbcast:8 row_mask:0xf bank_mask:0xf\n" \
  "v_mfma_f32_16x16x32_bf16 v[28:31], v[48:51], v[220:223], v[28:31]\n" \
  "v_and_b32_dpp v48, v40, v4 row_newbcast:12 row_mask:0xf bank_mask:0xf\n" \
  "v_and_b32_dpp v49, v40, v5 row_newbcast:12 row_mask:0xf bank_mask:0xf\n" \
  "v_mfma_f32_16x16x32_bf16 v[24:27], v[44:47], v[224:227], v[24:27]\n" \
  "v_and_b32_dpp v50, v40, v6 row_newbcast:12 row_mask:0xf bank_mask:0xf\n" \
  "v_and_b32_dpp v51, v40, v7 row_newbcast:12 row_mask:0xf bank_mask:0xf\n" \
  "v_mfma_f32_16x16x32_bf16 v[28:31], v[44:47], v[228:231], v[28:31]\n" \
  "v_and_b32_dpp v44, v41, v4 row_newbcast:0 row_mask:0xf bank_mask:0xf\n" \
  "v_and_b32_dpp v45, v41, v5 row_newbcast:0 row_mask:0xf bank_mask:0xf\n" \
  "v_mfma_f32_16x16x32_bf16 v[24:27], v[48:51], v[232:235], v[24:27]\n" \
  "s_add_u32 s33, s33, s27\n" \
  "buffer_load_dwordx4 v[64:67], v0, s[36:39], s4 offen nt\n" \
  "v_and_b32_dpp v46, v41, v6 row_newbcast:0 row_mask:0xf bank_mask:0xf\n" \
  "v_and_b32_dpp v47, v41, v7 row_newbcast:0 row_mask:0xf bank_mask:0xf\n" \
  "v_mfma_f32_16x16x32_bf16 v[28:31], v[48:51], v[236:239], v[28:31]\n" \
  "v_and_b32_dpp v48, v41, v4 row_newbcast:4 row_mask:0xf bank_mask:0xf\n" \
  "v_and_b32_dpp v49, v41, v5 row_newbcast:4 row_mask:0xf bank_mask:0xf\n" \
  "v_mfma_f32_16x16x32_bf16 v[24:27], v[44:47], v[240:243], v[24:27]\n" \
  "v_and_b32_dpp v50, v41, v6 row_newbcast:4 row_mask:0xf bank_mask:0xf\n" \
  "v_and_b32_dpp v51, v41, v7 row_newbcast:4 row_mask:0xf bank_mask:0xf\n" \
  "v_mfma_f32_16x16x32_bf16 v[28:31], v[44:47], v[244:247], v[28:31]\n" \
  "v_and_b32_dpp v44, v41, v4 row_newbcast:8 row_mask:0xf bank_mask:0xf\n" \
  "v_and_b32_dpp v45, v41, v5 row_newbcast:8 row_mask:0xf bank_mask:0xf\n" \
  "v_mfma_f32_16x16x32_bf16 v[24:27], v[48:51], v[248:251], v[24:27]\n" \
  "v_and_b32_dpp v46, v41, v6 row_newbcast:8 row_mask:0xf bank_mask:0xf\n" \
  "v_and_b32_dpp v47, v41, v7 row_newbcast:8 row_mask:0xf bank_mask:0xf\n" \
  "v_mfma_f32_16x16x32_bf16 v[28:31], v[48:51], v[252:255], v[28:31]\n" \
  "v_and_b32_dpp v48, v37, v4 row_newbcast:0 row_mask:0xf bank_mask:0xf\n" \
  "v_and_b32_dpp v49, v37, v5 row_newbcast:0 row_mask:0xf bank_mask:0xf\n" \
  "s_waitcnt lgkmcnt(0)\n" \
  "v_mfma_f32_16x16x32_bf16 v[24:27], v[44:47], v[168:171], v[24:27]\n" \
  "v_and_b32_dpp v50, v37, v6 row_newbcast:0 row_mask:0xf bank_mask:0xf\n" \
  "v_and_b32_dpp v51, v37, v7 row_newbcast:0 row_mask:0xf bank_mask:0xf\n" \
  "v_mfma_f32_16x16x32_bf16 v[28:31], v[44:47], v[172:175], v[28:31]\n" \
  "v_and_b32_dpp v44, v37, v4 row_newbcast:4 row_mask:0xf bank_mask:0xf\n" \
  "v_and_b32_dpp v45, v37, v5 row_newbcast:4 row_mask:0xf bank_mask:0xf\n" \
  "v_mfma_f32_16x16x32_bf16 v[24:27], v[48:51], v[104:107], v[24:27]\n" \
  "v_and_b32_dpp v46, v37, v6 row_newbcast:4 row_mask:0xf bank_mask:0xf\n" \
  "v_and_b32_dpp v47, v37, v7 row_newbcast:4 row_mask:0xf bank_mask:0xf\n" \
  "v_mfma_f32_16x16x32_bf16 v[28:31], v[48:51], v[108:111], v[28:31]\n" \
  "v_and_b32_dpp v48, v37, v4 row_newbcast:8 row_mask:0xf bank_mask:0xf\n" \
  "v_and_b32_dpp v49, v37, v5 row_newbcast:8 row_mask:0xf bank_mask:0xf\n" \
  "v_mfma_f32_16x16x32_bf16 v[24:27], v[44:47], v[112:115], v[24:27]\n" \
  "v_and_b32_dpp v50, v37, v6 row_newbcast:8 row_mask:0xf bank_mask:0xf\n" \
  "v_and_b32_dpp v51, v37, v7 row_newbcast:8 row_mask:0xf bank_mask:0xf\n" \
  "v_mfma_f32_16x16x32_bf16 v[28:31], v[44:47], v[116:119], v[28:31]\n" \
  "buffer_load_dwordx4 v[68:71], v0, s[36:39], s4 offen offset:256 nt\n" \
  "v_and_b32_dpp v44, v37, v4 row_newbcast:12 row_mask:0xf bank_mask:0xf\n" \
  "v_and_b32_dpp v45, v37, v5 row_newbcast:12 row_mask:0xf bank_mask:0xf\n" \
  "v_mfma_f32_16x16x32_bf16 v[24:27], v[48:51], v[120:123], v[24:27]\n" \
  "v_and_b32_dpp v46, v37, v6 row_newbcast:12 row_mask:0xf bank_mask:0xf\n" \
  "v_and_b32_dpp v47, v37, v7 row_newbcast:12 row_mask:0xf bank_mask:0xf\n" \
  "v_mfma_f32_16x16x32_bf16 v[28:31], v[48:51], v[124:127], v[28:31]\n" \
  "v_and_b32_dpp v48, v38, v4 row_newbcast:0 row_mask:0xf bank_mask:0xf\n" \
  "v_and_b32_dpp v49, v38, v5 row_newbcast:0 row_mask:0xf bank_mask:0xf\n" \
  "v_mfma_f32_16x16x32_bf16 v[24:27], v[44:47], v[128:131], v[24:27]\n" \
  "v_and_b32_dpp v50, v38, v6 row_newbcast:0 row_mask:0xf bank_mask:0xf\n" \
  "v_and_b32_dpp v51, v38, v7 row_newbcast:0 row_mask:0xf bank_mask:0xf\n" \
  "v_mfma_f32_16x16x32_bf16 v[28:31], v[44:47], v[132:135], v[28:31]\n" \
  "v_and_b32_dpp v44, v38, v4 row_newbcast:4 row_mask:0xf bank_mask:0xf\n" \
  "v_and_b32_dpp v45, v38, v5 row_newbcast:4 row_mask:0xf bank_mask:0xf\n" \
  "v_mfma_f32_16x16x32_bf16 v[24:27], v[48:51], v[136:139], v[24:27]\n" \
  "v_and_b32_dpp v46, v38, v6 row_newbcast:4 row_mask:0xf bank_mask:0xf\n" \
  "v_and_b32_dpp v47, v38, v7 row_newbcast:4 row_mask:0xf bank_mask:0xf\n" \
  "v_mfma_f32_16x16x32_bf16 v[28:31], v[48:51], v[140:143], v[28:31]\n" \
  "v_and_b32_dpp v48, v38, v4 row_newbcast:8 row_mask:0xf bank_mask:0xf\n" \
  "v_and_b32_dpp v49, v38, v5 row_newbcast:8 row_mask:0xf bank_mask:0xf\n" \
  "v_mfma_f32_16x16x32_bf16 v[24:27], v[44:47], v[144:147], v[24:27]\n" \
  "v_and_b32_dpp v50, v38, v6 row_newbcast:8 row_mask:0xf bank_mask:0xf\n" \
  "v_and_b32_dpp v51, v38, v7 row_newbcast:8 row_mask:0xf bank_mask:0xf\n" \
  "v_mfma_f32_16x16x32_bf16 v[28:31], v[44:47], v[148:151], v[28:31]\n" \
  "v_and_b32_dpp v44, v38, v4 row_newbcast:12 row_mask:0xf bank_mask:0xf\n" \
  "v_and_b32_dpp v45, v38, v5 row_newbcast:12 row_mask:0xf bank_mask:0xf\n" \
  "v_mfma_f32_16x16x32_bf16 v[24:27], v[48:51], v[152:155], v[24:27]\n" \
  "v_and_b32_dpp v46, v38, v6 row_newbcast:12 row_mask:0xf bank_mask:0xf\n" \
  "v_and_b32_dpp v47, v38, v7 row_newbcast:12 row_mask:0xf bank_mask:0xf\n" \
  "v_mfma_f32_16x16x32_bf16 v[28:31], v[48:51], v[156:159], v[28:31]\n" \
  "s_nop 0\n" \
  "v_mfma_f32_16x16x32_bf16 v[24:27], v[44:47], v[160:163], v[24:27]\n" \
  "v_mfma_f32_16x16x32_bf16 v[28:31], v[44:47], v[164:167], v[28:31]\n" \
  "s_waitcnt vmcnt(13)\n" \
  "s_add_u32 s33, s33, s27\n" \
  "buffer_load_dwordx4 v[72:75], v0, s[36:39], s5 offen nt\n" \
  "s_mov_b32 s81, 155776\n" \
  "s_mov_b32 s82, s79\n" \
  "s_add_u32 s79, s79, 143360\n" \
  "s_add_u32 s59, s87, 155776\n" \
  "s_sub_u32 s85, 155776, s67\n" \
  "v_add_u32 v42, 155776, v3\n" \
  "v_and_b32 v43, 0x30, v2\n" \
  "v_add_u32 v63, s85, v2\n" \
  "v_mov_b32 v52, 1\n" \
  "v_add_u32 v43, 155776, v43\n" \
  "v_mov_b32 v48, s69\n" \
  "v_mov_b32 v49, s71\n" \
  "v_mov_b32 v50, s59\n" \
  "v_mov_b32 v51, s81\n" \
  "v_cndmask_b32 v44, v24, v28, s[88:89]\n" \
  "v_cndmask_b32 v45, v25, v29, s[88:89]\n" \
  "v_cndmask_b32 v46, v26, v30, s[88:89]\n" \
  "v_cndmask_b32 v47, v27, v31, s[88:89]\n" \
  "s_call_b64 s[94:95], .LA_MERGE\n" \
  "s_branch .LA_END_A\n" \
  ".LA_DEFER_A:\n" \
  "v_and_b32_dpp v44, v35, v4 row_newbcast:0 row_mask:0xf bank_mask:0xf\n" \
  "v_and_b32_dpp v45, v35, v5 row_newbcast:0 row_mask:0xf bank_mask:0xf\n" \
  "v_and_b32_dpp v46, v35, v6 row_newbcast:0 row_mask:0xf bank_mask:0xf\n" \
  "v_and_b32_dpp v47, v35, v7 row_newbcast:0 row_mask:0xf bank_mask:0xf\n" \
  "s_waitcnt vmcnt(13)\n" \
  "s_nop 0\n" \
  "v_mfma_f32_16x16x32_bf16 v[16:19], v[44:47], a[176:179], v[16:19]\n" \
  "s_waitcnt vmcnt(12)\n" \
  "v_mfma_f32_16x16x32_bf16 v[20:23], v[44:47], a[180:183], v[20:23]\n" \
  "v_and_b32 v42, 0xc0, v0\n" \
  "v_and_b32 v43, 0xf0, v0\n" \
  "v_add_u32 v42, v42, v43\n" \
  "buffer_load_dwordx4 v[8:11], v42, s[52:55], s76 offen sc1\n" \
  "buffer_load_dwordx4 v[12:15], v42, s[52:55], s76 offen offset:512 sc1\n" \
  "v_and_b32_dpp v48, v35, v4 row_newbcast:4 row_mask:0xf bank_mask:0xf\n" \
  "v_and_b32_dpp v49, v35, v5 row_newbcast:4 row_mask:0xf bank_mask:0xf\n" \
  "v_and_b32_dpp v50, v35, v6 row_newbcast:4 row_mask:0xf bank_mask:0xf\n" \
  "v_and_b32_dpp v51, v35, v7 row_newbcast:4 row_mask:0xf bank_mask:0xf\n" \
  "s_waitcnt vmcnt(13)\n" \
  "s_nop 0\n" \
  "v_mfma_f32_16x16x32_bf16 v[16:19], v[48:51], a[184:187], v[16:19]\n" \
  "s_waitcnt vmcnt(12)\n" \
  "v_mfma_f32_16x16x32_bf16 v[20:23], v[48:51], a[188:191], v[20:23]\n" \
  "v_and_b32_dpp v44, v35, v4 row_newbcast:8 row_mask:0xf bank_mask:0xf\n" \
  "v_and_b32_dpp v45, v35, v5 row_newbcast:8 row_mask:0xf bank_mask:0xf\n" \
  "v_and_b32_dpp v46, v35, v6 row_newbcast:8 row_mask:0xf bank_mask:0xf\n" \
  "v_and_b32_dpp v47, v35, v7 row_newbcast:8 row_mask:0xf bank_mask:0xf\n" \
  "s_waitcnt vmcnt(11)\n" \
  "s_nop 0\n" \
  "v_mfma_f32_16x16x32_bf16 v[16:19], v[44:47], a[192:195], v[16:19]\n" \
  "s_waitcnt vmcnt(10)\n" \
  "v_mfma_f32_16x16x32_bf16 v[20:23], v[44:47], a[196:199], v[20:23]\n" \
  "v_and_b32_dpp v48, v35, v4 row_newbcast:12 row_mask:0xf bank_mask:0xf\n" \
  "v_and_b32_dpp v49, v35, v5 row_newbcast:12 row_mask:0xf bank_mask:0xf\n" \
  "v_and_b32_dpp v50, v35, v6 row_newbcast:12 row_mask:0xf bank_mask:0xf\n" \
  "v_and_b32_dpp v51, v35, v7 row_newbcast:12 row_mask:0xf bank_mask:0xf\n" \
  "s_waitcnt vmcnt(7)\n" \
  "s_nop 0\n" \
  "v_mfma_f32_16x16x32_bf16 v[16:19], v[48:51], a[200:203], v[16:19]\n" \
  "s_waitcnt vmcnt(6)\n" \
  "v_mfma_f32_16x16x32_bf16 v[20:23], v[48:51], a[204:207], v[20:23]\n" \
  "v_and_b32_dpp v44, v36, v4 row_newbcast:0 row_mask:0xf bank_mask:0xf\n" \
  "v_and_b32_dpp v45, v36, v5 row_newbcast:0 row_mask:0xf bank_mask:0xf\n" \
  "v_and_b32_dpp v46, v36, v6 row_newbcast:0 row_mask:0xf bank_mask:0xf\n" \
  "v_and_b32_dpp v47, v36, v7 row_newbcast:0 row_mask:0xf bank_mask:0xf\n" \
  "s_waitcnt vmcnt(5)\n" \
  "s_nop 0\n" \
  "v_mfma_f32_16x16x32_bf16 v[16:19], v[44:47], a[208:211], v[16:19]\n" \
  "s_waitcnt vmcnt(4)\n" \
  "v_mfma_f32_16x16x32_bf16 v[20:23], v[44:47], a[212:215], v[20:23]\n" \
  "v_and_b32_dpp v48, v36, v4 row_newbcast:4 row_mask:0xf bank_mask:0xf\n" \
  "v_and_b32_dpp v49, v36, v5 row_newbcast:4 row_mask:0xf bank_mask:0xf\n" \
  "v_and_b32_dpp v50, v36, v6 row_newbcast:4 row_mask:0xf bank_mask:0xf\n" \
  "v_and_b32_dpp v51, v36, v7 row_newbcast:4 row_mask:0xf bank_mask:0xf\n" \
  "s_waitcnt vmcnt(3)\n" \
  "s_nop 0\n" \
  "v_mfma_f32_16x16x32_bf16 v[16:19], v[48:51], a[216:219], v[16:19]\n" \
  "s_waitcnt vmcnt(2)\n" \
  "v_mfma_f32_16x16x32_bf16 v[20:23], v[48:51], a[220:223], v[20:23]\n" \
  "s_mov_b32 s81, 151552\n" \
  "s_mov_b32 s82, s77\n" \
  "s_add_u32 s77, s77, 143360\n" \
  "s_add_u32 s59, s87, 151552\n" \
  "s_sub_u32 s85, 151552, s67\n" \
  "v_add_u32 v42, 151552, v3\n" \
  "v_and_b32 v43, 0x30, v2\n" \
  "v_add_u32 v63, s85, v2\n" \
  "v_mov_b32 v52, 1\n" \
  "v_add_u32 v43, 151552, v43\n" \
  "v_mov_b32 v48, s68\n" \
  "v_mov_b32 v49, s70\n" \
  "v_mov_b32 v50, s59\n" \
  "v_mov_b32 v51, s81\n" \
  "v_cndmask_b32 v44, v16, v20, s[88:89]\n" \
  "v_cndmask_b32 v45, v17, v21, s[88:89]\n" \
  "v_cndmask_b32 v46, v18, v22, s[88:89]\n" \
  "v_cndmask_b32 v47, v19, v23, s[88:89]\n" \
  "s_call_b64 s[94:95], .LA_MERGE\n" \
  "s_waitcnt vmcnt(2)\n" \
  "s_mov_b32 s86, s76\n" \
  "s_mov_b32 s56, s74\n" \
  "s_call_b64 s[94:95], .LA_QWAIT\n" \
  "s_memrealtime s[30:31]\n" \
  "s_waitcnt lgkmcnt(0)\n" \
  "s_lshl_b32 s30, s30, 6\n" \
  "s_sub_u32 s30, s30, 1920\n" \
  "s_mov_b32 s33, s30\n" \
  "s_add_u32 s76, s76, 16384\n" \
  "v_mfma_f32_16x16x32_bf16 v[44:47], a[224:227], v[8:11], 0\n" \
  "v_add_u32 v43, s80, v2\n" \
  "v_mov_b32 v41, 0xff800000\n" \
  "v_mfma_f32_16x16x32_bf16 v[44:47], a[228:231], v[12:15], v[44:47]\n" \
  "ds_read_b128 v[56:59], v43\n" \
  "ds_read_b128 v[60:63], v43 offset:1024\n" \
  "v_mov_b32 v42, s64\n" \
  "v_mfma_f32_16x16x32_bf16 v[48:51], a[232:235], v[8:11], 0\n" \
  "v_mfma_f32_16x16x32_bf16 v[48:51], a[236:239], v[12:15], v[48:51]\n" \
  "v_mov_b32_dpp v41, v42 quad_perm:[0,1,2,3] row_mask:0xf bank_mask:0x4\n" \
  "v_mfma_f32_16x16x32_bf16 v[52:55], a[240:243], v[8:11], 0\n" \
  "s_nop 0\n" \
  "v_mov_b32_dpp v37, v44 quad_perm:[0,0,0,0] row_mask:0xf bank_mask:0x1\n" \
  "v_mov_b32_dpp v42, v45 quad_perm:[1,1,1,1] row_mask:0xf bank_mask:0x1\n" \
  "v_mfma_f32_16x16x32_bf16 v[52:55], a[244:247], v[12:15], v[52:55]\n" \
  "v_add_f32_dpp v37, v46, v37 quad_perm:[2,2,2,2] row_mask:0xf bank_mask:0x1\n" \
  "v_add_f32_dpp v42, v47, v42 quad_perm:[3,3,3,3] row_mask:0xf bank_mask:0x1\n" \
  "v_mfma_f32_16x16x32_bf16 v[44:47], a[248:251], v[8:11], 0\n" \
  "v_mov_b32_dpp v37, v48 quad_perm:[0,0,0,0] row_mask:0xf bank_mask:0x2\n" \
  "v_mov_b32_dpp v42, v49 quad_perm:[1,1,1,1] row_mask:0xf bank_mask:0x2\n" \
  "v_mfma_f32_16x16x32_bf16 v[44:47], a[252:255], v[12:15], v[44:47]\n" \
  "v_add_f32_dpp v37, v50, v37 quad_perm:[2,2,2,2] row_mask:0xf bank_mask:0x2\n" \
  "v_add_f32_dpp v42, v51, v42 quad_perm:[3,3,3,3] row_mask:0xf bank_mask:0x2\n" \
  "v_mfma_f32_16x16x32_bf16 v[48:51], v[64:67], v[8:11], 0\n" \
  "v_mov_b32_dpp v37, v52 quad_perm:[0,0,0,0] row_mask:0xf bank_mask:0x4\n" \
  "v_mov_b32_dpp v42, v53 quad_perm:[1,1,1,1] row_mask:0xf bank_mask:0x4\n" \
  "v_mfma_f32_16x16x32_bf16 v[48:51], v[68:71], v[12:15], v[48:51]\n" \
  "v_add_f32_dpp v37, v54, v37 quad_perm:[2,2,2,2] row_mask:0xf bank_mask:0x4\n" \
  "v_add_f32_dpp v42, v55, v42 quad_perm:[3,3,3,3] row_mask:0xf bank_mask:0x4\n" \
  "v_mfma_f32_16x16x32_bf16 v[52:55], v[72:75], v[8:11], 0\n" \
  "v_mov_b32_dpp v37, v44 quad_perm:[0,0,0,0] row_mask:0xf bank_mask:0x8\n" \
  "v_mov_b32_dpp v42, v45 quad_perm:[1,1,1,1] row_mask:0xf bank_mask:0x8\n" \
  "v_mfma_f32_16x16x32_bf16 v[52:55], v[76:79], v[12:15], v[52:55]\n" \
  "v_add_f32_dpp v37, v46, v37 quad_perm:[2,2,2,2] row_mask:0xf bank_mask:0x8\n" \
  "v_add_f32_dpp v42, v47, v42 quad_perm:[3,3,3,3] row_mask:0xf bank_mask:0x8\n" \
  "v_mfma_f32_16x16x32_bf16 v[44:47], v[80:83], v[8:11], 0\n" \
  "v_mov_b32_dpp v38, v48 quad_perm:[0,0,0,0] row_mask:0xf bank_mask:0x1\n" \
  "v_mov_b32_dpp v43, v49 quad_perm:[1,1,1,1] row_mask:0xf bank_mask:0x1\n" \
  "v_mfma_f32_16x16x32_bf16 v[44:47], v[84:87], v[12:15], v[44:47]\n" \
  "v_add_f32_dpp v38, v50, v38 quad_perm:[2,2,2,2] row_mask:0xf bank_mask:0x1\n" \
  "v_add_f32_dpp v43, v51, v43 quad_perm:[3,3,3,3] row_mask:0xf bank_mask:0x1\n" \
  "v_add_f32 v37, v37, v42\n" \
  "v_mfma_f32_16x16x32_bf16 v[48:51], v[88:91], v[8:11], 0\n" \
  "v_mov_b32_dpp v38, v52 quad_perm:[0,0,0,0] row_mask:0xf bank_mask:0x2\n" \
  "v_mov_b32_dpp v43, v53 quad_perm:[1,1,1,1] row_mask:0xf bank_mask:0x2\n" \
  "v_mfma_f32_16x16x32_bf16 v[48:51], v[92:95], v[12:15], v[48:51]\n" \
  "v_add_f32_dpp v38, v54, v38 quad_perm:[2,2,2,2] row_mask:0xf bank_mask:0x2\n" \
  "v_add_f32_dpp v43, v55, v43 quad_perm:[3,3,3,3] row_mask:0xf bank_mask:0x2\n" \
  "v_mfma_f32_16x16x32_bf16 v[52:55], v[96:99], v[8:11], 0\n" \
  "v_mov_b32_dpp v38, v44 quad_perm:[0,0,0,0] row_mask:0xf bank_mask:0x4\n" \
  "v_mov_b32_dpp v43, v45 quad_perm:[1,1,1,1] row_mask:0xf bank_mask:0x4\n" \
  "v_mfma_f32_16x16x32_bf16 v[52:55], v[100:103], v[12:15], v[52:55]\n" \
  "v_add_f32_dpp v38, v46, v38 quad_perm:[2,2,2,2] row_mask:0xf bank_mask:0x4\n" \
  "v_add_f32_dpp v43, v47, v43 quad_perm:[3,3,3,3] row_mask:0xf bank_mask:0x4\n" \
  "v_mfma_f32_16x16x32_bf16 v[44:47], v[104:107], v[8:11], 0\n" \
  "v_mov_b32_dpp v38, v48 quad_perm:[0,0,0,0] row_mask:0xf bank_mask:0x8\n" \
  "v_mov_b32_dpp v43, v49 quad_perm:[1,1,1,1] row_mask:0xf bank_mask:0x8\n" \
  "v_mfma_f32_16x16x32_bf16 v[44:47], v[108:111], v[12:15], v[44:47]\n" \
  "v_add_f32_dpp v38, v50, v38 quad_perm:[2,2,2,2] row_mask:0xf bank_mask:0x8\n" \
  "v_add_f32_dpp v43, v51, v43 quad_perm:[3,3,3,3] row_mask:0xf bank_mask:0x8\n" \
  "v_mfma_f32_16x16x32_bf16 v[48:51], v[112:115], v[8:11], 0\n" \
  "v_mov_b32_dpp v39, v52 quad_perm:[0,0,0,0] row_mask:0xf bank_mask:0x1\n" \
  "v_mov_b32_dpp v42, v53 quad_perm:[1,1,1,1] row_mask:0xf bank_mask:0x1\n" \
  "v_mfma_f32_16x16x32_bf16 v[48:51], v[116:119], v[12:15], v[48:51]\n" \
  "v_add_f32_dpp v39, v54, v39 quad_perm:[2,2,2,2] row_mask:0xf bank_mask:0x1\n" \
  "v_add_f32_dpp v42, v55, v42 quad_perm:[3,3,3,3] row_mask:0xf bank_mask:0x1\n" \
  "v_add_f32 v38, v38, v43\n" \
  "v_mfma_f32_16x16x32_bf16 v[52:55], v[120:123], v[8:11], 0\n" \
  "v_mov_b32_dpp v39, v44 quad_perm:[0,0,0,0] row_mask:0xf bank_mask:0x2\n" \
  "v_mov_b32_dpp v42, v45 quad_perm:[1,1,1,1] row_mask:0xf bank_mask:0x2\n" \
  "v_mfma_f32_16x16x32_bf16 v[52:55], v[124:127], v[12:15], v[52:55]\n" \
  "v_add_f32_dpp v39, v46, v39 quad_perm:[2,2,2,2] row_mask:0xf bank_mask:0x2\n" \
  "v_add_f32_dpp v42, v47, v42 quad_perm:[3,3,3,3] row_mask:0xf bank_mask:0x2\n" \
  "v_mfma_f32_16x16x32_bf16 v[44:47], v[128:131], v[8:11], 0\n" \
  "v_mov_b32_dpp v39, v48 quad_perm:[0,0,0,0] row_mask:0xf bank_mask:0x4\n" \
  "v_mov_b32_dpp v42, v49 quad_perm:[1,1,1,1] row_mask:0xf bank_mask:0x4\n" \
  "v_mfma_f32_16x16x32_bf16 v[44:47], v[132:135], v[12:15], v[44:47]\n" \
  "v_add_f32_dpp v39, v50, v39 quad_perm:[2,2,2,2] row_mask:0xf bank_mask:0x4\n" \
  "v_add_f32_dpp v42, v51, v42 quad_perm:[3,3,3,3] row_mask:0xf bank_mask:0x4\n" \
  "v_mfma_f32_16x16x32_bf16 v[48:51], v[136:139], v[8:11], 0\n" \
  "v_mov_b32_dpp v39, v52 quad_perm:[0,0,0,0] row_mask:0xf bank_mask:0x8\n" \
  "v_mov_b32_dpp v42, v53 quad_perm:[1,1,1,1] row_mask:0xf bank_mask:0x8\n" \
  "v_mfma_f32_16x16x32_bf16 v[48:51], v[140:143], v[12:15], v[48:51]\n" \
  "v_add_f32_dpp v39, v54, v39 quad_perm:[2,2,2,2] row_mask:0xf bank_mask:0x8\n" \
  "v_add_f32_dpp v42, v55, v42 quad_perm:[3,3,3,3] row_mask:0xf bank_mask:0x8\n" \
  "v_mfma_f32_16x16x32_bf16 v[52:55], v[144:147], v[8:11], 0\n" \
  "v_mov_b32_dpp v40, v44 quad_perm:[0,0,0,0] row_mask:0xf bank_mask:0x1\n" \
  "v_mov_b32_dpp v43, v45 quad_perm:[1,1,1,1] row_mask:0xf bank_mask:0x1\n" \
  "v_mfma_f32_16x16x32_bf16 v[52:55], v[148:151], v[12:15], v[52:55]\n" \
  "v_add_f32_dpp v40, v46, v40 quad_perm:[2,2,2,2] row_mask:0xf bank_mask:0x1\n" \
  "v_add_f32_dpp v43, v47, v43 quad_perm:[3,3,3,3] row_mask:0xf bank_mask:0x1\n" \
  "v_add_f32 v39, v39, v42\n" \
  "v_mfma_f32_16x16x32_bf16 v[44:47], v[152:155], v[8:11], 0\n" \
  "v_mov_b32_dpp v40, v48 quad_perm:[0,0,0,0] row_mask:0xf bank_mask:0x2\n" \
  "v_mov_b32_dpp v43, v49 quad_perm:[1,1,1,1] row_mask:0xf bank_mask:0x2\n" \
  "v_mfma_f32_16x16x32_bf16 v[44:47], v[156:159], v[12:15], v[44:47]\n" \
  "v_add_f32_dpp v40, v50, v40 quad_perm:[2,2,2,2] row_mask:0xf bank_mask:0x2\n" \
  "v_add_f32_dpp v43, v51, v43 quad_perm:[3,3,3,3] row_mask:0xf bank_mask:0x2\n" \
  "v_mfma_f32_16x16x32_bf16 v[48:51], v[160:163], v[8:11], 0\n" \
  "v_mov_b32_dpp v40, v52 quad_perm:[0,0,0,0] row_mask:0xf bank_mask:0x4\n" \
  "v_mov_b32_dpp v43, v53 quad_perm:[1,1,1,1] row_mask:0xf bank_mask:0x4\n" \
  "v_mfma_f32_16x16x32_bf16 v[48:51], v[164:167], v[12:15], v[48:51]\n" \
  "v_add_f32_dpp v40, v54, v40 quad_perm:[2,2,2,2] row_mask:0xf bank_mask:0x4\n" \
  "v_add_f32_dpp v43, v55, v43 quad_perm:[3,3,3,3] row_mask:0xf bank_mask:0x4\n" \
  "v_mfma_f32_16x16x32_bf16 v[52:55], v[168:171], v[8:11], 0\n" \
  "v_mov_b32_dpp v40, v44 quad_perm:[0,0,0,0] row_mask:0xf bank_mask:0x8\n" \
  "v_mov_b32_dpp v43, v45 quad_perm:[1,1,1,1] row_mask:0xf bank_mask:0x8\n" \
  "v_mfma_f32_16x16x32_bf16 v[52:55], v[172:175], v[12:15], v[52:55]\n" \
  "v_add_f32_dpp v40, v46, v40 quad_perm:[2,2,2,2] row_mask:0xf bank_mask:0x8\n" \
  "v_add_f32_dpp v43, v47, v43 quad_perm:[3,3,3,3] row_mask:0xf bank_mask:0x8\n" \
  "s_waitcnt lgkmcnt(0)\n" \
  "v_mfma_f32_16x16x32_bf16 v[44:47], v[56:59], v[8:11], 0\n" \
  "v_mov_b32_dpp v41, v48 quad_perm:[0,0,0,0] row_mask:0xf bank_mask:0x1\n" \
  "v_mov_b32_dpp v42, v49 quad_perm:[1,1,1,1] row_mask:0xf bank_mask:0x1\n" \
  "v_mfma_f32_16x16x32_bf16 v[44:47], v[60:63], v[12:15], v[44:47]\n" \
  "v_add_f32_dpp v41, v50, v41 quad_perm:[2,2,2,2] row_mask:0xf bank_mask:0x1\n" \
  "v_add_f32_dpp v42, v51, v42 quad_perm:[3,3,3,3] row_mask:0xf bank_mask:0x1\n" \
  "v_add_f32 v40, v40, v43\n" \
  "v_mov_b32_dpp v41, v52 quad_perm:[0,0,0,0] row_mask:0xf bank_mask:0x2\n" \
  "v_mov_b32_dpp v42, v53 quad_perm:[1,1,1,1] row_mask:0xf bank_mask:0x2\n" \
  "s_nop 0\n" \
  "v_add_f32_dpp v41, v54, v41 quad_perm:[2,2,2,2] row_mask:0xf bank_mask:0x2\n" \
  "v_add_f32_dpp v42, v55, v42 quad_perm:[3,3,3,3] row_mask:0xf bank_mask:0x2\n" \
  "s_nop 0\n" \
  "v_add_f32_dpp v41, v44, v41 quad_perm:[0,0,0,0] row_mask:0xf bank_mask:0x4\n" \
  "v_mov_b32_dpp v42, v45 quad_perm:[1,1,1,1] row_mask:0xf bank_mask:0x4\n" \
  "s_nop 0\n" \
  "v_add_f32_dpp v41, v46, v41 quad_perm:[2,2,2,2] row_mask:0xf bank_mask:0x4\n" \
  "v_add_f32_dpp v42, v47, v42 quad_perm:[3,3,3,3] row_mask:0xf bank_mask:0x4\n" \
  "v_add_f32 v41, v41, v42\n" \
  "s_add_u32 s36, s36, 0x2000000\n" \
  "s_addc_u32 s37, s37, 0\n" \
  "s_mov_b64 s[44:45], s[36:37]\n" \
  "s_mov_b32 s46, s62\n" \
  "s_add_u32 m0, s66, 0\n" \
  "s_nop 0\n" \
  "buffer_load_dwordx4 v0, s[44:47], s58 offen nt lds\n" \
  "s_add_u32 m0, s66, 768\n" \
  "s_nop 0\n" \
  "buffer_load_dwordx4 v0, s[44:47], s58 offen offset:256 nt lds\n" \
  "buffer_load_dwordx4 a[224:227], v0, s[36:39], s0 offen nt\n" \
  "buffer_load_dwordx4 a[228:231], v0, s[36:39], s0 offen offset:256 nt\n" \
  "buffer_load_dwordx4 a[232:235], v0, s[36:39], s1 offen nt\n" \
  "buffer_load_dwordx4 a[236:239], v0, s[36:39], s1 offen offset:256 nt\n" \
  "buffer_load_dwordx4 a[240:243], v0, s[36:39], s2 offen nt\n" \
  "buffer_load_dwordx4 a[244:247], v0, s[36:39], s2 offen offset:256 nt\n" \
  "buffer_load_dwordx4 a[248:251], v0, s[36:39], s3 offen nt\n" \
  "buffer_load_dwordx4 a[252:255], v0, s[36:39], s3 offen offset:256 nt\n" \
  "buffer_load_dwordx4 v[64:67], v0, s[36:39], s4 offen nt\n" \
  "buffer_load_dwordx4 v[68:71], v0, s[36:39], s4 offen offset:256 nt\n" \
  "buffer_load_dwordx4 v[72:75], v0, s[36:39], s5 offen nt\n" \
  "s_add_u32 s33, s33, 9960\n" \
  "v_add_u32 v43, s80, v2\n" \
  "v_max3_f32 v42, v37, v38, v39\n" \
  "v_max3_f32 v42, v42, v40, v41\n" \
  "ds_read_b128 v[168:171], v43 offset:2048\n" \
  "ds_read_b128 v[172:175], v43 offset:3072\n" \
  "v_max_f32_dpp v42, v42, v42 row_ror:4 row_mask:0xf bank_mask:0xf\n" \
  "ds_read_b128 v[104:107], v2 offset:16384\n" \
  "ds_read_b128 v[108:111], v2 offset:17408\n" \
  "v_max_f32_dpp v42, v42, v42 row_ror:8 row_mask:0xf bank_mask:0xf\n" \
  "ds_read_b128 v[112:115], v2 offset:18432\n" \
  "ds_read_b128 v[116:119], v2 offset:19456\n" \
  "v_max_f32_dpp v42, v42, v42 row_bcast:15 row_mask:0xa bank_mask:0xf\n" \
  "ds_read_b128 v[120:123], v2 offset:20480\n" \
  "ds_read_b128 v[124:127], v2 offset:21504\n" \
  "v_max_f32_dpp v42, v42, v42 row_bcast:31 row_mask:0xc bank_mask:0xf\n" \
  "ds_read_b128 v[128:131], v2 offset:22528\n" \
  "ds_read_b128 v[132:135], v2 offset:23552\n" \
  "v_readlane_b32 s69, v42, 63\n" \
  "ds_read_b128 v[136:139], v2 offset:24576\n" \
  "ds_read_b128 v[140:143], v2 offset:25600\n" \
  "v_subrev_f32 v37, s69, v37\n" \
  "v_subrev_f32 v38, s69, v38\n" \
  "v_subrev_f32 v39, s69, v39\n" \
  "v_subrev_f32 v40, s69, v40\n" \
  "v_subrev_f32 v41, s69, v41\n" \
  "v_exp_f32 v37, v37\n" \
  "v_exp_f32 v38, v38\n" \
  "v_exp_f32 v39, v39\n" \
  "v_exp_f32 v40, v40\n" \
  "v_exp_f32 v41, v41\n" \
  "v_add_f32 v8, v37, v38\n" \
  "v_add_f32 v9, v39, v40\n" \
  "v_cvt_pk_bf16_f32 v37, v37, v37\n" \
  "v_cvt_pk_bf16_f32 v38, v38, v38\n" \
  "v_cvt_pk_bf16_f32 v39, v39, v39\n" \
  "v_cvt_pk_bf16_f32 v40, v40, v40\n" \
  "v_add_f32 v9, v9, v41\n" \
  "v_add_f32 v8, v8, v9\n" \
  "v_cvt_pk_bf16_f32 v41, v41, v41\n" \
  "v_and_b32_dpp v44, v39, v4 row_newbcast:0 row_mask:0xf bank_mask:0xf\n" \
  "v_and_b32_dpp v45, v39, v5 row_newbcast:0 row_mask:0xf bank_mask:0xf\n" \
  "v_and_b32_dpp v46, v39, v6 row_newbcast:0 row_mask:0xf bank_mask:0xf\n" \
  "v_and_b32_dpp v47, v39, v7 row_newbcast:0 row_mask:0xf bank_mask:0xf\n" \
  "v_and_b32_dpp v48, v39, v4 row_newbcast:4 row_mask:0xf bank_mask:0xf\n" \
  "v_and_b32_dpp v49, v39, v5 row_newbcast:4 row_mask:0xf bank_mask:0xf\n" \
  "v_mfma_f32_16x16x32_bf16 v[24:27], v[44:47], v[176:179], 0\n" \
  "v_and_b32_dpp v50, v39, v6 row_newbcast:4 row_mask:0xf bank_mask:0xf\n" \
  "v_and_b32_dpp v51, v39, v7 row_newbcast:4 row_mask:0xf bank_mask:0xf\n" \
  "ds_read_b128 v[144:147], v2 offset:26624\n" \
  "v_mfma_f32_16x16x32_bf16 v[28:31], v[44:47], v[180:183], 0\n" \
  "v_and_b32_dpp v44, v39, v4 row_newbcast:8 row_mask:0xf bank_mask:0xf\n" \
  "v_and_b32_dpp v45, v39, v5 row_newbcast:8 row_mask:0xf bank_mask:0xf\n" \
  "ds_read_b128 v[148:151], v2 offset:27648\n" \
  "v_mfma_f32_16x16x32_bf16 v[24:27], v[48:51], v[184:187], v[24:27]\n" \
  "v_and_b32_dpp v46, v39, v6 row_newbcast:8 row_mask:0xf bank_mask:0xf\n" \
  "v_and_b32_dpp v47, v39, v7 row_newbcast:8 row_mask:0xf bank_mask:0xf\n" \
  "ds_read_b128 v[152:155], v2 offset:28672\n" \
  "v_mfma_f32_16x16x32_bf16 v[28:31], v[48:51], v[188:191], v[28:31]\n" \
  "v_and_b32_dpp v48, v39, v4 row_newbcast:12 row_mask:0xf bank_mask:0xf\n" \
  "v_and_b32_dpp v49, v39, v5 row_newbcast:12 row_mask:0xf bank_mask:0xf\n" \
  "ds_read_b128 v[156:159], v2 offset:29696\n" \
  "v_mfma_f32_16x16x32_bf16 v[24:27], v[44:47], v[192:195], v[24:27]\n" \
  "v_and_b32_dpp v50, v39, v6 row_newbcast:12 row_mask:0xf bank_mask:0xf\n" \
  "v_and_b32_dpp v51, v39, v7 row_newbcast:12 row_mask:0xf bank_mask:0xf\n" \
  "ds_read_b128 v[160:163], v2 offset:30720\n" \
  "v_mfma_f32_16x16x32_bf16 v[28:31], v[44:47], v[196:199], v[28:31]\n" \
  "v_and_b32_dpp v44, v40, v4 row_newbcast:0 row_mask:0xf bank_mask:0xf\n" \
  "v_and_b32_dpp v45, v40, v5 row_newbcast:0 row_mask:0xf bank_mask:0xf\n" \
  "ds_read_b128 v[164:167], v2 offset:31744\n" \
  "v_mfma_f32_16x16x32_bf16 v[24:27], v[48:51], v[200:203], v[24:27]\n" \
  "v_and_b32_dpp v46, v40, v6 row_newbcast:0 row_mask:0xf bank_mask:0xf\n" \
  "v_and_b32_dpp v47, v40, v7 row_newbcast:0 row_mask:0xf bank_mask:0xf\n" \
  "v_add_f32_dpp v8, v8, v8 row_ror:4 row_mask:0xf bank_mask:0xf\n" \
  "v_mfma_f32_16x16x32_bf16 v[28:31], v[48:51], v[204:207], v[28:31]\n" \
  "v_and_b32_dpp v48, v40, v4 row_newbcast:4 row_mask:0xf bank_mask:0xf\n" \
  "v_and_b32_dpp v49, v40, v5 row_newbcast:4 row_mask:0xf bank_mask:0xf\n" \
  "v_mfma_f32_16x16x32_bf16 v[24:27], v[44:47], v[208:211], v[24:27]\n" \
  "v_and_b32_dpp v50, v40, v6 row_newbcast:4 row_mask:0xf bank_mask:0xf\n" \
  "v_and_b32_dpp v51, v40, v7 row_newbcast:4 row_mask:0xf bank_mask:0xf\n" \
  "v_add_f32_dpp v8, v8, v8 row_ror:8 row_mask:0xf bank_mask:0xf\n" \
  "v_mfma_f32_16x16x32_bf16 v[28:31], v[44:47], v[212:215], v[28:31]\n" \
  "v_and_b32_dpp v44, v40, v4 row_newbcast:8 row_mask:0xf bank_mask:0xf\n" \
  "v_and_b32_dpp v45, v40, v5 row_newbcast:8 row_mask:0xf bank_mask:0xf\n" \
  "v_mfma_f32_16x16x32_bf16 v[24:27], v[48:51], v[216:219], v[24:27]\n" \
  "v_and_b32_dpp v46, v40, v6 row_newbcast:8 row_mask:0xf bank_mask:0xf\n" \
  "v_and_b32_dpp v47, v40, v7 row_newbcast:8 row_mask:0xf bank_mask:0xf\n" \
  "v_add_f32_dpp v8, v8, v8 row_bcast:15 row_mask:0xa bank_mask:0xf\n" \
  "v_mfma_f32_16x16x32_bf16 v[28:31], v[48:51], v[220:223], v[28:31]\n" \
  "v_and_b32_dpp v48, v40, v4 row_newbcast:12 row_mask:0xf bank_mask:0xf\n" \
  "v_and_b32_dpp v49, v40, v5 row_newbcast:12 row_mask:0xf bank_mask:0xf\n" \
  "v_mfma_f32_16x16x32_bf16 v[24:27], v[44:47], v[224:227], v[24:27]\n" \
  "v_and_b32_dpp v50, v40, v6 row_newbcast:12 row_mask:0xf bank_mask:0xf\n" \
  "v_and_b32_dpp v51, v40, v7 row_newbcast:12 row_mask:0xf bank_mask:0xf\n" \
  "v_add_f32_dpp v8, v8, v8 row_bcast:31 row_mask:0xc bank_mask:0xf\n" \
  "v_mfma_f32_16x16x32_bf16 v[28:31], v[44:47], v[228:231], v[28:31]\n" \
  "v_and_b32_dpp v44, v41, v4 row_newbcast:0 row_mask:0xf bank_mask:0xf\n" \
  "v_and_b32_dpp v45, v41, v5 row_newbcast:0 row_mask:0xf bank_mask:0xf\n" \
  "v_mfma_f32_16x16x32_bf16 v[24:27], v[48:51], v[232:235], v[24:27]\n" \
  "v_and_b32_dpp v46, v41, v6 row_newbcast:0 row_mask:0xf bank_mask:0xf\n" \
  "v_and_b32_dpp v47, v41, v7 row_newbcast:0 row_mask:0xf bank_mask:0xf\n" \
  "v_readlane_b32 s71, v8, 63\n" \
  "v_mfma_f32_16x16x32_bf16 v[28:31], v[48:51], v[236:239], v[28:31]\n" \
  "v_and_b32_dpp v48, v41, v4 row_newbcast:4 row_mask:0xf bank_mask:0xf\n" \
  "v_and_b32_dpp v49, v41, v5 row_newbcast:4 row_mask:0xf bank_mask:0xf\n" \
  "v_mfma_f32_16x16x32_bf16 v[24:27], v[44:47], v[240:243], v[24:27]\n" \
  "v_and_b32_dpp v50, v41, v6 row_newbcast:4 row_mask:0xf bank_mask:0xf\n" \
  "v_and_b32_dpp v51, v41, v7 row_newbcast:4 row_mask:0xf bank_mask:0xf\n" \
  "v_mfma_f32_16x16x32_bf16 v[28:31], v[44:47], v[244:247], v[28:31]\n" \
  "v_and_b32_dpp v44, v41, v4 row_newbcast:8 row_mask:0xf bank_mask:0xf\n" \
  "v_and_b32_dpp v45, v41, v5 row_newbcast:8 row_mask:0xf bank_mask:0xf\n" \
  "v_mfma_f32_16x16x32_bf16 v[24:27], v[48:51], v[248:251], v[24:27]\n" \
  "v_and_b32_dpp v46, v41, v6 row_newbcast:8 row_mask:0xf bank_mask:0xf\n" \
  "v_and_b32_dpp v47, v41, v7 row_newbcast:8 row_mask:0xf bank_mask:0xf\n" \
  "v_mfma_f32_16x16x32_bf16 v[28:31], v[48:51], v[252:255], v[28:31]\n" \
  "v_and_b32_dpp v48, v37, v4 row_newbcast:0 row_mask:0xf bank_mask:0xf\n" \
  "v_and_b32_dpp v49, v37, v5 row_newbcast:0 row_mask:0xf bank_mask:0xf\n" \
  "s_waitcnt lgkmcnt(0)\n" \
  "v_mfma_f32_16x16x32_bf16 v[24:27], v[44:47], v[168:171], v[24:27]\n" \
  "v_and_b32_dpp v50, v37, v6 row_newbcast:0 row_mask:0xf bank_mask:0xf\n" \
  "v_and_b32_dpp v51, v37, v7 row_newbcast:0 row_mask:0xf bank_mask:0xf\n" \
  "v_mfma_f32_16x16x32_bf16 v[28:31], v[44:47], v[172:175], v[28:31]\n" \
  "v_and_b32_dpp v44, v37, v4 row_newbcast:4 row_mask:0xf bank_mask:0xf\n" \
  "v_and_b32_dpp v45, v37, v5 row_newbcast:4 row_mask:0xf bank_mask:0xf\n" \
  "v_mfma_f32_16x16x32_bf16 v[24:27], v[48:51], v[104:107], v[24:27]\n" \
  "v_and_b32_dpp v46, v37, v6 row_newbcast:4 row_mask:0xf bank_mask:0xf\n" \
  "v_and_b32_dpp v47, v37, v7 row_newbcast:4 row_mask:0xf bank_mask:0xf\n" \
  "v_mfma_f32_16x16x32_bf16 v[28:31], v[48:51], v[108:111], v[28:31]\n" \
  "v_and_b32_dpp v48, v37, v4 row_newbcast:8 row_mask:0xf bank_mask:0xf\n" \
  "v_and_b32_dpp v49, v37, v5 row_newbcast:8 row_mask:0xf bank_mask:0xf\n" \
  "v_mfma_f32_16x16x32_bf16 v[24:27], v[44:47], v[112:115], v[24:27]\n" \
  "v_and_b32_dpp v50, v37, v6 row_newbcast:8 row_mask:0xf bank_mask:0xf\n" \
  "v_and_b32_dpp v51, v37, v7 row_newbcast:8 row_mask:0xf bank_mask:0xf\n" \
  "v_mfma_f32_16x16x32_bf16 v[28:31], v[44:47], v[116:119], v[28:31]\n" \
  "v_and_b32_dpp v44, v37, v4 row_newbcast:12 row_mask:0xf bank_mask:0xf\n" \
  "v_and_b32_dpp v45, v37, v5 row_newbcast:12 row_mask:0xf bank_mask:0xf\n" \
  "v_mfma_f32_16x16x32_bf16 v[24:27], v[48:51], v[120:123], v[24:27]\n" \
  "v_and_b32_dpp v46, v37, v6 row_newbcast:12 row_mask:0xf bank_mask:0xf\n" \
  "v_and_b32_dpp v47, v37, v7 row_newbcast:12 row_mask:0xf bank_mask:0xf\n" \
  "v_mfma_f32_16x16x32_bf16 v[28:31], v[48:51], v[124:127], v[28:31]\n" \
  "v_and_b32_dpp v48, v38, v4 row_newbcast:0 row_mask:0xf bank_mask:0xf\n" \
  "v_and_b32_dpp v49, v38, v5 row_newbcast:0 row_mask:0xf bank_mask:0xf\n" \
  "v_mfma_f32_16x16x32_bf16 v[24:27], v[44:47], v[128:131], v[24:27]\n" \
  "v_and_b32_dpp v50, v38, v6 row_newbcast:0 row_mask:0xf bank_mask:0xf\n" \
  "v_and_b32_dpp v51, v38, v7 row_newbcast:0 row_mask:0xf bank_mask:0xf\n" \
  "v_mfma_f32_16x16x32_bf16 v[28:31], v[44:47], v[132:135], v[28:31]\n" \
  "v_and_b32_dpp v44, v38, v4 row_newbcast:4 row_mask:0xf bank_mask:0xf\n" \
  "v_and_b32_dpp v45, v38, v5 row_newbcast:4 row_mask:0xf bank_mask:0xf\n" \
  "v_mfma_f32_16x16x32_bf16 v[24:27], v[48:51], v[136:139], v[24:27]\n" \
  "v_and_b32_dpp v46, v38, v6 row_newbcast:4 row_mask:0xf bank_mask:0xf\n" \
  "v_and_b32_dpp v47, v38, v7 row_newbcast:4 row_mask:0xf bank_mask:0xf\n" \
  "v_mfma_f32_16x16x32_bf16 v[28:31], v[48:51], v[140:143], v[28:31]\n" \
  "v_and_b32_dpp v48, v38, v4 row_newbcast:8 row_mask:0xf bank_mask:0xf\n" \
  "v_and_b32_dpp v49, v38, v5 row_newbcast:8 row_mask:0xf bank_mask:0xf\n" \
  "v_mfma_f32_16x16x32_bf16 v[24:27], v[44:47], v[144:147], v[24:27]\n" \
  "v_and_b32_dpp v50, v38, v6 row_newbcast:8 row_mask:0xf bank_mask:0xf\n" \
  "v_and_b32_dpp v51, v38, v7 row_newbcast:8 row_mask:0xf bank_mask:0xf\n" \
  "v_mfma_f32_16x16x32_bf16 v[28:31], v[44:47], v[148:151], v[28:31]\n" \
  "v_and_b32_dpp v44, v38, v4 row_newbcast:12 row_mask:0xf bank_mask:0xf\n" \
  "v_and_b32_dpp v45, v38, v5 row_newbcast:12 row_mask:0xf bank_mask:0xf\n" \
  "v_mfma_f32_16x16x32_bf16 v[24:27], v[48:51], v[152:155], v[24:27]\n" \
  "v_and_b32_dpp v46, v38, v6 row_newbcast:12 row_mask:0xf bank_mask:0xf\n" \
  "v_and_b32_dpp v47, v38, v7 row_newbcast:12 row_mask:0xf bank_mask:0xf\n" \
  "v_mfma_f32_16x16x32_bf16 v[28:31], v[48:51], v[156:159], v[28:31]\n" \
  "s_nop 0\n" \
  "v_mfma_f32_16x16x32_bf16 v[24:27], v[44:47], v[160:163], v[24:27]\n" \
  "v_mfma_f32_16x16x32_bf16 v[28:31], v[44:47], v[164:167], v[28:31]\n" \
  "s_mov_b32 s81, 155776\n" \
  "s_mov_b32 s82, s79\n" \
  "s_add_u32 s79, s79, 143360\n" \
  "s_add_u32 s59, s87, 155776\n" \
  "s_sub_u32 s85, 155776, s67\n" \
  "v_add_u32 v42, 155776, v3\n" \
  "v_and_b32 v43, 0x30, v2\n" \
  "v_add_u32 v63, s85, v2\n" \
  "v_mov_b32 v52, 1\n" \
  "v_add_u32 v43, 155776, v43\n" \
  "v_mov_b32 v48, s69\n" \
  "v_mov_b32 v49, s71\n" \
  "v_mov_b32 v50, s59\n" \
  "v_mov_b32 v51, s81\n" \
  "v_cndmask_b32 v44, v24, v28, s[88:89]\n" \
  "v_cndmask_b32 v45, v25, v29, s[88:89]\n" \
  "v_cndmask_b32 v46, v26, v30, s[88:89]\n" \
  "v_cndmask_b32 v47, v27, v31, s[88:89]\n" \
  "s_call_b64 s[94:95], .LA_MERGE\n" \
  ".LA_PAD_A:\n" \
  "buffer_store_dword v42, off, s[48:51], 0\n" \
  "buffer_store_dword v42, off, s[48:51], 0\n" \
  ".LA_END_A:\n" \
  "s_cmp_eq_u32 s72, 0\n" \
  "s_mov_b32 s27, 1660\n" \
  "s_cselect_b32 s27, 1660, s27\n" \
  "s_waitcnt vmcnt(15)\n" \
  "buffer_load_dwordx4 v[76:79], v0, s[36:39], s5 offen offset:256 nt\n" \
  "s_waitcnt vmcnt(15)\n" \
  "s_call_b64 s[34:35], .LA_GATE\n" \
  "buffer_load_dwordx4 v[80:83], v0, s[36:39], s6 offen nt\n" \
  "s_waitcnt vmcnt(15)\n" \
  "buffer_load_dwordx4 v[84:87], v0, s[36:39], s6 offen offset:256 nt\n" \
  "s_waitcnt vmcnt(15)\n" \
  "s_call_b64 s[34:35], .LA_GATE\n" \
  "buffer_load_dwordx4 v[88:91], v0, s[36:39], s7 offen nt\n" \
  "s_waitcnt vmcnt(15)\n" \
  "buffer_load_dwordx4 v[92:95], v0, s[36:39], s7 offen offset:256 nt\n" \
  "s_waitcnt vmcnt(15)\n" \
  "s_call_b64 s[34:35], .LA_GATE\n" \
  "buffer_load_dwordx4 v[96:99], v0, s[36:39], s8 offen nt\n" \
  "s_waitcnt vmcnt(13)\n" \
  "buffer_load_dwordx4 v[100:103], v0, s[36:39], s8 offen offset:256 nt\n" \
  "s_waitcnt vmcnt(13)\n" \
  "s_call_b64 s[34:35], .LA_GATE\n" \
  "buffer_load_dwordx4 v[104:107], v0, s[36:39], s9 offen nt\n" \
  "s_waitcnt vmcnt(13)\n" \
  "buffer_load_dwordx4 v[108:111], v0, s[36:39], s9 offen offset:256 nt\n" \
  "s_waitcnt vmcnt(13)\n" \
  "s_call_b64 s[34:35], .LA_GATE\n" \
  "buffer_load_dwordx4 v[112:115], v0, s[36:39], s10 offen nt\n" \
  "s_waitcnt vmcnt(13)\n" \
  "buffer_load_dwordx4 v[116:119], v0, s[36:39], s10 offen offset:256 nt\n" \
  "s_waitcnt vmcnt(13)\n" \
  "s_call_b64 s[34:35], .LA_GATE\n" \
  "buffer_load_dwordx4 v[120:123], v0, s[36:39], s11 offen nt\n" \
  "s_waitcnt vmcnt(11)\n" \
  "buffer_load_dwordx4 v[124:127], v0, s[36:39], s11 offen offset:256 nt\n" \
  "s_waitcnt vmcnt(11)\n" \
  "s_call_b64 s[34:35], .LA_GATE\n" \
  "buffer_load_dwordx4 v[128:131], v0, s[36:39], s12 offen nt\n" \
  "s_waitcnt vmcnt(11)\n" \
  "buffer_load_dwordx4 v[132:135], v0, s[36:39], s12 offen offset:256 nt\n" \
  "s_waitcnt vmcnt(11)\n" \
  "s_call_b64 s[34:35], .LA_GATE\n" \
  "buffer_load_dwordx4 v[136:139], v0, s[36:39], s13 offen nt\n" \
  "s_waitcnt vmcnt(11)\n" \
  "buffer_load_dwordx4 v[140:143], v0, s[36:39], s13 offen offset:256 nt\n" \
  "s_waitcnt vmcnt(11)\n" \
  "s_call_b64 s[34:35], .LA_GATE\n" \
  "buffer_load_dwordx4 v[144:147], v0, s[36:39], s14 offen nt\n" \
  "s_waitcnt vmcnt(11)\n" \
  "buffer_load_dwordx4 v[148:151], v0, s[36:39], s14 offen offset:256 nt\n" \
  "s_waitcnt vmcnt(11)\n" \
  "s_call_b64 s[34:35], .LA_GATE\n" \
  "buffer_load_dwordx4 v[152:155], v0, s[36:39], s15 offen nt\n" \
  "s_waitcnt vmcnt(11)\n" \
  "buffer_load_dwordx4 v[156:159], v0, s[36:39], s15 offen offset:256 nt\n" \
  "s_waitcnt vmcnt(11)\n" \
  "s_call_b64 s[34:35], .LA_GATE\n" \
  "buffer_load_dwordx4 v[160:163], v0, s[36:39], s16 offen nt\n" \
  "s_waitcnt vmcnt(11)\n" \
  "buffer_load_dwordx4 v[164:167], v0, s[36:39], s16 offen offset:256 nt\n" \
  "s_waitcnt vmcnt(11)\n" \
  "s_call_b64 s[34:35], .LA_GATE\n" \
  "buffer_load_dwordx4 v[168:171], v0, s[36:39], s17 offen nt\n" \
  "s_waitcnt vmcnt(11)\n" \
  "buffer_load_dwordx4 v[172:175], v0, s[36:39], s17 offen offset:256 nt\n" \
  "s_waitcnt vmcnt(11)\n" \
  "s_add_u32 s40, s40, 0x2000000\n" \
  "s_addc_u32 s41, s41, 0\n" \
  "s_mov_b64 s[44:45], s[40:41]\n" \
  "s_mov_b32 s46, s62\n" \
  "s_add_u32 m0, s66, 2048\n" \
  "s_nop 0\n" \
  "buffer_load_dwordx4 v1, s[44:47], s58 offen nt lds\n" \
  "s_waitcnt vmcnt(11)\n" \
  "s_add_u32 m0, s66, 2816\n" \
  "s_nop 0\n" \
  "buffer_load_dwordx4 v1, s[44:47], s58 offen offset:256 nt lds\n" \
  "s_waitcnt vmcnt(11)\n" \
  "s_call_b64 s[34:35], .LA_GATE\n" \
  "s_add_u32 m0, s67, 16384\n" \
  "s_nop 0\n" \
  "buffer_load_dwordx4 v1, s[40:43], s0 offen nt lds\n" \
  "s_waitcnt vmcnt(11)\n" \
  "s_add_u32 m0, s67, 17152\n" \
  "s_nop 0\n" \
  "buffer_load_dwordx4 v1, s[40:43], s0 offen offset:256 nt lds\n" \
  "v_and_b32 v42, 0xc0, v0\n" \
  "v_and_b32 v43, 0xf0, v0\n" \
  "v_add_u32 v42, v42, v43\n" \
  "buffer_load_dwordx4 v[8:11], v42, s[52:55], s75 offen sc1\n" \
  "buffer_load_dwordx4 v[12:15], v42, s[52:55], s75 offen offset:512 sc1\n" \
  "s_waitcnt vmcnt(13)\n" \
  "s_call_b64 s[34:35], .LA_GATE\n" \
  "s_add_u32 m0, s67, 18432\n" \
  "s_nop 0\n" \
  "buffer_load_dwordx4 v1, s[40:43], s1 offen nt lds\n" \
  "s_waitcnt vmcnt(13)\n" \
  "s_add_u32 m0, s67, 19200\n" \
  "s_nop 0\n" \
  "buffer_load_dwordx4 v1, s[40:43], s1 offen offset:256 nt lds\n" \
  "s_waitcnt vmcnt(13)\n" \
  "s_call_b64 s[34:35], .LA_GATE\n" \
  "s_add_u32 m0, s67, 20480\n" \
  "s_nop 0\n" \
  "buffer_load_dwordx4 v1, s[40:43], s2 offen nt lds\n" \
  "s_waitcnt vmcnt(13)\n" \
  "s_add_u32 m0, s67, 21248\n" \
  "s_nop 0\n" \
  "buffer_load_dwordx4 v1, s[40:43], s2 offen offset:256 nt lds\n" \
  "s_waitcnt vmcnt(13)\n" \
  "s_call_b64 s[34:35], .LA_GATE\n" \
  "s_add_u32 m0, s67, 22528\n" \
  "s_nop 0\n" \
  "buffer_load_dwordx4 v1, s[40:43], s3 offen nt lds\n" \
  "s_waitcnt vmcnt(13)\n" \
  "s_add_u32 m0, s67, 23296\n" \
  "s_nop 0\n" \
  "buffer_load_dwordx4 v1, s[40:43], s3 offen offset:256 nt lds\n" \
  "s_waitcnt vmcnt(13)\n" \
  "s_call_b64 s[34:35], .LA_GATE\n" \
  "s_add_u32 m0, s67, 24576\n" \
  "s_nop 0\n" \
  "buffer_load_dwordx4 v1, s[40:43], s4 offen nt lds\n" \
  "s_waitcnt vmcnt(13)\n" \
  "s_add_u32 m0, s67, 25344\n" \
  "s_nop 0\n" \
  "buffer_load_dwordx4 v1, s[40:43], s4 offen offset:256 nt lds\n" \
  "s_waitcnt vmcnt(8)\n" \
  "s_mov_b32 s86, s75\n" \
  "s_call_b64 s[94:95], .LA_QWAIT\n" \
  "s_add_u32 s75, s75, 16384\n" \
  "v_mfma_f32_16x16x32_bf16 v[44:47], a[224:227], v[8:11], 0\n" \
  "v_add_u32 v43, s80, v2\n" \
  "v_mov_b32 v41, 0xff800000\n" \
  "v_mfma_f32_16x16x32_bf16 v[44:47], a[228:231], v[12:15], v[44:47]\n" \
  "ds_read_b128 v[56:59], v43\n" \
  "ds_read_b128 v[60:63], v43 offset:1024\n" \
  "v_mov_b32 v42, s64\n" \
  "v_mfma_f32_16x16x32_bf16 v[48:51], a[232:235], v[8:11], 0\n" \
  "v_mfma_f32_16x16x32_bf16 v[48:51], a[236:239], v[12:15], v[48:51]\n" \
  "v_mov_b32_dpp v41, v42 quad_perm:[0,1,2,3] row_mask:0xf bank_mask:0x4\n" \
  "v_mfma_f32_16x16x32_bf16 v[52:55], a[240:243], v[8:11], 0\n" \
  "s_nop 0\n" \
  "v_mov_b32_dpp v37, v44 quad_perm:[0,0,0,0] row_mask:0xf bank_mask:0x1\n" \
  "v_mov_b32_dpp v42, v45 quad_perm:[1,1,1,1] row_mask:0xf bank_mask:0x1\n" \
  "v_mfma_f32_16x16x32_bf16 v[52:55], a[244:247], v[12:15], v[52:55]\n" \
  "v_add_f32_dpp v37, v46, v37 quad_perm:[2,2,2,2] row_mask:0xf bank_mask:0x1\n" \
  "v_add_f32_dpp v42, v47, v42 quad_perm:[3,3,3,3] row_mask:0xf bank_mask:0x1\n" \
  "v_mfma_f32_16x16x32_bf16 v[44:47], a[248:251], v[8:11], 0\n" \
  "s_waitcnt vmcnt(13)\n" \
  "s_add_u32 s33, s33, s27\n" \
  "s_add_u32 m0, s67, 26624\n" \
  "s_nop 0\n" \
  "buffer_load_dwordx4 v1, s[40:43], s5 offen nt lds\n" \
  "v_mov_b32_dpp v37, v48 quad_perm:[0,0,0,0] row_mask:0xf bank_mask:0x2\n" \
  "v_mov_b32_dpp v42, v49 quad_perm:[1,1,1,1] row_mask:0xf bank_mask:0x2\n" \
  "v_mfma_f32_16x16x32_bf16 v[44:47], a[252:255], v[12:15], v[44:47]\n" \
  "v_add_f32_dpp v37, v50, v37 quad_perm:[2,2,2,2] row_mask:0xf bank_mask:0x2\n" \
  "v_add_f32_dpp v42, v51, v42 quad_perm:[3,3,3,3] row_mask:0xf bank_mask:0x2\n" \
  "v_mfma_f32_16x16x32_bf16 v[48:51], v[64:67], v[8:11], 0\n" \
  "v_mov_b32_dpp v37, v52 quad_perm:[0,0,0,0] row_mask:0xf bank_mask:0x4\n" \
  "v_mov_b32_dpp v42, v53 quad_perm:[1,1,1,1] row_mask:0xf bank_mask:0x4\n" \
  "v_mfma_f32_16x16x32_bf16 v[48:51], v[68:71], v[12:15], v[48:51]\n" \
  "v_add_f32_dpp v37, v54, v37 quad_perm:[2,2,2,2] row_mask:0xf bank_mask:0x4\n" \
  "v_add_f32_dpp v42, v55, v42 quad_perm:[3,3,3,3] row_mask:0xf bank_mask:0x4\n" \
  "v_mfma_f32_16x16x32_bf16 v[52:55], v[72:75], v[8:11], 0\n" \
  "v_mov_b32_dpp v37, v44 quad_perm:[0,0,0,0] row_mask:0xf bank_mask:0x8\n" \
  "v_mov_b32_dpp v42, v45 quad_perm:[1,1,1,1] row_mask:0xf bank_mask:0x8\n" \
  "v_mfma_f32_16x16x32_bf16 v[52:55], v[76:79], v[12:15], v[52:55]\n" \
  "v_add_f32_dpp v37, v46, v37 quad_perm:[2,2,2,2] row_mask:0xf bank_mask:0x8\n" \
  "v_add_f32_dpp v42, v47, v42 quad_perm:[3,3,3,3] row_mask:0xf bank_mask:0x8\n" \
  "v_mfma_f32_16x16x32_bf16 v[44:47], v[80:83], v[8:11], 0\n" \
  "v_mov_b32_dpp v38, v48 quad_perm:[0,0,0,0] row_mask:0xf bank_mask:0x1\n" \
  "v_mov_b32_dpp v43, v49 quad_perm:[1,1,1,1] row_mask:0xf bank_mask:0x1\n" \
  "v_mfma_f32_16x16x32_bf16 v[44:47], v[84:87], v[12:15], v[44:47]\n" \
  "v_add_f32_dpp v38, v50, v38 quad_perm:[2,2,2,2] row_mask:0xf bank_mask:0x1\n" \
  "v_add_f32_dpp v43, v51, v43 quad_perm:[3,3,3,3] row_mask:0xf bank_mask:0x1\n" \
  "v_add_f32 v37, v37, v42\n" \
  "v_mfma_f32_16x16x32_bf16 v[48:51], v[88:91], v[8:11], 0\n" \
  "v_mov_b32_dpp v38, v52 quad_perm:[0,0,0,0] row_mask:0xf bank_mask:0x2\n" \
  "v_mov_b32_dpp v43, v53 quad_perm:[1,1,1,1] row_mask:0xf bank_mask:0x2\n" \
  "v_mfma_f32_16x16x32_bf16 v[48:51], v[92:95], v[12:15], v[48:51]\n" \
  "v_add_f32_dpp v38, v54, v38 quad_perm:[2,2,2,2] row_mask:0xf bank_mask:0x2\n" \
  "v_add_f32_dpp v43, v55, v43 quad_perm:[3,3,3,3] row_mask:0xf bank_mask:0x2\n" \
  "v_mfma_f32_16x16x32_bf16 v[52:55], v[96:99], v[8:11], 0\n" \
  "v_mov_b32_dpp v38, v44 quad_perm:[0,0,0,0] row_mask:0xf bank_mask:0x4\n" \
  "v_mov_b32_dpp v43, v45 quad_perm:[1,1,1,1] row_mask:0xf bank_mask:0x4\n" \
  "v_mfma_f32_16x16x32_bf16 v[52:55], v[100:103], v[12:15], v[52:55]\n" \
  "s_waitcnt vmcnt(13)\n" \
  "s_add_u32 m0, s67, 27392\n" \
  "s_nop 0\n" \
  "buffer_load_dwordx4 v1, s[40:43], s5 offen offset:256 nt lds\n" \
  "v_add_f32_dpp v38, v46, v38 quad_perm:[2,2,2,2] row_mask:0xf bank_mask:0x4\n" \
  "v_add_f32_dpp v43, v47, v43 quad_perm:[3,3,3,3] row_mask:0xf bank_mask:0x4\n" \
  "v_mfma_f32_16x16x32_bf16 v[44:47], v[104:107], v[8:11], 0\n" \
  "v_mov_b32_dpp v38, v48 quad_perm:[0,0,0,0] row_mask:0xf bank_mask:0x8\n" \
  "v_mov_b32_dpp v43, v49 quad_perm:[1,1,1,1] row_mask:0xf bank_mask:0x8\n" \
  "v_mfma_f32_16x16x32_bf16 v[44:47], v[108:111], v[12:15], v[44:47]\n" \
  "v_add_f32_dpp v38, v50, v38 quad_perm:[2,2,2,2] row_mask:0xf bank_mask:0x8\n" \
  "v_add_f32_dpp v43, v51, v43 quad_perm:[3,3,3,3] row_mask:0xf bank_mask:0x8\n" \
  "v_mfma_f32_16x16x32_bf16 v[48:51], v[112:115], v[8:11], 0\n" \
  "v_mov_b32_dpp v39, v52 quad_perm:[0,0,0,0] row_mask:0xf bank_mask:0x1\n" \
  "v_mov_b32_dpp v42, v53 quad_perm:[1,1,1,1] row_mask:0xf bank_mask:0x1\n" \
  "v_mfma_f32_16x16x32_bf16 v[48:51], v[116:119], v[12:15], v[48:51]\n" \
  "v_add_f32_dpp v39, v54, v39 quad_perm:[2,2,2,2] row_mask:0xf bank_mask:0x1\n" \
  "v_add_f32_dpp v42, v55, v42 quad_perm:[3,3,3,3] row_mask:0xf bank_mask:0x1\n" \
  "v_add_f32 v38, v38, v43\n" \
  "v_mfma_f32_16x16x32_bf16 v[52:55], v[120:123], v[8:11], 0\n" \
  "v_mov_b32_dpp v39, v44 quad_perm:[0,0,0,0] row_mask:0xf bank_mask:0x2\n" \
  "v_mov_b32_dpp v42, v45 quad_perm:[1,1,1,1] row_mask:0xf bank_mask:0x2\n" \
  "v_mfma_f32_16x16x32_bf16 v[52:55], v[124:127], v[12:15], v[52:55]\n" \
  "v_add_f32_dpp v39, v46, v39 quad_perm:[2,2,2,2] row_mask:0xf bank_mask:0x2\n" \
  "v_add_f32_dpp v42, v47, v42 quad_perm:[3,3,3,3] row_mask:0xf bank_mask:0x2\n" \
  "v_mfma_f32_16x16x32_bf16 v[44:47], v[128:131], v[8:11], 0\n" \
  "v_mov_b32_dpp v39, v48 quad_perm:[0,0,0,0] row_mask:0xf bank_mask:0x4\n" \
  "v_mov_b32_dpp v42, v49 quad_perm:[1,1,1,1] row_mask:0xf bank_mask:0x4\n" \
  "v_mfma_f32_16x16x32_bf16 v[44:47], v[132:135], v[12:15], v[44:47]\n" \
  "v_add_f32_dpp v39, v50, v39 quad_perm:[2,2,2,2] row_mask:0xf bank_mask:0x4\n" \
  "v_add_f32_dpp v42, v51, v42 quad_perm:[3,3,3,3] row_mask:0xf bank_mask:0x4\n" \
  "v_mfma_f32_16x16x32_bf16 v[48:51], v[136:139], v[8:11], 0\n" \
  "v_mov_b32_dpp v39, v52 quad_perm:[0,0,0,0] row_mask:0xf bank_mask:0x8\n" \
  "v_mov_b32_dpp v42, v53 quad_perm:[1,1,1,1] row_mask:0xf bank_mask:0x8\n" \
  "v_mfma_f32_16x16x32_bf16 v[48:51], v[140:143], v[12:15], v[48:51]\n" \
  "v_add_f32_dpp v39, v54, v39 quad_perm:[2,2,2,2] row_mask:0xf bank_mask:0x8\n" \
  "v_add_f32_dpp v42, v55, v42 quad_perm:[3,3,3,3] row_mask:0xf bank_mask:0x8\n" \
  "v_mfma_f32_16x16x32_bf16 v[52:55], v[144:147], v[8:11], 0\n" \
  "s_waitcnt vmcnt(13)\n" \
  "s_add_u32 s33, s33, s27\n" \
  "s_add_u32 m0, s67, 28672\n" \
  "s_nop 0\n" \
  "buffer_load_dwordx4 v1, s[40:43], s6 offen nt lds\n" \
  "v_mov_b32_dpp v40, v44 quad_perm:[0,0,0,0] row_mask:0xf bank_mask:0x1\n" \
  "v_mov_b32_dpp v43, v45 quad_perm:[1,1,1,1] row_mask:0xf bank_mask:0x1\n" \
  "v_mfma_f32_16x16x32_bf16 v[52:55], v[148:151], v[12:15], v[52:55]\n" \
  "v_add_f32_dpp v40, v46, v40 quad_perm:[2,2,2,2] row_mask:0xf bank_mask:0x1\n" \
  "v_add_f32_dpp v43, v47, v43 quad_perm:[3,3,3,3] row_mask:0xf bank_mask:0x1\n" \
  "v_add_f32 v39, v39, v42\n" \
  "v_mfma_f32_16x16x32_bf16 v[44:47], v[152:155], v[8:11], 0\n" \
  "v_mov_b32_dpp v40, v48 quad_perm:[0,0,0,0] row_mask:0xf bank_mask:0x2\n" \
  "v_mov_b32_dpp v43, v49 quad_perm:[1,1,1,1] row_mask:0xf bank_mask:0x2\n" \
  "v_mfma_f32_16x16x32_bf16 v[44:47], v[156:159], v[12:15], v[44:47]\n" \
  "v_add_f32_dpp v40, v50, v40 quad_perm:[2,2,2,2] row_mask:0xf bank_mask:0x2\n" \
  "v_add_f32_dpp v43, v51, v43 quad_perm:[3,3,3,3] row_mask:0xf bank_mask:0x2\n" \
  "v_mfma_f32_16x16x32_bf16 v[48:51], v[160:163], v[8:11], 0\n" \
  "v_mov_b32_dpp v40, v52 quad_perm:[0,0,0,0] row_mask:0xf bank_mask:0x4\n" \
  "v_mov_b32_dpp v43, v53 quad_perm:[1,1,1,1] row_mask:0xf bank_mask:0x4\n" \
  "v_mfma_f32_16x16x32_bf16 v[48:51], v[164:167], v[12:15], v[48:51]\n" \
  "v_add_f32_dpp v40, v54, v40 quad_perm:[2,2,2,2] row_mask:0xf bank_mask:0x4\n" \
  "v_add_f32_dpp v43, v55, v43 quad_perm:[3,3,3,3] row_mask:0xf bank_mask:0x4\n" \
  "v_mfma_f32_16x16x32_bf16 v[52:55], v[168:171], v[8:11], 0\n" \
  "v_mov_b32_dpp v40, v44 quad_perm:[0,0,0,0] row_mask:0xf bank_mask:0x8\n" \
  "v_mov_b32_dpp v43, v45 quad_perm:[1,1,1,1] row_mask:0xf bank_mask:0x8\n" \
  "v_mfma_f32_16x16x32_bf16 v[52:55], v[172:175], v[12:15], v[52:55]\n" \
  "v_add_f32_dpp v40, v46, v40 quad_perm:[2,2,2,2] row_mask:0xf bank_mask:0x8\n" \
  "v_add_f32_dpp v43, v47, v43 quad_perm:[3,3,3,3] row_mask:0xf bank_mask:0x8\n" \
  "s_waitcnt lgkmcnt(0)\n" \
  "v_mfma_f32_16x16x32_bf16 v[44:47], v[56:59], v[8:11], 0\n" \
  "v_mov_b32_dpp v41, v48 quad_perm:[0,0,0,0] row_mask:0xf bank_mask:0x1\n" \
  "v_mov_b32_dpp v42, v49 quad_perm:[1,1,1,1] row_mask:0xf bank_mask:0x1\n" \
  "v_mfma_f32_16x16x32_bf16 v[44:47], v[60:63], v[12:15], v[44:47]\n" \
  "v_add_f32_dpp v41, v50, v41 quad_perm:[2,2,2,2] row_mask:0xf bank_mask:0x1\n" \
  "v_add_f32_dpp v42, v51, v42 quad_perm:[3,3,3,3] row_mask:0xf bank_mask:0x1\n" \
  "v_add_f32 v40, v40, v43\n" \
  "v_mov_b32_dpp v41, v52 quad_perm:[0,0,0,0] row_mask:0xf bank_mask:0x2\n" \
  "v_mov_b32_dpp v42, v53 quad_perm:[1,1,1,1] row_mask:0xf bank_mask:0x2\n" \
  "s_nop 0\n" \
  "v_add_f32_dpp v41, v54, v41 quad_perm:[2,2,2,2] row_mask:0xf bank_mask:0x2\n" \
  "v_add_f32_dpp v42, v55, v42 quad_perm:[3,3,3,3] row_mask:0xf bank_mask:0x2\n" \
  "s_nop 0\n" \
  "v_add_f32_dpp v41, v44, v41 quad_perm:[0,0,0,0] row_mask:0xf bank_mask:0x4\n" \
  "v_mov_b32_dpp v42, v45 quad_perm:[1,1,1,1] row_mask:0xf bank_mask:0x4\n" \
  "s_nop 0\n" \
  "v_add_f32_dpp v41, v46, v41 quad_perm:[2,2,2,2] row_mask:0xf bank_mask:0x4\n" \
  "v_add_f32_dpp v42, v47, v42 quad_perm:[3,3,3,3] row_mask:0xf bank_mask:0x4\n" \
  "v_add_f32 v41, v41, v42\n" \
  "s_waitcnt vmcnt(13)\n" \
  "s_add_u32 m0, s67, 29440\n" \
  "s_nop 0\n" \
  "buffer_load_dwordx4 v1, s[40:43], s6 offen offset:256 nt lds\n" \
  "v_max3_f32 v42, v37, v38, v39\n" \
  "v_max3_f32 v42, v42, v40, v41\n" \
  "s_nop 1\n" \
  "v_max_f32_dpp v42, v42, v42 row_ror:4 row_mask:0xf bank_mask:0xf\n" \
  "s_nop 1\n" \
  "v_max_f32_dpp v42, v42, v42 row_ror:8 row_mask:0xf bank_mask:0xf\n" \
  "s_nop 1\n" \
  "v_max_f32_dpp v42, v42, v42 row_bcast:15 row_mask:0xa bank_mask:0xf\n" \
  "s_nop 1\n" \
  "v_max_f32_dpp v42, v42, v42 row_bcast:31 row_mask:0xc bank_mask:0xf\n" \
  "s_nop 0\n" \
  "v_readlane_b32 s69, v42, 63\n" \
  "s_nop 1\n" \
  "v_subrev_f32 v37, s69, v37\n" \
  "v_subrev_f32 v38, s69, v38\n" \
  "v_subrev_f32 v39, s69, v39\n" \
  "v_subrev_f32 v40, s69, v40\n" \
  "v_subrev_f32 v41, s69, v41\n" \
  "v_exp_f32 v37, v37\n" \
  "v_exp_f32 v38, v38\n" \
  "v_exp_f32 v39, v39\n" \
  "v_exp_f32 v40, v40\n" \
  "v_exp_f32 v41, v41\n" \
  "v_add_f32 v8, v37, v38\n" \
  "v_add_f32 v9, v39, v40\n" \
  "v_cvt_pk_bf16_f32 v37, v37, v37\n" \
  "v_cvt_pk_bf16_f32 v38, v38, v38\n" \
  "v_cvt_pk_bf16_f32 v39, v39, v39\n" \
  "v_cvt_pk_bf16_f32 v40, v40, v40\n" \
  "v_add_f32 v9, v9, v41\n" \
  "v_add_f32 v8, v8, v9\n" \
  "v_cvt_pk_bf16_f32 v41, v41, v41\n" \
  "s_waitcnt vmcnt(11)\n" \
  "s_call_b64 s[34:35], .LA_GATE\n" \
  "s_add_u32 m0, s67, 30720\n" \
  "s_nop 0\n" \
  "buffer_load_dwordx4 v1, s[40:43], s7 offen nt lds\n" \
  "v_add_f32_dpp v8, v8, v8 row_ror:4 row_mask:0xf bank_mask:0xf\n" \
  "s_nop 1\n" \
  "v_add_f32_dpp v8, v8, v8 row_ror:8 row_mask:0xf bank_mask:0xf\n" \
  "s_nop 1\n" \
  "v_add_f32_dpp v8, v8, v8 row_bcast:15 row_mask:0xa bank_mask:0xf\n" \
  "s_nop 1\n" \
  "v_add_f32_dpp v8, v8, v8 row_bcast:31 row_mask:0xc bank_mask:0xf\n" \
  "s_nop 0\n" \
  "v_readlane_b32 s71, v8, 63\n" \
  "v_and_b32_dpp v48, v41, v4 row_newbcast:8 row_mask:0xf bank_mask:0xf\n" \
  "v_and_b32_dpp v49, v41, v5 row_newbcast:8 row_mask:0xf bank_mask:0xf\n" \
  "v_and_b32_dpp v50, v41, v6 row_newbcast:8 row_mask:0xf bank_mask:0xf\n" \
  "v_and_b32_dpp v51, v41, v7 row_newbcast:8 row_mask:0xf bank_mask:0xf\n" \
  "v_add_u32 v43, s80, v2\n" \
  "ds_read_b128 v[56:59], v43 offset:2048\n" \
  "ds_read_b128 v[60:63], v43 offset:3072\n" \
  "s_waitcnt lgkmcnt(1)\n" \
  "v_mfma_f32_16x16x32_bf16 v[24:27], v[48:51], v[56:59], 0\n" \
  "s_waitcnt lgkmcnt(0)\n" \
  "v_mfma_f32_16x16x32_bf16 v[28:31], v[48:51], v[60:63], 0\n" \
  "v_and_b32_dpp v44, v37, v4 row_newbcast:0 row_mask:0xf bank_mask:0xf\n" \
  "v_and_b32_dpp v45, v37, v5 row_newbcast:0 row_mask:0xf bank_mask:0xf\n" \
  "v_and_b32_dpp v46, v37, v6 row_newbcast:0 row_mask:0xf bank_mask:0xf\n" \
  "v_and_b32_dpp v47, v37, v7 row_newbcast:0 row_mask:0xf bank_mask:0xf\n" \
  "ds_read_b128 v[56:59], v2 offset:16384\n" \
  "ds_read_b128 v[60:63], v2 offset:17408\n" \
  "s_waitcnt lgkmcnt(1)\n" \
  "v_mfma_f32_16x16x32_bf16 v[24:27], v[44:47], v[56:59], v[24:27]\n" \
  "s_waitcnt lgkmcnt(0)\n" \
  "v_mfma_f32_16x16x32_bf16 v[28:31], v[44:47], v[60:63], v[28:31]\n" \
  "v_and_b32_dpp v48, v37, v4 row_newbcast:4 row_mask:0xf bank_mask:0xf\n" \
  "v_and_b32_dpp v49, v37, v5 row_newbcast:4 row_mask:0xf bank_mask:0xf\n" \
  "v_and_b32_dpp v50, v37, v6 row_newbcast:4 row_mask:0xf bank_mask:0xf\n" \
  "v_and_b32_dpp v51, v37, v7 row_newbcast:4 row_mask:0xf bank_mask:0xf\n" \
  "s_waitcnt vmcnt(11)\n" \
  "s_add_u32 m0, s67, 31488\n" \
  "s_nop 0\n" \
  "buffer_load_dwordx4 v1, s[40:43], s7 offen offset:256 nt lds\n" \
  "ds_read_b128 v[56:59], v2 offset:18432\n" \
  "ds_read_b128 v[60:63], v2 offset:19456\n" \
  "s_waitcnt lgkmcnt(1)\n" \
  "v_mfma_f32_16x16x32_bf16 v[24:27], v[48:51], v[56:59], v[24:27]\n" \
  "s_waitcnt lgkmcnt(0)\n" \
  "v_mfma_f32_16x16x32_bf16 v[28:31], v[48:51], v[60:63], v[28:31]\n" \
  "v_and_b32_dpp v44, v37, v4 row_newbcast:8 row_mask:0xf bank_mask:0xf\n" \
  "v_and_b32_dpp v45, v37, v5 row_newbcast:8 row_mask:0xf bank_mask:0xf\n" \
  "v_and_b32_dpp v46, v37, v6 row_newbcast:8 row_mask:0xf bank_mask:0xf\n" \
  "v_and_b32_dpp v47, v37, v7 row_newbcast:8 row_mask:0xf bank_mask:0xf\n" \
  "s_waitcnt vmcnt(11)\n" \
  "s_nop 1\n" \
  "s_call_b64 s[34:35], .LA_GATE\n" \
  "buffer_load_dwordx4 v[176:179], v1, s[40:43], s8 offen nt\n" \
  "s_waitcnt vmcnt(11)\n" \
  "buffer_load_dwordx4 v[180:183], v1, s[40:43], s8 offen offset:256 nt\n" \
  "ds_read_b128 v[56:59], v2 offset:20480\n" \
  "ds_read_b128 v[60:63], v2 offset:21504\n" \
  "s_waitcnt lgkmcnt(1)\n" \
  "v_mfma_f32_16x16x32_bf16 v[24:27], v[44:47], v[56:59], v[24:27]\n" \
  "s_waitcnt lgkmcnt(0)\n" \
  "v_mfma_f32_16x16x32_bf16 v[28:31], v[44:47], v[60:63], v[28:31]\n" \
  "v_and_b32_dpp v48, v37, v4 row_newbcast:12 row_mask:0xf bank_mask:0xf\n" \
  "v_and_b32_dpp v49, v37, v5 row_newbcast:12 row_mask:0xf bank_mask:0xf\n" \
  "v_and_b32_dpp v50, v37, v6 row_newbcast:12 row_mask:0xf bank_mask:0xf\n" \
  "v_and_b32_dpp v51, v37, v7 row_newbcast:12 row_mask:0xf bank_mask:0xf\n" \
  "s_waitcnt vmcnt(11)\n" \
  "s_nop 1\n" \
  "s_call_b64 s[34:35], .LA_GATE\n" \
  "buffer_load_dwordx4 v[184:187], v1, s[40:43], s9 offen nt\n" \
  "s_waitcnt vmcnt(11)\n" \
  "buffer_load_dwordx4 v[188:191], v1, s[40:43], s9 offen offset:256 nt\n" \
  "ds_read_b128 v[56:59], v2 offset:22528\n" \
  "ds_read_b128 v[60:63], v2 offset:23552\n" \
  "s_waitcnt lgkmcnt(1)\n" \
  "v_mfma_f32_16x16x32_bf16 v[24:27], v[48:51], v[56:59], v[24:27]\n" \
  "s_waitcnt lgkmcnt(0)\n" \
  "v_mfma_f32_16x16x32_bf16 v[28:31], v[48:51], v[60:63], v[28:31]\n" \
  "v_and_b32_dpp v44, v38, v4 row_newbcast:0 row_mask:0xf bank_mask:0xf\n" \
  "v_and_b32_dpp v45, v38, v5 row_newbcast:0 row_mask:0xf bank_mask:0xf\n" \
  "v_and_b32_dpp v46, v38, v6 row_newbcast:0 row_mask:0xf bank_mask:0xf\n" \
  "v_and_b32_dpp v47, v38, v7 row_newbcast:0 row_mask:0xf bank_mask:0xf\n" \
  "s_waitcnt vmcnt(11)\n" \
  "s_nop 1\n" \
  "s_call_b64 s[34:35], .LA_GATE\n" \
  "buffer_load_dwordx4 v[192:195], v1, s[40:43], s10 offen nt\n" \
  "s_waitcnt vmcnt(11)\n" \
  "buffer_load_dwordx4 v[196:199], v1, s[40:43], s10 offen offset:256 nt\n" \
  "ds_read_b128 v[56:59], v2 offset:24576\n" \
  "ds_read_b128 v[60:63], v2 offset:25600\n" \
  "s_waitcnt lgkmcnt(1)\n" \
  "v_mfma_f32_16x16x32_bf16 v[24:27], v[44:47], v[56:59], v[24:27]\n" \
  "s_waitcnt lgkmcnt(0)\n" \
  "v_mfma_f32_16x16x32_bf16 v[28:31], v[44:47], v[60:63], v[28:31]\n" \
  "v_and_b32_dpp v48, v38, v4 row_newbcast:4 row_mask:0xf bank_mask:0xf\n" \
  "v_and_b32_dpp v49, v38, v5 row_newbcast:4 row_mask:0xf bank_mask:0xf\n" \
  "v_and_b32_dpp v50, v38, v6 row_newbcast:4 row_mask:0xf bank_mask:0xf\n" \
  "v_and_b32_dpp v51, v38, v7 row_newbcast:4 row_mask:0xf bank_mask:0xf\n" \
  "s_waitcnt vmcnt(11)\n" \
  "s_nop 1\n" \
  "s_call_b64 s[34:35], .LA_GATE\n" \
  "buffer_load_dwordx4 v[200:203], v1, s[40:43], s11 offen nt\n" \
  "s_waitcnt vmcnt(11)\n" \
  "buffer_load_dwordx4 v[204:207], v1, s[40:43], s11 offen offset:256 nt\n" \
  "ds_read_b128 v[56:59], v2 offset:26624\n" \
  "ds_read_b128 v[60:63], v2 offset:27648\n" \
  "s_waitcnt lgkmcnt(1)\n" \
  "v_mfma_f32_16x16x32_bf16 v[24:27], v[48:51], v[56:59], v[24:27]\n" \
  "s_waitcnt lgkmcnt(0)\n" \
  "v_mfma_f32_16x16x32_bf16 v[28:31], v[48:51], v[60:63], v[28:31]\n" \
  "v_and_b32_dpp v44, v38, v4 row_newbcast:8 row_mask:0xf bank_mask:0xf\n" \
  "v_and_b32_dpp v45, v38, v5 row_newbcast:8 row_mask:0xf bank_mask:0xf\n" \
  "v_and_b32_dpp v46, v38, v6 row_newbcast:8 row_mask:0xf bank_mask:0xf\n" \
  "v_and_b32_dpp v47, v38, v7 row_newbcast:8 row_mask:0xf bank_mask:0xf\n" \
  "s_waitcnt vmcnt(11)\n" \
  "s_nop 1\n" \
  "s_call_b64 s[34:35], .LA_GATE\n" \
  "buffer_load_dwordx4 v[208:211], v1, s[40:43], s12 offen nt\n" \
  "s_waitcnt vmcnt(11)\n" \
  "buffer_load_dwordx4 v[212:215], v1, s[40:43], s12 offen offset:256 nt\n" \
  "ds_read_b128 v[56:59], v2 offset:28672\n" \
  "ds_read_b128 v[60:63], v2 offset:29696\n" \
  "s_waitcnt lgkmcnt(1)\n" \
  "v_mfma_f32_16x16x32_bf16 v[24:27], v[44:47], v[56:59], v[24:27]\n" \
  "s_waitcnt lgkmcnt(0)\n" \
  "v_mfma_f32_16x16x32_bf16 v[28:31], v[44:47], v[60:63], v[28:31]\n" \
  "v_and_b32_dpp v48, v38, v4 row_newbcast:12 row_mask:0xf bank_mask:0xf\n" \
  "v_and_b32_dpp v49, v38, v5 row_newbcast:12 row_mask:0xf bank_mask:0xf\n" \
  "v_and_b32_dpp v50, v38, v6 row_newbcast:12 row_mask:0xf bank_mask:0xf\n" \
  "v_and_b32_dpp v51, v38, v7 row_newbcast:12 row_mask:0xf bank_mask:0xf\n" \
  "s_waitcnt vmcnt(11)\n" \
  "s_nop 1\n" \
  "s_call_b64 s[34:35], .LA_GATE\n" \
  "buffer_load_dwordx4 v[216:219], v1, s[40:43], s13 offen nt\n" \
  "s_waitcnt vmcnt(11)\n" \
  "buffer_load_dwordx4 v[220:223], v1, s[40:43], s13 offen offset:256 nt\n" \
  "ds_read_b128 v[56:59], v2 offset:30720\n" \
  "ds_read_b128 v[60:63], v2 offset:31744\n" \
  "s_waitcnt lgkmcnt(1)\n" \
  "v_mfma_f32_16x16x32_bf16 v[24:27], v[48:51], v[56:59], v[24:27]\n" \
  "s_waitcnt lgkmcnt(0)\n" \
  "v_mfma_f32_16x16x32_bf16 v[28:31], v[48:51], v[60:63], v[28:31]\n" \
  "v_and_b32_dpp v44, v39, v4 row_newbcast:0 row_mask:0xf bank_mask:0xf\n" \
  "v_and_b32_dpp v45, v39, v5 row_newbcast:0 row_mask:0xf bank_mask:0xf\n" \
  "v_and_b32_dpp v46, v39, v6 row_newbcast:0 row_mask:0xf bank_mask:0xf\n" \
  "v_and_b32_dpp v47, v39, v7 row_newbcast:0 row_mask:0xf bank_mask:0xf\n" \
  "s_waitcnt vmcnt(11)\n" \
  "s_nop 0\n" \
  "v_mfma_f32_16x16x32_bf16 v[24:27], v[44:47], v[176:179], v[24:27]\n" \
  "s_nop 6\n" \
  "s_call_b64 s[34:35], .LA_GATE\n" \
  "buffer_load_dwordx4 v[224:227], v1, s[40:43], s14 offen nt\n" \
  "s_waitcnt vmcnt(11)\n" \
  "v_mfma_f32_16x16x32_bf16 v[28:31], v[44:47], v[180:183], v[28:31]\n" \
  "buffer_load_dwordx4 v[228:231], v1, s[40:43], s14 offen offset:256 nt\n" \
  "s_sub_u32 s59, s75, 16384\n" \
  "s_cmp_eq_u32 s72, 1\n" \
  "s_cselect_b32 s59, s59, s76\n" \
  "v_and_b32 v42, 0xc0, v0\n" \
  "v_and_b32 v43, 0xf0, v0\n" \
  "v_add_u32 v42, v42, v43\n" \
  "buffer_load_dwordx4 v[8:11], v42, s[52:55], s59 offen sc1\n" \
  "buffer_load_dwordx4 v[12:15], v42, s[52:55], s59 offen offset:512 sc1\n" \
  "v_and_b32_dpp v48, v39, v4 row_newbcast:4 row_mask:0xf bank_mask:0xf\n" \
  "v_and_b32_dpp v49, v39, v5 row_newbcast:4 row_mask:0xf bank_mask:0xf\n" \
  "v_and_b32_dpp v50, v39, v6 row_newbcast:4 row_mask:0xf bank_mask:0xf\n" \
  "v_and_b32_dpp v51, v39, v7 row_newbcast:4 row_mask:0xf bank_mask:0xf\n" \
  "s_waitcnt vmcnt(13)\n" \
  "s_nop 0\n" \
  "v_mfma_f32_16x16x32_bf16 v[24:27], v[48:51], v[184:187], v[24:27]\n" \
  "s_nop 6\n" \
  "s_call_b64 s[34:35], .LA_GATE\n" \
  "buffer_load_dwordx4 v[232:235], v1, s[40:43], s15 offen nt\n" \
  "s_waitcnt vmcnt(13)\n" \
  "v_mfma_f32_16x16x32_bf16 v[28:31], v[48:51], v[188:191], v[28:31]\n" \
  "buffer_load_dwordx4 v[236:239], v1, s[40:43], s15 offen offset:256 nt\n" \
  "v_and_b32_dpp v44, v39, v4 row_newbcast:8 row_mask:0xf bank_mask:0xf\n" \
  "v_and_b32_dpp v45, v39, v5 row_newbcast:8 row_mask:0xf bank_mask:0xf\n" \
  "v_and_b32_dpp v46, v39, v6 row_newbcast:8 row_mask:0xf bank_mask:0xf\n" \
  "v_and_b32_dpp v47, v39, v7 row_newbcast:8 row_mask:0xf bank_mask:0xf\n" \
  "s_waitcnt vmcnt(13)\n" \
  "s_nop 0\n" \
  "v_mfma_f32_16x16x32_bf16 v[24:27], v[44:47], v[192:195], v[24:27]\n" \
  "s_nop 6\n" \
  "s_call_b64 s[34:35], .LA_GATE\n" \
  "buffer_load_dwordx4 v[240:243], v1, s[40:43], s16 offen nt\n" \
  "s_waitcnt vmcnt(13)\n" \
  "v_mfma_f32_16x16x32_bf16 v[28:31], v[44:47], v[196:199], v[28:31]\n" \
  "buffer_load_dwordx4 v[244:247], v1, s[40:43], s16 offen offset:256 nt\n" \
  "v_and_b32_dpp v48, v39, v4 row_newbcast:12 row_mask:0xf bank_mask:0xf\n" \
  "v_and_b32_dpp v49, v39, v5 row_newbcast:12 row_mask:0xf bank_mask:0xf\n" \
  "v_and_b32_dpp v50, v39, v6 row_newbcast:12 row_mask:0xf bank_mask:0xf\n" \
  "v_and_b32_dpp v51, v39, v7 row_newbcast:12 row_mask:0xf bank_mask:0xf\n" \
  "s_waitcnt vmcnt(13)\n" \
  "s_nop 0\n" \
  "v_mfma_f32_16x16x32_bf16 v[24:27], v[48:51], v[200:203], v[24:27]\n" \
  "s_nop 6\n" \
  "s_call_b64 s[34:35], .LA_GATE\n" \
  "buffer_load_dwordx4 v[248:251], v1, s[40:43], s17 offen nt\n" \
  "s_waitcnt vmcnt(13)\n" \
  "v_mfma_f32_16x16x32_bf16 v[28:31], v[48:51], v[204:207], v[28:31]\n" \
  "buffer_load_dwordx4 v[252:255], v1, s[40:43], s17 offen offset:256 nt\n" \
  "s_lshl_b32 s84, s72, 1\n" \
  "s_add_u32 s84, s84, 1\n" \
  "s_nop 3\n" \
  "s_call_b64 s[94:95], .LA_GUARD\n" \
  "s_xor_b32 s73, s72, 1\n" \
  "s_cmp_lg_u32 s72, 0\n" \
  "s_cbranch_scc1 .LA_DEFER_B\n" \
  "s_waitcnt vmcnt(6)\n" \
  "v_max3_u32 v42, v8, v9, v10\n" \
  "v_max3_u32 v43, v11, v12, v13\n" \
  "v_max3_u32 v42, v42, v14, v15\n" \
  "v_max_u32 v42, v42, v43\n" \
  "v_cmp_eq_u32 vcc, -1, v42\n" \
  "s_nop 0\n" \
  "s_cbranch_vccnz .LA_DEFER_B\n" \
  "s_add_u32 s76, s76, 16384\n" \
  "v_mfma_f32_16x16x32_bf16 v[44:47], a[0:3], v[8:11], 0\n" \
  "v_add_u32 v43, s78, v2\n" \
  "v_mov_b32 v36, 0xff800000\n" \
  "v_mfma_f32_16x16x32_bf16 v[44:47], a[4:7], v[12:15], v[44:47]\n" \
  "ds_read_b128 v[56:59], v43\n" \
  "ds_read_b128 v[60:63], v43 offset:1024\n" \
  "v_mov_b32 v42, s63\n" \
  "v_mfma_f32_16x16x32_bf16 v[48:51], a[8:11], v[8:11], 0\n" \
  "v_mfma_f32_16x16x32_bf16 v[48:51], a[12:15], v[12:15], v[48:51]\n" \
  "v_mov_b32_dpp v36, v42 quad_perm:[0,1,2,3] row_mask:0xf bank_mask:0x4\n" \
  "v_mfma_f32_16x16x32_bf16 v[52:55], a[16:19], v[8:11], 0\n" \
  "s_nop 0\n" \
  "v_mov_b32_dpp v32, v44 quad_perm:[0,0,0,0] row_mask:0xf bank_mask:0x1\n" \
  "v_mov_b32_dpp v42, v45 quad_perm:[1,1,1,1] row_mask:0xf bank_mask:0x1\n" \
  "v_mfma_f32_16x16x32_bf16 v[52:55], a[20:23], v[12:15], v[52:55]\n" \
  "v_add_f32_dpp v32, v46, v32 quad_perm:[2,2,2,2] row_mask:0xf bank_mask:0x1\n" \
  "v_add_f32_dpp v42, v47, v42 quad_perm:[3,3,3,3] row_mask:0xf bank_mask:0x1\n" \
  "v_mfma_f32_16x16x32_bf16 v[44:47], a[24:27], v[8:11], 0\n" \
  "v_mov_b32_dpp v32, v48 quad_perm:[0,0,0,0] row_mask:0xf bank_mask:0x2\n" \
  "v_mov_b32_dpp v42, v49 quad_perm:[1,1,1,1] row_mask:0xf bank_mask:0x2\n" \
  "v_mfma_f32_16x16x32_bf16 v[44:47], a[28:31], v[12:15], v[44:47]\n" \
  "v_add_f32_dpp v32, v50, v32 quad_perm:[2,2,2,2] row_mask:0xf bank_mask:0x2\n" \
  "v_add_f32_dpp v42, v51, v42 quad_perm:[3,3,3,3] row_mask:0xf bank_mask:0x2\n" \
  "s_waitcnt lgkmcnt(0)\n" \
  "v_mfma_f32_16x16x32_bf16 v[48:51], a[32:35], v[8:11], 0\n" \
  "s_waitcnt vmcnt(13)\n" \
  "s_add_u32 s36, s36, 0x2000000\n" \
  "s_addc_u32 s37, s37, 0\n" \
  "s_mov_b64 s[44:45], s[36:37]\n" \
  "s_mov_b32 s46, s61\n" \
  "s_add_u32 m0, s65, 0\n" \
  "s_nop 0\n" \
  "buffer_load_dwordx4 v0, s[44:47], s58 offen nt lds\n" \
  "s_waitcnt vmcnt(13)\n" \
  "s_add_u32 m0, s65, 768\n" \
  "s_nop 0\n" \
  "buffer_load_dwordx4 v0, s[44:47], s58 offen offset:256 nt lds\n" \
  "s_waitcnt vmcnt(13)\n" \
  "s_add_u32 s33, s33, s27\n" \
  "buffer_load_dwordx4 a[0:3], v0, s[36:39], s0 offen nt\n" \
  "v_mov_b32_dpp v32, v52 quad_perm:[0,0,0,0] row_mask:0xf bank_mask:0x4\n" \
  "v_mov_b32_dpp v42, v53 quad_perm:[1,1,1,1] row_mask:0xf bank_mask:0x4\n" \
  "v_mfma_f32_16x16x32_bf16 v[48:51], a[36:39], v[12:15], v[48:51]\n" \
  "v_add_f32_dpp v32, v54, v32 quad_perm:[2,2,2,2] row_mask:0xf bank_mask:0x4\n" \
  "v_add_f32_dpp v42, v55, v42 quad_perm:[3,3,3,3] row_mask:0xf bank_mask:0x4\n" \
  "v_mfma_f32_16x16x32_bf16 v[52:55], a[40:43], v[8:11], 0\n" \
  "v_mov_b32_dpp v32, v44 quad_perm:[0,0,0,0] row_mask:0xf bank_mask:0x8\n" \
  "v_mov_b32_dpp v42, v45 quad_perm:[1,1,1,1] row_mask:0xf bank_mask:0x8\n" \
  "v_mfma_f32_16x16x32_bf16 v[52:55], a[44:47], v[12:15], v[52:55]\n" \
  "v_add_f32_dpp v32, v46, v32 quad_perm:[2,2,2,2] row_mask:0xf bank_mask:0x8\n" \
  "v_add_f32_dpp v42, v47, v42 quad_perm:[3,3,3,3] row_mask:0xf bank_mask:0x8\n" \
  "v_mfma_f32_16x16x32_bf16 v[44:47], a[48:51], v[8:11], 0\n" \
  "v_mov_b32_dpp v33, v48 quad_perm:[0,0,0,0] row_mask:0xf bank_mask:0x1\n" \
  "v_mov_b32_dpp v43, v49 quad_perm:[1,1,1,1] row_mask:0xf bank_mask:0x1\n" \
  "v_mfma_f32_16x16x32_bf16 v[44:47], a[52:55], v[12:15], v[44:47]\n" \
  "v_add_f32_dpp v33, v50, v33 quad_perm:[2,2,2,2] row_mask:0xf bank_mask:0x1\n" \
  "v_add_f32_dpp v43, v51, v43 quad_perm:[3,3,3,3] row_mask:0xf bank_mask:0x1\n" \
  "v_add_f32 v32, v32, v42\n" \
  "v_mfma_f32_16x16x32_bf16 v[48:51], a[56:59], v[8:11], 0\n" \
  "v_mov_b32_dpp v33, v52 quad_perm:[0,0,0,0] row_mask:0xf bank_mask:0x2\n" \
  "v_mov_b32_dpp v43, v53 quad_perm:[1,1,1,1] row_mask:0xf bank_mask:0x2\n" \
  "v_mfma_f32_16x16x32_bf16 v[48:51], a[60:63], v[12:15], v[48:51]\n" \
  "v_add_f32_dpp v33, v54, v33 quad_perm:[2,2,2,2] row_mask:0xf bank_mask:0x2\n" \
  "v_add_f32_dpp v43, v55, v43 quad_perm:[3,3,3,3] row_mask:0xf bank_mask:0x2\n" \
  "v_mfma_f32_16x16x32_bf16 v[52:55], a[64:67], v[8:11], 0\n" \
  "v_mov_b32_dpp v33, v44 quad_perm:[0,0,0,0] row_mask:0xf bank_mask:0x4\n" \
  "v_mov_b32_dpp v43, v45 quad_perm:[1,1,1,1] row_mask:0xf bank_mask:0x4\n" \
  "v_mfma_f32_16x16x32_bf16 v[52:55], a[68:71], v[12:15], v[52:55]\n" \
  "v_add_f32_dpp v33, v46, v33 quad_perm:[2,2,2,2] row_mask:0xf bank_mask:0x4\n" \
  "v_add_f32_dpp v43, v47, v43 quad_perm:[3,3,3,3] row_mask:0xf bank_mask:0x4\n" \
  "v_mfma_f32_16x16x32_bf16 v[44:47], a[72:75], v[8:11], 0\n" \
  "v_mov_b32_dpp v33, v48 quad_perm:[0,0,0,0] row_mask:0xf bank_mask:0x8\n" \
  "v_mov_b32_dpp v43, v49 quad_perm:[1,1,1,1] row_mask:0xf bank_mask:0x8\n" \
  "v_mfma_f32_16x16x32_bf16 v[44:47], a[76:79], v[12:15], v[44:47]\n" \
  "s_waitcnt vmcnt(13)\n" \
  "buffer_load_dwordx4 a[4:7], v0, s[36:39], s0 offen offset:256 nt\n" \
  "v_add_f32_dpp v33, v50, v33 quad_perm:[2,2,2,2] row_mask:0xf bank_mask:0x8\n" \
  "v_add_f32_dpp v43, v51, v43 quad_perm:[3,3,3,3] row_mask:0xf bank_mask:0x8\n" \
  "v_mfma_f32_16x16x32_bf16 v[48:51], a[80:83], v[8:11], 0\n" \
  "v_mov_b32_dpp v34, v52 quad_perm:[0,0,0,0] row_mask:0xf bank_mask:0x1\n" \
  "v_mov_b32_dpp v42, v53 quad_perm:[1,1,1,1] row_mask:0xf bank_mask:0x1\n" \
  "v_mfma_f32_16x16x32_bf16 v[48:51], a[84:87], v[12:15], v[48:51]\n" \
  "v_add_f32_dpp v34, v54, v34 quad_perm:[2,2,2,2] row_mask:0xf bank_mask:0x1\n" \
  "v_add_f32_dpp v42, v55, v42 quad_perm:[3,3,3,3] row_mask:0xf bank_mask:0x1\n" \
  "v_add_f32 v33, v33, v43\n" \
  "v_mfma_f32_16x16x32_bf16 v[52:55], a[88:91], v[8:11], 0\n" \
  "v_mov_b32_dpp v34, v44 quad_perm:[0,0,0,0] row_mask:0xf bank_mask:0x2\n" \
  "v_mov_b32_dpp v42, v45 quad_perm:[1,1,1,1] row_mask:0xf bank_mask:0x2\n" \
  "v_mfma_f32_16x16x32_bf16 v[52:55], a[92:95], v[12:15], v[52:55]\n" \
  "v_add_f32_dpp v34, v46, v34 quad_perm:[2,2,2,2] row_mask:0xf bank_mask:0x2\n" \
  "v_add_f32_dpp v42, v47, v42 quad_perm:[3,3,3,3] row_mask:0xf bank_mask:0x2\n" \
  "v_mfma_f32_16x16x32_bf16 v[44:47], a[96:99], v[8:11], 0\n" \
  "v_mov_b32_dpp v34, v48 quad_perm:[0,0,0,0] row_mask:0xf bank_mask:0x4\n" \
  "v_mov_b32_dpp v42, v49 quad_perm:[1,1,1,1] row_mask:0xf bank_mask:0x4\n" \
  "v_mfma_f32_16x16x32_bf16 v[44:47], a[100:103], v[12:15], v[44:47]\n" \
  "v_add_f32_dpp v34, v50, v34 quad_perm:[2,2,2,2] row_mask:0xf bank_mask:0x4\n" \
  "v_add_f32_dpp v42, v51, v42 quad_perm:[3,3,3,3] row_mask:0xf bank_mask:0x4\n" \
  "v_mfma_f32_16x16x32_bf16 v[48:51], a[104:107], v[8:11], 0\n" \
  "v_mov_b32_dpp v34, v52 quad_perm:[0,0,0,0] row_mask:0xf bank_mask:0x8\n" \
  "v_mov_b32_dpp v42, v53 quad_perm:[1,1,1,1] row_mask:0xf bank_mask:0x8\n" \
  "v_mfma_f32_16x16x32_bf16 v[48:51], a[108:111], v[12:15], v[48:51]\n" \
  "v_add_f32_dpp v34, v54, v34 quad_perm:[2,2,2,2] row_mask:0xf bank_mask:0x8\n" \
  "v_add_f32_dpp v42, v55, v42 quad_perm:[3,3,3,3] row_mask:0xf bank_mask:0x8\n" \
  "v_mfma_f32_16x16x32_bf16 v[52:55], a[112:115], v[8:11], 0\n" \
  "v_mov_b32_dpp v35, v44 quad_perm:[0,0,0,0] row_mask:0xf bank_mask:0x1\n" \
  "v_mov_b32_dpp v43, v45 quad_perm:[1,1,1,1] row_mask:0xf bank_mask:0x1\n" \
  "v_mfma_f32_16x16x32_bf16 v[52:55], a[116:119], v[12:15], v[52:55]\n" \
  "v_add_f32_dpp v35, v46, v35 quad_perm:[2,2,2,2] row_mask:0xf bank_mask:0x1\n" \
  "v_add_f32_dpp v43, v47, v43 quad_perm:[3,3,3,3] row_mask:0xf bank_mask:0x1\n" \
  "v_add_f32 v34, v34, v42\n" \
  "v_mfma_f32_16x16x32_bf16 v[44:47], a[120:123], v[8:11], 0\n" \
  "s_waitcnt vmcnt(13)\n" \
  "s_add_u32 s33, s33, s27\n" \
  "buffer_load_dwordx4 a[8:11], v0, s[36:39], s1 offen nt\n" \
  "v_mov_b32_dpp v35, v48 quad_perm:[0,0,0,0] row_mask:0xf bank_mask:0x2\n" \
  "v_mov_b32_dpp v43, v49 quad_perm:[1,1,1,1] row_mask:0xf bank_mask:0x2\n" \
  "v_mfma_f32_16x16x32_bf16 v[44:47], a[124:127], v[12:15], v[44:47]\n" \
  "v_add_f32_dpp v35, v50, v35 quad_perm:[2,2,2,2] row_mask:0xf bank_mask:0x2\n" \
  "v_add_f32_dpp v43, v51, v43 quad_perm:[3,3,3,3] row_mask:0xf bank_mask:0x2\n" \
  "v_mfma_f32_16x16x32_bf16 v[48:51], a[128:131], v[8:11], 0\n" \
  "v_mov_b32_dpp v35, v52 quad_perm:[0,0,0,0] row_mask:0xf bank_mask:0x4\n" \
  "v_mov_b32_dpp v43, v53 quad_perm:[1,1,1,1] row_mask:0xf bank_mask:0x4\n" \
  "v_mfma_f32_16x16x32_bf16 v[48:51], a[132:135], v[12:15], v[48:51]\n" \
  "v_add_f32_dpp v35, v54, v35 quad_perm:[2,2,2,2] row_mask:0xf bank_mask:0x4\n" \
  "v_add_f32_dpp v43, v55, v43 quad_perm:[3,3,3,3] row_mask:0xf bank_mask:0x4\n" \
  "v_mfma_f32_16x16x32_bf16 v[52:55], a[136:139], v[8:11], 0\n" \
  "v_mov_b32_dpp v35, v44 quad_perm:[0,0,0,0] row_mask:0xf bank_mask:0x8\n" \
  "v_mov_b32_dpp v43, v45 quad_perm:[1,1,1,1] row_mask:0xf bank_mask:0x8\n" \
  "v_mfma_f32_16x16x32_bf16 v[52:55], a[140:143], v[12:15], v[52:55]\n" \
  "v_add_f32_dpp v35, v46, v35 quad_perm:[2,2,2,2] row_mask:0xf bank_mask:0x8\n" \
  "v_add_f32_dpp v43, v47, v43 quad_perm:[3,3,3,3] row_mask:0xf bank_mask:0x8\n" \
  "v_mfma_f32_16x16x32_bf16 v[44:47], v[56:59], v[8:11], 0\n" \
  "v_mov_b32_dpp v36, v48 quad_perm:[0,0,0,0] row_mask:0xf bank_mask:0x1\n" \
  "v_mov_b32_dpp v42, v49 quad_perm:[1,1,1,1] row_mask:0xf bank_mask:0x1\n" \
  "v_mfma_f32_16x16x32_bf16 v[44:47], v[60:63], v[12:15], v[44:47]\n" \
  "v_add_f32_dpp v36, v50, v36 quad_perm:[2,2,2,2] row_mask:0xf bank_mask:0x1\n" \
  "v_add_f32_dpp v42, v51, v42 quad_perm:[3,3,3,3] row_mask:0xf bank_mask:0x1\n" \
  "v_add_f32 v35, v35, v43\n" \
  "v_mov_b32_dpp v36, v52 quad_perm:[0,0,0,0] row_mask:0xf bank_mask:0x2\n" \
  "v_mov_b32_dpp v42, v53 quad_perm:[1,1,1,1] row_mask:0xf bank_mask:0x2\n" \
  "s_nop 0\n" \
  "v_add_f32_dpp v36, v54, v36 quad_perm:[2,2,2,2] row_mask:0xf bank_mask:0x2\n" \
  "v_add_f32_dpp v42, v55, v42 quad_perm:[3,3,3,3] row_mask:0xf bank_mask:0x2\n" \
  "s_nop 0\n" \
  "v_add_f32_dpp v36, v44, v36 quad_perm:[0,0,0,0] row_mask:0xf bank_mask:0x4\n" \
  "v_mov_b32_dpp v42, v45 quad_perm:[1,1,1,1] row_mask:0xf bank_mask:0x4\n" \
  "s_nop 0\n" \
  "v_add_f32_dpp v36, v46, v36 quad_perm:[2,2,2,2] row_mask:0xf bank_mask:0x4\n" \
  "v_add_f32_dpp v42, v47, v42 quad_perm:[3,3,3,3] row_mask:0xf bank_mask:0x4\n" \
  "v_add_f32 v36, v36, v42\n" \
  "v_add_u32 v43, s78, v2\n" \
  "v_max3_f32 v42, v32, v33, v34\n" \
  "v_max3_f32 v42, v42, v35, v36\n" \
  "ds_read_b128 a[136:139], v43 offset:2048\n" \
  "ds_read_b128 a[140:143], v43 offset:3072\n" \
  "v_max_f32_dpp v42, v42, v42 row_ror:4 row_mask:0xf bank_mask:0xf\n" \
  "ds_read_b128 a[72:75], v2 offset:0\n" \
  "ds_read_b128 a[76:79], v2 offset:1024\n" \
  "v_max_f32_dpp v42, v42, v42 row_ror:8 row_mask:0xf bank_mask:0xf\n" \
  "ds_read_b128 a[80:83], v2 offset:2048\n" \
  "ds_read_b128 a[84:87], v2 offset:3072\n" \
  "v_max_f32_dpp v42, v42, v42 row_bcast:15 row_mask:0xa bank_mask:0xf\n" \
  "ds_read_b128 a[88:91], v2 offset:4096\n" \
  "ds_read_b128 a[92:95], v2 offset:5120\n" \
  "v_max_f32_dpp v42, v42, v42 row_bcast:31 row_mask:0xc bank_mask:0xf\n" \
  "ds_read_b128 a[96:99], v2 offset:6144\n" \
  "ds_read_b128 a[100:103], v2 offset:7168\n" \
  "v_readlane_b32 s68, v42, 63\n" \
  "ds_read_b128 a[104:107], v2 offset:8192\n" \
  "ds_read_b128 a[108:111], v2 offset:9216\n" \
  "v_subrev_f32 v32, s68, v32\n" \
  "v_subrev_f32 v33, s68, v33\n" \
  "v_subrev_f32 v34, s68, v34\n" \
  "v_subrev_f32 v35, s68, v35\n" \
  "v_subrev_f32 v36, s68, v36\n" \
  "v_exp_f32 v32, v32\n" \
  "v_exp_f32 v33, v33\n" \
  "v_exp_f32 v34, v34\n" \
  "v_exp_f32 v35, v35\n" \
  "v_exp_f32 v36, v36\n" \
  "v_add_f32 v8, v32, v33\n" \
  "v_add_f32 v9, v34, v35\n" \
  "v_cvt_pk_bf16_f32 v32, v32, v32\n" \
  "v_cvt_pk_bf16_f32 v33, v33, v33\n" \
  "v_cvt_pk_bf16_f32 v34, v34, v34\n" \
  "v_cvt_pk_bf16_f32 v35, v35, v35\n" \
  "v_add_f32 v9, v9, v36\n" \
  "v_add_f32 v8, v8, v9\n" \
  "v_cvt_pk_bf16_f32 v36, v36, v36\n" \
  "ds_read_b128 a[112:115], v2 offset:10240\n" \
  "ds_read_b128 a[116:119], v2 offset:11264\n" \
  "ds_read_b128 a[120:123], v2 offset:12288\n" \
  "ds_read_b128 a[124:127], v2 offset:13312\n" \
  "ds_read_b128 a[128:131], v2 offset:14336\n" \
  "ds_read_b128 a[132:135], v2 offset:15360\n" \
  "s_waitcnt vmcnt(13)\n" \
  "buffer_load_dwordx4 a[12:15], v0, s[36:39], s1 offen offset:256 nt\n" \
  ".LA_FAST_B:\n" \
  "v_and_b32_dpp v44, v40, v4 row_newbcast:0 row_mask:0xf bank_mask:0xf\n" \
  "v_and_b32_dpp v45, v40, v5 row_newbcast:0 row_mask:0xf bank_mask:0xf\n" \
  "v_and_b32_dpp v46, v40, v6 row_newbcast:0 row_mask:0xf bank_mask:0xf\n" \
  "v_and_b32_dpp v47, v40, v7 row_newbcast:0 row_mask:0xf bank_mask:0xf\n" \
  "s_nop 1\n" \
  "v_mfma_f32_16x16x32_bf16 v[24:27], v[44:47], v[208:211], v[24:27]\n" \
  "v_mfma_f32_16x16x32_bf16 v[28:31], v[44:47], v[212:215], v[28:31]\n" \
  "v_and_b32_dpp v48, v40, v4 row_newbcast:4 row_mask:0xf bank_mask:0xf\n" \
  "v_and_b32_dpp v49, v40, v5 row_newbcast:4 row_mask:0xf bank_mask:0xf\n" \
  "v_and_b32_dpp v50, v40, v6 row_newbcast:4 row_mask:0xf bank_mask:0xf\n" \
  "v_and_b32_dpp v51, v40, v7 row_newbcast:4 row_mask:0xf bank_mask:0xf\n" \
  "s_nop 1\n" \
  "v_mfma_f32_16x16x32_bf16 v[24:27], v[48:51], v[216:219], v[24:27]\n" \
  "v_mfma_f32_16x16x32_bf16 v[28:31], v[48:51], v[220:223], v[28:31]\n" \
  "v_and_b32_dpp v44, v40, v4 row_newbcast:8 row_mask:0xf bank_mask:0xf\n" \
  "v_and_b32_dpp v45, v40, v5 row_newbcast:8 row_mask:0xf bank_mask:0xf\n" \
  "v_and_b32_dpp v46, v40, v6 row_newbcast:8 row_mask:0xf bank_mask:0xf\n" \
  "v_and_b32_dpp v47, v40, v7 row_newbcast:8 row_mask:0xf bank_mask:0xf\n" \
  "s_nop 1\n" \
  "v_mfma_f32_16x16x32_bf16 v[24:27], v[44:47], v[224:227], v[24:27]\n" \
  "v_mfma_f32_16x16x32_bf16 v[28:31], v[44:47], v[228:231], v[28:31]\n" \
  "v_and_b32_dpp v48, v40, v4 row_newbcast:12 row_mask:0xf bank_mask:0xf\n" \
  "v_and_b32_dpp v49, v40, v5 row_newbcast:12 row_mask:0xf bank_mask:0xf\n" \
  "v_and_b32_dpp v50, v40, v6 row_newbcast:12 row_mask:0xf bank_mask:0xf\n" \
  "v_and_b32_dpp v51, v40, v7 row_newbcast:12 row_mask:0xf bank_mask:0xf\n" \
  "s_waitcnt vmcnt(11)\n" \
  "s_nop 0\n" \
  "v_mfma_f32_16x16x32_bf16 v[24:27], v[48:51], v[232:235], v[24:27]\n" \
  "s_waitcnt vmcnt(10)\n" \
  "v_mfma_f32_16x16x32_bf16 v[28:31], v[48:51], v[236:239], v[28:31]\n" \
  "v_and_b32_dpp v44, v41, v4 row_newbcast:0 row_mask:0xf bank_mask:0xf\n" \
  "v_and_b32_dpp v45, v41, v5 row_newbcast:0 row_mask:0xf bank_mask:0xf\n" \
  "v_and_b32_dpp v46, v41, v6 row_newbcast:0 row_mask:0xf bank_mask:0xf\n" \
  "v_and_b32_dpp v47, v41, v7 row_newbcast:0 row_mask:0xf bank_mask:0xf\n" \
  "s_waitcnt vmcnt(9)\n" \
  "s_nop 0\n" \
  "v_mfma_f32_16x16x32_bf16 v[24:27], v[44:47], v[240:243], v[24:27]\n" \
  "s_waitcnt vmcnt(8)\n" \
  "v_mfma_f32_16x16x32_bf16 v[28:31], v[44:47], v[244:247], v[28:31]\n" \
  "s_add_u32 s33, s33, s27\n" \
  "buffer_load_dwordx4 a[16:19], v0, s[36:39], s2 offen nt\n" \
  "v_and_b32_dpp v48, v41, v4 row_newbcast:4 row_mask:0xf bank_mask:0xf\n" \
  "v_and_b32_dpp v49, v41, v5 row_newbcast:4 row_mask:0xf bank_mask:0xf\n" \
  "v_and_b32_dpp v50, v41, v6 row_newbcast:4 row_mask:0xf bank_mask:0xf\n" \
  "v_and_b32_dpp v51, v41, v7 row_newbcast:4 row_mask:0xf bank_mask:0xf\n" \
  "s_waitcnt vmcnt(8)\n" \
  "s_nop 0\n" \
  "v_mfma_f32_16x16x32_bf16 v[24:27], v[48:51], v[248:251], v[24:27]\n" \
  "s_waitcnt vmcnt(7)\n" \
  "v_mfma_f32_16x16x32_bf16 v[28:31], v[48:51], v[252:255], v[28:31]\n" \
  "s_mov_b32 s81, 151552\n" \
  "s_mov_b32 s82, s77\n" \
  "s_add_u32 s77, s77, 143360\n" \
  "s_add_u32 s59, s87, 151552\n" \
  "s_sub_u32 s85, 151552, s67\n" \
  "v_add_u32 v42, 151552, v3\n" \
  "v_and_b32 v43, 0x30, v2\n" \
  "v_add_u32 v63, s85, v2\n" \
  "v_mov_b32 v52, 1\n" \
  "v_add_u32 v43, 151552, v43\n" \
  "v_mov_b32 v48, s69\n" \
  "v_mov_b32 v49, s71\n" \
  "v_mov_b32 v50, s59\n" \
  "v_mov_b32 v51, s81\n" \
  "v_cndmask_b32 v44, v24, v28, s[88:89]\n" \
  "v_cndmask_b32 v45, v25, v29, s[88:89]\n" \
  "v_cndmask_b32 v46, v26, v30, s[88:89]\n" \
  "v_cndmask_b32 v47, v27, v31, s[88:89]\n" \
  "s_call_b64 s[94:95], .LA_MERGE\n" \
  "buffer_load_dwordx4 a[20:23], v0, s[36:39], s2 offen offset:256 nt\n" \
  "s_add_u32 s33, s33, s27\n" \
  "buffer_load_dwordx4 a[24:27], v0, s[36:39], s3 offen nt\n" \
  "v_and_b32_dpp v44, v34, v4 row_newbcast:0 row_mask:0xf bank_mask:0xf\n" \
  "v_and_b32_dpp v45, v34, v5 row_newbcast:0 row_mask:0xf bank_mask:0xf\n" \
  "v_and_b32_dpp v46, v34, v6 row_newbcast:0 row_mask:0xf bank_mask:0xf\n" \
  "v_and_b32_dpp v47, v34, v7 row_newbcast:0 row_mask:0xf bank_mask:0xf\n" \
  "v_and_b32_dpp v48, v34, v4 row_newbcast:4 row_mask:0xf bank_mask:0xf\n" \
  "v_and_b32_dpp v49, v34, v5 row_newbcast:4 row_mask:0xf bank_mask:0xf\n" \
  "v_mfma_f32_16x16x32_bf16 v[16:19], v[44:47], a[144:147], 0\n" \
  "v_and_b32_dpp v50, v34, v6 row_newbcast:4 row_mask:0xf bank_mask:0xf\n" \
  "v_and_b32_dpp v51, v34, v7 row_newbcast:4 row_mask:0xf bank_mask:0xf\n" \
  "v_add_f32_dpp v8, v8, v8 row_ror:4 row_mask:0xf bank_mask:0xf\n" \
  "v_mfma_f32_16x16x32_bf16 v[20:23], v[44:47], a[148:151], 0\n" \
  "v_and_b32_dpp v44, v34, v4 row_newbcast:8 row_mask:0xf bank_mask:0xf\n" \
  "v_and_b32_dpp v45, v34, v5 row_newbcast:8 row_mask:0xf bank_mask:0xf\n" \
  "v_mfma_f32_16x16x32_bf16 v[16:19], v[48:51], a[152:155], v[16:19]\n" \
  "v_and_b32_dpp v46, v34, v6 row_newbcast:8 row_mask:0xf bank_mask:0xf\n" \
  "v_and_b32_dpp v47, v34, v7 row_newbcast:8 row_mask:0xf bank_mask:0xf\n" \
  "v_add_f32_dpp v8, v8, v8 row_ror:8 row_mask:0xf bank_mask:0xf\n" \
  "v_mfma_f32_16x16x32_bf16 v[20:23], v[48:51], a[156:159], v[20:23]\n" \
  "buffer_load_dwordx4 a[28:31], v0, s[36:39], s3 offen offset:256 nt\n" \
  "v_and_b32_dpp v48, v34, v4 row_newbcast:12 row_mask:0xf bank_mask:0xf\n" \
  "v_and_b32_dpp v49, v34, v5 row_newbcast:12 row_mask:0xf bank_mask:0xf\n" \
  "v_mfma_f32_16x16x32_bf16 v[16:19], v[44:47], a[160:163], v[16:19]\n" \
  "v_and_b32_dpp v50, v34, v6 row_newbcast:12 row_mask:0xf bank_mask:0xf\n" \
  "v_and_b32_dpp v51, v34, v7 row_newbcast:12 row_mask:0xf bank_mask:0xf\n" \
  "v_add_f32_dpp v8, v8, v8 row_bcast:15 row_mask:0xa bank_mask:0xf\n" \
  "v_mfma_f32_16x16x32_bf16 v[20:23], v[44:47], a[164:167], v[20:23]\n" \
  "v_and_b32_dpp v44, v35, v4 row_newbcast:0 row_mask:0xf bank_mask:0xf\n" \
  "v_and_b32_dpp v45, v35, v5 row_newbcast:0 row_mask:0xf bank_mask:0xf\n" \
  "v_mfma_f32_16x16x32_bf16 v[16:19], v[48:51], a[168:171], v[16:19]\n" \
  "v_and_b32_dpp v46, v35, v6 row_newbcast:0 row_mask:0xf bank_mask:0xf\n" \
  "v_and_b32_dpp v47, v35, v7 row_newbcast:0 row_mask:0xf bank_mask:0xf\n" \
  "v_add_f32_dpp v8, v8, v8 row_bcast:31 row_mask:0xc bank_mask:0xf\n" \
  "v_mfma_f32_16x16x32_bf16 v[20:23], v[48:51], a[172:175], v[20:23]\n" \
  "v_and_b32_dpp v48, v35, v4 row_newbcast:4 row_mask:0xf bank_mask:0xf\n" \
  "v_and_b32_dpp v49, v35, v5 row_newbcast:4 row_mask:0xf bank_mask:0xf\n" \
  "v_mfma_f32_16x16x32_bf16 v[16:19], v[44:47], a[176:179], v[16:19]\n" \
  "v_and_b32_dpp v50, v35, v6 row_newbcast:4 row_mask:0xf bank_mask:0xf\n" \
  "v_and_b32_dpp v51, v35, v7 row_newbcast:4 row_mask:0xf bank_mask:0xf\n" \
  "v_readlane_b32 s70, v8, 63\n" \
  "v_mfma_f32_16x16x32_bf16 v[20:23], v[44:47], a[180:183], v[20:23]\n" \
  "v_and_b32_dpp v44, v35, v4 row_newbcast:8 row_mask:0xf bank_mask:0xf\n" \
  "v_and_b32_dpp v45, v35, v5 row_newbcast:8 row_mask:0xf bank_mask:0xf\n" \
  "v_mfma_f32_16x16x32_bf16 v[16:19], v[48:51], a[184:187], v[16:19]\n" \
  "v_and_b32_dpp v46, v35, v6 row_newbcast:8 row_mask:0xf bank_mask:0xf\n" \
  "v_and_b32_dpp v47, v35, v7 row_newbcast:8 row_mask:0xf bank_mask:0xf\n" \
  "v_mfma_f32_16x16x32_bf16 v[20:23], v[48:51], a[188:191], v[20:23]\n" \
  "v_and_b32_dpp v48, v35, v4 row_newbcast:12 row_mask:0xf bank_mask:0xf\n" \
  "v_and_b32_dpp v49, v35, v5 row_newbcast:12 row_mask:0xf bank_mask:0xf\n" \
  "v_mfma_f32_16x16x32_bf16 v[16:19], v[44:47], a[192:195], v[16:19]\n" \
  "v_and_b32_dpp v50, v35, v6 row_newbcast:12 row_mask:0xf bank_mask:0xf\n" \
  "v_and_b32_dpp v51, v35, v7 row_newbcast:12 row_mask:0xf bank_mask:0xf\n" \
  "v_mfma_f32_16x16x32_bf16 v[20:23], v[44:47], a[196:199], v[20:23]\n" \
  "v_and_b32_dpp v44, v36, v4 row_newbcast:0 row_mask:0xf bank_mask:0xf\n" \
  "v_and_b32_dpp v45, v36, v5 row_newbcast:0 row_mask:0xf bank_mask:0xf\n" \
  "v_mfma_f32_16x16x32_bf16 v[16:19], v[48:51], a[200:203], v[16:19]\n" \
  "s_add_u32 s33, s33, s27\n" \
  "buffer_load_dwordx4 a[32:35], v0, s[36:39], s4 offen nt\n" \
  "v_and_b32_dpp v46, v36, v6 row_newbcast:0 row_mask:0xf bank_mask:0xf\n" \
  "v_and_b32_dpp v47, v36, v7 row_newbcast:0 row_mask:0xf bank_mask:0xf\n" \
  "v_mfma_f32_16x16x32_bf16 v[20:23], v[48:51], a[204:207], v[20:23]\n" \
  "v_and_b32_dpp v48, v36, v4 row_newbcast:4 row_mask:0xf bank_mask:0xf\n" \
  "v_and_b32_dpp v49, v36, v5 row_newbcast:4 row_mask:0xf bank_mask:0xf\n" \
  "v_mfma_f32_16x16x32_bf16 v[16:19], v[44:47], a[208:211], v[16:19]\n" \
  "v_and_b32_dpp v50, v36, v6 row_newbcast:4 row_mask:0xf bank_mask:0xf\n" \
  "v_and_b32_dpp v51, v36, v7 row_newbcast:4 row_mask:0xf bank_mask:0xf\n" \
  "v_mfma_f32_16x16x32_bf16 v[20:23], v[44:47], a[212:215], v[20:23]\n" \
  "v_and_b32_dpp v44, v36, v4 row_newbcast:8 row_mask:0xf bank_mask:0xf\n" \
  "v_and_b32_dpp v45, v36, v5 row_newbcast:8 row_mask:0xf bank_mask:0xf\n" \
  "v_mfma_f32_16x16x32_bf16 v[16:19], v[48:51], a[216:219], v[16:19]\n" \
  "v_and_b32_dpp v46, v36, v6 row_newbcast:8 row_mask:0xf bank_mask:0xf\n" \
  "v_and_b32_dpp v47, v36, v7 row_newbcast:8 row_mask:0xf bank_mask:0xf\n" \
  "v_mfma_f32_16x16x32_bf16 v[20:23], v[48:51], a[220:223], v[20:23]\n" \
  "v_and_b32_dpp v48, v32, v4 row_newbcast:0 row_mask:0xf bank_mask:0xf\n" \
  "v_and_b32_dpp v49, v32, v5 row_newbcast:0 row_mask:0xf bank_mask:0xf\n" \
  "s_waitcnt lgkmcnt(0)\n" \
  "v_mfma_f32_16x16x32_bf16 v[16:19], v[44:47], a[136:139], v[16:19]\n" \
  "v_and_b32_dpp v50, v32, v6 row_newbcast:0 row_mask:0xf bank_mask:0xf\n" \
  "v_and_b32_dpp v51, v32, v7 row_newbcast:0 row_mask:0xf bank_mask:0xf\n" \
  "v_mfma_f32_16x16x32_bf16 v[20:23], v[44:47], a[140:143], v[20:23]\n" \
  "v_and_b32_dpp v44, v32, v4 row_newbcast:4 row_mask:0xf bank_mask:0xf\n" \
  "v_and_b32_dpp v45, v32, v5 row_newbcast:4 row_mask:0xf bank_mask:0xf\n" \
  "v_mfma_f32_16x16x32_bf16 v[16:19], v[48:51], a[72:75], v[16:19]\n" \
  "v_and_b32_dpp v46, v32, v6 row_newbcast:4 row_mask:0xf bank_mask:0xf\n" \
  "v_and_b32_dpp v47, v32, v7 row_newbcast:4 row_mask:0xf bank_mask:0xf\n" \
  "v_mfma_f32_16x16x32_bf16 v[20:23], v[48:51], a[76:79], v[20:23]\n" \
  "v_and_b32_dpp v48, v32, v4 row_newbcast:8 row_mask:0xf bank_mask:0xf\n" \
  "v_and_b32_dpp v49, v32, v5 row_newbcast:8 row_mask:0xf bank_mask:0xf\n" \
  "v_mfma_f32_16x16x32_bf16 v[16:19], v[44:47], a[80:83], v[16:19]\n" \
  "v_and_b32_dpp v50, v32, v6 row_newbcast:8 row_mask:0xf bank_mask:0xf\n" \
  "v_and_b32_dpp v51, v32, v7 row_newbcast:8 row_mask:0xf bank_mask:0xf\n" \
  "v_mfma_f32_16x16x32_bf16 v[20:23], v[44:47], a[84:87], v[20:23]\n" \
  "buffer_load_dwordx4 a[36:39], v0, s[36:39], s4 offen offset:256 nt\n" \
  "v_and_b32_dpp v44, v32, v4 row_newbcast:12 row_mask:0xf bank_mask:0xf\n" \
  "v_and_b32_dpp v45, v32, v5 row_newbcast:12 row_mask:0xf bank_mask:0xf\n" \
  "v_mfma_f32_16x16x32_bf16 v[16:19], v[48:51], a[88:91], v[16:19]\n" \
  "v_and_b32_dpp v46, v32, v6 row_newbcast:12 row_mask:0xf bank_mask:0xf\n" \
  "v_and_b32_dpp v47, v32, v7 row_newbcast:12 row_mask:0xf bank_mask:0xf\n" \
  "v_mfma_f32_16x16x32_bf16 v[20:23], v[48:51], a[92:95], v[20:23]\n" \
  "v_and_b32_dpp v48, v33, v4 row_newbcast:0 row_mask:0xf bank_mask:0xf\n" \
  "v_and_b32_dpp v49, v33, v5 row_newbcast:0 row_mask:0xf bank_mask:0xf\n" \
  "v_mfma_f32_16x16x32_bf16 v[16:19], v[44:47], a[96:99], v[16:19]\n" \
  "v_and_b32_dpp v50, v33, v6 row_newbcast:0 row_mask:0xf bank_mask:0xf\n" \
  "v_and_b32_dpp v51, v33, v7 row_newbcast:0 row_mask:0xf bank_mask:0xf\n" \
  "v_mfma_f32_16x16x32_bf16 v[20:23], v[44:47], a[100:103], v[20:23]\n" \
  "v_and_b32_dpp v44, v33, v4 row_newbcast:4 row_mask:0xf bank_mask:0xf\n" \
  "v_and_b32_dpp v45, v33, v5 row_newbcast:4 row_mask:0xf bank_mask:0xf\n" \
  "v_mfma_f32_16x16x32_bf16 v[16:19], v[48:51], a[104:107], v[16:19]\n" \
  "v_and_b32_dpp v46, v33, v6 row_newbcast:4 row_mask:0xf bank_mask:0xf\n" \
  "v_and_b32_dpp v47, v33, v7 row_newbcast:4 row_mask:0xf bank_mask:0xf\n" \
  "v_mfma_f32_16x16x32_bf16 v[20:23], v[48:51], a[108:111], v[20:23]\n" \
  "v_and_b32_dpp v48, v33, v4 row_newbcast:8 row_mask:0xf bank_mask:0xf\n" \
  "v_and_b32_dpp v49, v33, v5 row_newbcast:8 row_mask:0xf bank_mask:0xf\n" \
  "v_mfma_f32_16x16x32_bf16 v[16:19], v[44:47], a[112:115], v[16:19]\n" \
  "v_and_b32_dpp v50, v33, v6 row_newbcast:8 row_mask:0xf bank_mask:0xf\n" \
  "v_and_b32_dpp v51, v33, v7 row_newbcast:8 row_mask:0xf bank_mask:0xf\n" \
  "v_mfma_f32_16x16x32_bf16 v[20:23], v[44:47], a[116:119], v[20:23]\n" \
  "v_and_b32_dpp v44, v33, v4 row_newbcast:12 row_mask:0xf bank_mask:0xf\n" \
  "v_and_b32_dpp v45, v33, v5 row_newbcast:12 row_mask:0xf bank_mask:0xf\n" \
  "v_mfma_f32_16x16x32_bf16 v[16:19], v[48:51], a[120:123], v[16:19]\n" \
  "v_and_b32_dpp v46, v33, v6 row_newbcast:12 row_mask:0xf bank_mask:0xf\n" \
  "v_and_b32_dpp v47, v33, v7 row_newbcast:12 row_mask:0xf bank_mask:0xf\n" \
  "v_mfma_f32_16x16x32_bf16 v[20:23], v[48:51], a[124:127], v[20:23]\n" \
  "s_nop 0\n" \
  "v_mfma_f32_16x16x32_bf16 v[16:19], v[44:47], a[128:131], v[16:19]\n" \
  "v_mfma_f32_16x16x32_bf16 v[20:23], v[44:47], a[132:135], v[20:23]\n" \
  "s_waitcnt vmcnt(13)\n" \
  "s_add_u32 s33, s33, s27\n" \
  "buffer_load_dwordx4 a[40:43], v0, s[36:39], s5 offen nt\n" \
  "s_mov_b32 s81, 155776\n" \
  "s_mov_b32 s82, s79\n" \
  "s_add_u32 s79, s79, 143360\n" \
  "s_add_u32 s59, s87, 155776\n" \
  "s_sub_u32 s85, 155776, s67\n" \
  "v_add_u32 v42, 155776, v3\n" \
  "v_and_b32 v43, 0x30, v2\n" \
  "v_add_u32 v63, s85, v2\n" \
  "v_mov_b32 v52, 1\n" \
  "v_add_u32 v43, 155776, v43\n" \
  "v_mov_b32 v48, s68\n" \
  "v_mov_b32 v49, s70\n" \
  "v_mov_b32 v50, s59\n" \
  "v_mov_b32 v51, s81\n" \
  "v_cndmask_b32 v44, v16, v20, s[88:89]\n" \
  "v_cndmask_b32 v45, v17, v21, s[88:89]\n" \
  "v_cndmask_b32 v46, v18, v22, s[88:89]\n" \
  "v_cndmask_b32 v47, v19, v23, s[88:89]\n" \
  "s_call_b64 s[94:95], .LA_MERGE\n" \
  "s_branch .LA_END_B\n" \
  ".LA_DEFER_B:\n" \
  "v_and_b32_dpp v44, v40, v4 row_newbcast:0 row_mask:0xf bank_mask:0xf\n" \
  "v_and_b32_dpp v45, v40, v5 row_newbcast:0 row_mask:0xf bank_mask:0xf\n" \
  "v_and_b32_dpp v46, v40, v6 row_newbcast:0 row_mask:0xf bank_mask:0xf\n" \
  "v_and_b32_dpp v47, v40, v7 row_newbcast:0 row_mask:0xf bank_mask:0xf\n" \
  "s_waitcnt vmcnt(13)\n" \
  "s_nop 0\n" \
  "v_mfma_f32_16x16x32_bf16 v[24:27], v[44:47], v[208:211], v[24:27]\n" \
  "s_waitcnt vmcnt(12)\n" \
  "v_mfma_f32_16x16x32_bf16 v[28:31], v[44:47], v[212:215], v[28:31]\n" \
  "v_and_b32 v42, 0xc0, v0\n" \
  "v_and_b32 v43, 0xf0, v0\n" \
  "v_add_u32 v42, v42, v43\n" \
  "buffer_load_dwordx4 v[8:11], v42, s[52:55], s76 offen sc1\n" \
  "buffer_load_dwordx4 v[12:15], v42, s[52:55], s76 offen offset:512 sc1\n" \
  "v_and_b32_dpp v48, v40, v4 row_newbcast:4 row_mask:0xf bank_mask:0xf\n" \
  "v_and_b32_dpp v49, v40, v5 row_newbcast:4 row_mask:0xf bank_mask:0xf\n" \
  "v_and_b32_dpp v50, v40, v6 row_newbcast:4 row_mask:0xf bank_mask:0xf\n" \
  "v_and_b32_dpp v51, v40, v7 row_newbcast:4 row_mask:0xf bank_mask:0xf\n" \
  "s_waitcnt vmcnt(13)\n" \
  "s_nop 0\n" \
  "v_mfma_f32_16x16x32_bf16 v[24:27], v[48:51], v[216:219], v[24:27]\n" \
  "s_waitcnt vmcnt(12)\n" \
  "v_mfma_f32_16x16x32_bf16 v[28:31], v[48:51], v[220:223], v[28:31]\n" \
  "v_and_b32_dpp v44, v40, v4 row_newbcast:8 row_mask:0xf bank_mask:0xf\n" \
  "v_and_b32_dpp v45, v40, v5 row_newbcast:8 row_mask:0xf bank_mask:0xf\n" \
  "v_and_b32_dpp v46, v40, v6 row_newbcast:8 row_mask:0xf bank_mask:0xf\n" \
  "v_and_b32_dpp v47, v40, v7 row_newbcast:8 row_mask:0xf bank_mask:0xf\n" \
  "s_waitcnt vmcnt(11)\n" \
  "s_nop 0\n" \
  "v_mfma_f32_16x16x32_bf16 v[24:27], v[44:47], v[224:227], v[24:27]\n" \
  "s_waitcnt vmcnt(10)\n" \
  "v_mfma_f32_16x16x32_bf16 v[28:31], v[44:47], v[228:231], v[28:31]\n" \
  "v_and_b32_dpp v48, v40, v4 row_newbcast:12 row_mask:0xf bank_mask:0xf\n" \
  "v_and_b32_dpp v49, v40, v5 row_newbcast:12 row_mask:0xf bank_mask:0xf\n" \
  "v_and_b32_dpp v50, v40, v6 row_newbcast:12 row_mask:0xf bank_mask:0xf\n" \
  "v_and_b32_dpp v51, v40, v7 row_newbcast:12 row_mask:0xf bank_mask:0xf\n" \
  "s_waitcnt vmcnt(7)\n" \
  "s_nop 0\n" \
  "v_mfma_f32_16x16x32_bf16 v[24:27], v[48:51], v[232:235], v[24:27]\n" \
  "s_waitcnt vmcnt(6)\n" \
  "v_mfma_f32_16x16x32_bf16 v[28:31], v[48:51], v[236:239], v[28:31]\n" \
  "v_and_b32_dpp v44, v41, v4 row_newbcast:0 row_mask:0xf bank_mask:0xf\n" \
  "v_and_b32_dpp v45, v41, v5 row_newbcast:0 row_mask:0xf bank_mask:0xf\n" \
  "v_and_b32_dpp v46, v41, v6 row_newbcast:0 row_mask:0xf bank_mask:0xf\n" \
  "v_and_b32_dpp v47, v41, v7 row_newbcast:0 row_mask:0xf bank_mask:0xf\n" \
  "s_waitcnt vmcnt(5)\n" \
  "s_nop 0\n" \
  "v_mfma_f32_16x16x32_bf16 v[24:27], v[44:47], v[240:243], v[24:27]\n" \
  "s_waitcnt vmcnt(4)\n" \
  "v_mfma_f32_16x16x32_bf16 v[28:31], v[44:47], v[244:247], v[28:31]\n" \
  "v_and_b32_dpp v48, v41, v4 row_newbcast:4 row_mask:0xf bank_mask:0xf\n" \
  "v_and_b32_dpp v49, v41, v5 row_newbcast:4 row_mask:0xf bank_mask:0xf\n" \
  "v_and_b32_dpp v50, v41, v6 row_newbcast:4 row_mask:0xf bank_mask:0xf\n" \
  "v_and_b32_dpp v51, v41, v7 row_newbcast:4 row_mask:0xf bank_mask:0xf\n" \
  "s_waitcnt vmcnt(3)\n" \
  "s_nop 0\n" \
  "v_mfma_f32_16x16x32_bf16 v[24:27], v[48:51], v[248:251], v[24:27]\n" \
  "s_waitcnt vmcnt(2)\n" \
  "v_mfma_f32_16x16x32_bf16 v[28:31], v[48:51], v[252:255], v[28:31]\n" \
  "s_mov_b32 s81, 151552\n" \
  "s_mov_b32 s82, s77\n" \
  "s_add_u32 s77, s77, 143360\n" \
  "s_add_u32 s59, s87, 151552\n" \
  "s_sub_u32 s85, 151552, s67\n" \
  "v_add_u32 v42, 151552, v3\n" \
  "v_and_b32 v43, 0x30, v2\n" \
  "v_add_u32 v63, s85, v2\n" \
  "v_mov_b32 v52, 1\n" \
  "v_add_u32 v43, 151552, v43\n" \
  "v_mov_b32 v48, s69\n" \
  "v_mov_b32 v49, s71\n" \
  "v_mov_b32 v50, s59\n" \
  "v_mov_b32 v51, s81\n" \
  "v_cndmask_b32 v44, v24, v28, s[88:89]\n" \
  "v_cndmask_b32 v45, v25, v29, s[88:89]\n" \
  "v_cndmask_b32 v46, v26, v30, s[88:89]\n" \
  "v_cndmask_b32 v47, v27, v31, s[88:89]\n" \
  "s_call_b64 s[94:95], .LA_MERGE\n" \
  "s_waitcnt vmcnt(2)\n" \
  "s_mov_b32 s86, s76\n" \
  "s_mov_b32 s56, s74\n" \
  "s_call_b64 s[94:95], .LA_QWAIT\n" \
  "s_memrealtime s[30:31]\n" \
  "s_waitcnt lgkmcnt(0)\n" \
  "s_lshl_b32 s30, s30, 6\n" \
  "s_sub_u32 s30, s30, 1920\n" \
  "s_mov_b32 s33, s30\n" \
  "s_add_u32 s76, s76, 16384\n" \
  "v_mfma_f32_16x16x32_bf16 v[44:47], a[0:3], v[8:11], 0\n" \
  "v_add_u32 v43, s78, v2\n" \
  "v_mov_b32 v36, 0xff800000\n" \
  "v_mfma_f32_16x16x32_bf16 v[44:47], a[4:7], v[12:15], v[44:47]\n" \
  "ds_read_b128 v[56:59], v43\n" \
  "ds_read_b128 v[60:63], v43 offset:1024\n" \
  "v_mov_b32 v42, s63\n" \
  "v_mfma_f32_16x16x32_bf16 v[48:51], a[8:11], v[8:11], 0\n" \
  "v_mfma_f32_16x16x32_bf16 v[48:51], a[12:15], v[12:15], v[48:51]\n" \
  "v_mov_b32_dpp v36, v42 quad_perm:[0,1,2,3] row_mask:0xf bank_mask:0x4\n" \
  "v_mfma_f32_16x16x32_bf16 v[52:55], a[16:19], v[8:11], 0\n" \
  "s_nop 0\n" \
  "v_mov_b32_dpp v32, v44 quad_perm:[0,0,0,0] row_mask:0xf bank_mask:0x1\n" \
  "v_mov_b32_dpp v42, v45 quad_perm:[1,1,1,1] row_mask:0xf bank_mask:0x1\n" \
  "v_mfma_f32_16x16x32_bf16 v[52:55], a[20:23], v[12:15], v[52:55]\n" \
  "v_add_f32_dpp v32, v46, v32 quad_perm:[2,2,2,2] row_mask:0xf bank_mask:0x1\n" \
  "v_add_f32_dpp v42, v47, v42 quad_perm:[3,3,3,3] row_mask:0xf bank_mask:0x1\n" \
  "v_mfma_f32_16x16x32_bf16 v[44:47], a[24:27], v[8:11], 0\n" \
  "v_mov_b32_dpp v32, v48 quad_perm:[0,0,0,0] row_mask:0xf bank_mask:0x2\n" \
  "v_mov_b32_dpp v42, v49 quad_perm:[1,1,1,1] row_mask:0xf bank_mask:0x2\n" \
  "v_mfma_f32_16x16x32_bf16 v[44:47], a[28:31], v[12:15], v[44:47]\n" \
  "v_add_f32_dpp v32, v50, v32 quad_perm:[2,2,2,2] row_mask:0xf bank_mask:0x2\n" \
  "v_add_f32_dpp v42, v51, v42 quad_perm:[3,3,3,3] row_mask:0xf bank_mask:0x2\n" \
  "v_mfma_f32_16x16x32_bf16 v[48:51], a[32:35], v[8:11], 0\n" \
  "v_mov_b32_dpp v32, v52 quad_perm:[0,0,0,0] row_mask:0xf bank_mask:0x4\n" \
  "v_mov_b32_dpp v42, v53 quad_perm:[1,1,1,1] row_mask:0xf bank_mask:0x4\n" \
  "v_mfma_f32_16x16x32_bf16 v[48:51], a[36:39], v[12:15], v[48:51]\n" \
  "v_add_f32_dpp v32, v54, v32 quad_perm:[2,2,2,2] row_mask:0xf bank_mask:0x4\n" \
  "v_add_f32_dpp v42, v55, v42 quad_perm:[3,3,3,3] row_mask:0xf bank_mask:0x4\n" \
  "v_mfma_f32_16x16x32_bf16 v[52:55], a[40:43], v[8:11], 0\n" \
  "v_mov_b32_dpp v32, v44 quad_perm:[0,0,0,0] row_mask:0xf bank_mask:0x8\n" \
  "v_mov_b32_dpp v42, v45 quad_perm:[1,1,1,1] row_mask:0xf bank_mask:0x8\n" \
  "v_mfma_f32_16x16x32_bf16 v[52:55], a[44:47], v[12:15], v[52:55]\n" \
  "v_add_f32_dpp v32, v46, v32 quad_perm:[2,2,2,2] row_mask:0xf bank_mask:0x8\n" \
  "v_add_f32_dpp v42, v47, v42 quad_perm:[3,3,3,3] row_mask:0xf bank_mask:0x8\n" \
  "v_mfma_f32_16x16x32_bf16 v[44:47], a[48:51], v[8:11], 0\n" \
  "v_mov_b32_dpp v33, v48 quad_perm:[0,0,0,0] row_mask:0xf bank_mask:0x1\n" \
  "v_mov_b32_dpp v43, v49 quad_perm:[1,1,1,1] row_mask:0xf bank_mask:0x1\n" \
  "v_mfma_f32_16x16x32_bf16 v[44:47], a[52:55], v[12:15], v[44:47]\n" \
  "v_add_f32_dpp v33, v50, v33 quad_perm:[2,2,2,2] row_mask:0xf bank_mask:0x1\n" \
  "v_add_f32_dpp v43, v51, v43 quad_perm:[3,3,3,3] row_mask:0xf bank_mask:0x1\n" \
  "v_add_f32 v32, v32, v42\n" \
  "v_mfma_f32_16x16x32_bf16 v[48:51], a[56:59], v[8:11], 0\n" \
  "v_mov_b32_dpp v33, v52 quad_perm:[0,0,0,0] row_mask:0xf bank_mask:0x2\n" \
  "v_mov_b32_dpp v43, v53 quad_perm:[1,1,1,1] row_mask:0xf bank_mask:0x2\n" \
  "v_mfma_f32_16x16x32_bf16 v[48:51], a[60:63], v[12:15], v[48:51]\n" \
  "v_add_f32_dpp v33, v54, v33 quad_perm:[2,2,2,2] row_mask:0xf bank_mask:0x2\n" \
  "v_add_f32_dpp v43, v55, v43 quad_perm:[3,3,3,3] row_mask:0xf bank_mask:0x2\n" \
  "v_mfma_f32_16x16x32_bf16 v[52:55], a[64:67], v[8:11], 0\n" \
  "v_mov_b32_dpp v33, v44 quad_perm:[0,0,0,0] row_mask:0xf bank_mask:0x4\n" \
  "v_mov_b32_dpp v43, v45 quad_perm:[1,1,1,1] row_mask:0xf bank_mask:0x4\n" \
  "v_mfma_f32_16x16x32_bf16 v[52:55], a[68:71], v[12:15], v[52:55]\n" \
  "v_add_f32_dpp v33, v46, v33 quad_perm:[2,2,2,2] row_mask:0xf bank_mask:0x4\n" \
  "v_add_f32_dpp v43, v47, v43 quad_perm:[3,3,3,3] row_mask:0xf bank_mask:0x4\n" \
  "v_mfma_f32_16x16x32_bf16 v[44:47], a[72:75], v[8:11], 0\n" \
  "v_mov_b32_dpp v33, v48 quad_perm:[0,0,0,0] row_mask:0xf bank_mask:0x8\n" \
  "v_mov_b32_dpp v43, v49 quad_perm:[1,1,1,1] row_mask:0xf bank_mask:0x8\n" \
  "v_mfma_f32_16x16x32_bf16 v[44:47], a[76:79], v[12:15], v[44:47]\n" \
  "v_add_f32_dpp v33, v50, v33 quad_perm:[2,2,2,2] row_mask:0xf bank_mask:0x8\n" \
  "v_add_f32_dpp v43, v51, v43 quad_perm:[3,3,3,3] row_mask:0xf bank_mask:0x8\n" \
  "v_mfma_f32_16x16x32_bf16 v[48:51], a[80:83], v[8:11], 0\n" \
  "v_mov_b32_dpp v34, v52 quad_perm:[0,0,0,0] row_mask:0xf bank_mask:0x1\n" \
  "v_mov_b32_dpp v42, v53 quad_perm:[1,1,1,1] row_mask:0xf bank_mask:0x1\n" \
  "v_mfma_f32_16x16x32_bf16 v[48:51], a[84:87], v[12:15], v[48:51]\n" \
  "v_add_f32_dpp v34, v54, v34 quad_perm:[2,2,2,2] row_mask:0xf bank_mask:0x1\n" \
  "v_add_f32_dpp v42, v55, v42 quad_perm:[3,3,3,3] row_mask:0xf bank_mask:0x1\n" \
  "v_add_f32 v33, v33, v43\n" \
  "v_mfma_f32_16x16x32_bf16 v[52:55], a[88:91], v[8:11], 0\n" \
  "v_mov_b32_dpp v34, v44 quad_perm:[0,0,0,0] row_mask:0xf bank_mask:0x2\n" \
  "v_mov_b32_dpp v42, v45 quad_perm:[1,1,1,1] row_mask:0xf bank_mask:0x2\n" \
  "v_mfma_f32_16x16x32_bf16 v[52:55], a[92:95], v[12:15], v[52:55]\n" \
  "v_add_f32_dpp v34, v46, v34 quad_perm:[2,2,2,2] row_mask:0xf bank_mask:0x2\n" \
  "v_add_f32_dpp v42, v47, v42 quad_perm:[3,3,3,3] row_mask:0xf bank_mask:0x2\n" \
  "v_mfma_f32_16x16x32_bf16 v[44:47], a[96:99], v[8:11], 0\n" \
  "v_mov_b32_dpp v34, v48 quad_perm:[0,0,0,0] row_mask:0xf bank_mask:0x4\n" \
  "v_mov_b32_dpp v42, v49 quad_perm:[1,1,1,1] row_mask:0xf bank_mask:0x4\n" \
  "v_mfma_f32_16x16x32_bf16 v[44:47], a[100:103], v[12:15], v[44:47]\n" \
  "v_add_f32_dpp v34, v50, v34 quad_perm:[2,2,2,2] row_mask:0xf bank_mask:0x4\n" \
  "v_add_f32_dpp v42, v51, v42 quad_perm:[3,3,3,3] row_mask:0xf bank_mask:0x4\n" \
  "v_mfma_f32_16x16x32_bf16 v[48:51], a[104:107], v[8:11], 0\n" \
  "v_mov_b32_dpp v34, v52 quad_perm:[0,0,0,0] row_mask:0xf bank_mask:0x8\n" \
  "v_mov_b32_dpp v42, v53 quad_perm:[1,1,1,1] row_mask:0xf bank_mask:0x8\n" \
  "v_mfma_f32_16x16x32_bf16 v[48:51], a[108:111], v[12:15], v[48:51]\n" \
  "v_add_f32_dpp v34, v54, v34 quad_perm:[2,2,2,2] row_mask:0xf bank_mask:0x8\n" \
  "v_add_f32_dpp v42, v55, v42 quad_perm:[3,3,3,3] row_mask:0xf bank_mask:0x8\n" \
  "v_mfma_f32_16x16x32_bf16 v[52:55], a[112:115], v[8:11], 0\n" \
  "v_mov_b32_dpp v35, v44 quad_perm:[0,0,0,0] row_mask:0xf bank_mask:0x1\n" \
  "v_mov_b32_dpp v43, v45 quad_perm:[1,1,1,1] row_mask:0xf bank_mask:0x1\n" \
  "v_mfma_f32_16x16x32_bf16 v[52:55], a[116:119], v[12:15], v[52:55]\n" \
  "v_add_f32_dpp v35, v46, v35 quad_perm:[2,2,2,2] row_mask:0xf bank_mask:0x1\n" \
  "v_add_f32_dpp v43, v47, v43 quad_perm:[3,3,3,3] row_mask:0xf bank_mask:0x1\n" \
  "v_add_f32 v34, v34, v42\n" \
  "v_mfma_f32_16x16x32_bf16 v[44:47], a[120:123], v[8:11], 0\n" \
  "v_mov_b32_dpp v35, v48 quad_perm:[0,0,0,0] row_mask:0xf bank_mask:0x2\n" \
  "v_mov_b32_dpp v43, v49 quad_perm:[1,1,1,1] row_mask:0xf bank_mask:0x2\n" \
  "v_mfma_f32_16x16x32_bf16 v[44:47], a[124:127], v[12:15], v[44:47]\n" \
  "v_add_f32_dpp v35, v50, v35 quad_perm:[2,2,2,2] row_mask:0xf bank_mask:0x2\n" \
  "v_add_f32_dpp v43, v51, v43 quad_perm:[3,3,3,3] row_mask:0xf bank_mask:0x2\n" \
  "v_mfma_f32_16x16x32_bf16 v[48:51], a[128:131], v[8:11], 0\n" \
  "v_mov_b32_dpp v35, v52 quad_perm:[0,0,0,0] row_mask:0xf bank_mask:0x4\n" \
  "v_mov_b32_dpp v43, v53 quad_perm:[1,1,1,1] row_mask:0xf bank_mask:0x4\n" \
  "v_mfma_f32_16x16x32_bf16 v[48:51], a[132:135], v[12:15], v[48:51]\n" \
  "v_add_f32_dpp v35, v54, v35 quad_perm:[2,2,2,2] row_mask:0xf bank_mask:0x4\n" \
  "v_add_f32_dpp v43, v55, v43 quad_perm:[3,3,3,3] row_mask:0xf bank_mask:0x4\n" \
  "v_mfma_f32_16x16x32_bf16 v[52:55], a[136:139], v[8:11], 0\n" \
  "v_mov_b32_dpp v35, v44 quad_perm:[0,0,0,0] row_mask:0xf bank_mask:0x8\n" \
  "v_mov_b32_dpp v43, v45 quad_perm:[1,1,1,1] row_mask:0xf bank_mask:0x8\n" \
  "v_mfma_f32_16x16x32_bf16 v[52:55], a[140:143], v[12:15], v[52:55]\n" \
  "v_add_f32_dpp v35, v46, v35 quad_perm:[2,2,2,2] row_mask:0xf bank_mask:0x8\n" \
  "v_add_f32_dpp v43, v47, v43 quad_perm:[3,3,3,3] row_mask:0xf bank_mask:0x8\n" \
  "s_waitcnt lgkmcnt(0)\n" \
  "v_mfma_f32_16x16x32_bf16 v[44:47], v[56:59], v[8:11], 0\n" \
  "v_mov_b32_dpp v36, v48 quad_perm:[0,0,0,0] row_mask:0xf bank_mask:0x1\n" \
  "v_mov_b32_dpp v42, v49 quad_perm:[1,1,1,1] row_mask:0xf bank_mask:0x1\n" \
  "v_mfma_f32_16x16x32_bf16 v[44:47], v[60:63], v[12:15], v[44:47]\n" \
  "v_add_f32_dpp v36, v50, v36 quad_perm:[2,2,2,2] row_mask:0xf bank_mask:0x1\n" \
  "v_add_f32_dpp v42, v51, v42 quad_perm:[3,3,3,3] row_mask:0xf bank_mask:0x1\n" \
  "v_add_f32 v35, v35, v43\n" \
  "v_mov_b32_dpp v36, v52 quad_perm:[0,0,0,0] row_mask:0xf bank_mask:0x2\n" \
  "v_mov_b32_dpp v42, v53 quad_perm:[1,1,1,1] row_mask:0xf bank_mask:0x2\n" \
  "s_nop 0\n" \
  "v_add_f32_dpp v36, v54, v36 quad_perm:[2,2,2,2] row_mask:0xf bank_mask:0x2\n" \
  "v_add_f32_dpp v42, v55, v42 quad_perm:[3,3,3,3] row_mask:0xf bank_mask:0x2\n" \
  "s_nop 0\n" \
  "v_add_f32_dpp v36, v44, v36 quad_perm:[0,0,0,0] row_mask:0xf bank_mask:0x4\n" \
  "v_mov_b32_dpp v42, v45 quad_perm:[1,1,1,1] row_mask:0xf bank_mask:0x4\n" \
  "s_nop 0\n" \
  "v_add_f32_dpp v36, v46, v36 quad_perm:[2,2,2,2] row_mask:0xf bank_mask:0x4\n" \
  "v_add_f32_dpp v42, v47, v42 quad_perm:[3,3,3,3] row_mask:0xf bank_mask:0x4\n" \
  "v_add_f32 v36, v36, v42\n" \
  "s_cmp_eq_u32 s73, 0\n" \
  "s_cbranch_scc1 .LA_NOISS\n" \
  "s_add_u32 s36, s36, 0x2000000\n" \
  "s_addc_u32 s37, s37, 0\n" \
  "s_mov_b64 s[44:45], s[36:37]\n" \
  "s_mov_b32 s46, s61\n" \
  "s_add_u32 m0, s65, 0\n" \
  "s_nop 0\n" \
  "buffer_load_dwordx4 v0, s[44:47], s58 offen nt lds\n" \
  "s_add_u32 m0, s65, 768\n" \
  "s_nop 0\n" \
  "buffer_load_dwordx4 v0, s[44:47], s58 offen offset:256 nt lds\n" \
  "buffer_load_dwordx4 a[0:3], v0, s[36:39], s0 offen nt\n" \
  "buffer_load_dwordx4 a[4:7], v0, s[36:39], s0 offen offset:256 nt\n" \
  "buffer_load_dwordx4 a[8:11], v0, s[36:39], s1 offen nt\n" \
  "buffer_load_dwordx4 a[12:15], v0, s[36:39], s1 offen offset:256 nt\n" \
  "buffer_load_dwordx4 a[16:19], v0, s[36:39], s2 offen nt\n" \
  "buffer_load_dwordx4 a[20:23], v0, s[36:39], s2 offen offset:256 nt\n" \
  "buffer_load_dwordx4 a[24:27], v0, s[36:39], s3 offen nt\n" \
  "buffer_load_dwordx4 a[28:31], v0, s[36:39], s3 offen offset:256 nt\n" \
  "buffer_load_dwordx4 a[32:35], v0, s[36:39], s4 offen nt\n" \
  "buffer_load_dwordx4 a[36:39], v0, s[36:39], s4 offen offset:256 nt\n" \
  "buffer_load_dwordx4 a[40:43], v0, s[36:39], s5 offen nt\n" \
  "s_add_u32 s33, s33, 9960\n" \
  ".LA_NOISS:\n" \
  "v_add_u32 v43, s78, v2\n" \
  "v_max3_f32 v42, v32, v33, v34\n" \
  "v_max3_f32 v42, v42, v35, v36\n" \
  "ds_read_b128 a[136:139], v43 offset:2048\n" \
  "ds_read_b128 a[140:143], v43 offset:3072\n" \
  "v_max_f32_dpp v42, v42, v42 row_ror:4 row_mask:0xf bank_mask:0xf\n" \
  "ds_read_b128 a[72:75], v2 offset:0\n" \
  "ds_read_b128 a[76:79], v2 offset:1024\n" \
  "v_max_f32_dpp v42, v42, v42 row_ror:8 row_mask:0xf bank_mask:0xf\n" \
  "ds_read_b128 a[80:83], v2 offset:2048\n" \
  "ds_read_b128 a[84:87], v2 offset:3072\n" \
  "v_max_f32_dpp v42, v42, v42 row_bcast:15 row_mask:0xa bank_mask:0xf\n" \
  "ds_read_b128 a[88:91], v2 offset:4096\n" \
  "ds_read_b128 a[92:95], v2 offset:5120\n" \
  "v_max_f32_dpp v42, v42, v42 row_bcast:31 row_mask:0xc bank_mask:0xf\n" \
  "ds_read_b128 a[96:99], v2 offset:6144\n" \
  "ds_read_b128 a[100:103], v2 offset:7168\n" \
  "v_readlane_b32 s68, v42, 63\n" \
  "ds_read_b128 a[104:107], v2 offset:8192\n" \
  "ds_read_b128 a[108:111], v2 offset:9216\n" \
  "v_subrev_f32 v32, s68, v32\n" \
  "v_subrev_f32 v33, s68, v33\n" \
  "v_subrev_f32 v34, s68, v34\n" \
  "v_subrev_f32 v35, s68, v35\n" \
  "v_subrev_f32 v36, s68, v36\n" \
  "v_exp_f32 v32, v32\n" \
  "v_exp_f32 v33, v33\n" \
  "v_exp_f32 v34, v34\n" \
  "v_exp_f32 v35, v35\n" \
  "v_exp_f32 v36, v36\n" \
  "v_add_f32 v8, v32, v33\n" \
  "v_add_f32 v9, v34, v35\n" \
  "v_cvt_pk_bf16_f32 v32, v32, v32\n" \
  "v_cvt_pk_bf16_f32 v33, v33, v33\n" \
  "v_cvt_pk_bf16_f32 v34, v34, v34\n" \
  "v_cvt_pk_bf16_f32 v35, v35, v35\n" \
  "v_add_f32 v9, v9, v36\n" \
  "v_add_f32 v8, v8, v9\n" \
  "v_cvt_pk_bf16_f32 v36, v36, v36\n" \
  "v_and_b32_dpp v44, v34, v4 row_newbcast:0 row_mask:0xf bank_mask:0xf\n" \
  "v_and_b32_dpp v45, v34, v5 row_newbcast:0 row_mask:0xf bank_mask:0xf\n" \
  "v_and_b32_dpp v46, v34, v6 row_newbcast:0 row_mask:0xf bank_mask:0xf\n" \
  "v_and_b32_dpp v47, v34, v7 row_newbcast:0 row_mask:0xf bank_mask:0xf\n" \
  "v_and_b32_dpp v48, v34, v4 row_newbcast:4 row_mask:0xf bank_mask:0xf\n" \
  "v_and_b32_dpp v49, v34, v5 row_newbcast:4 row_mask:0xf bank_mask:0xf\n" \
  "v_mfma_f32_16x16x32_bf16 v[16:19], v[44:47], a[144:147], 0\n" \
  "v_and_b32_dpp v50, v34, v6 row_newbcast:4 row_mask:0xf bank_mask:0xf\n" \
  "v_and_b32_dpp v51, v34, v7 row_newbcast:4 row_mask:0xf bank_mask:0xf\n" \
  "ds_read_b128 a[112:115], v2 offset:10240\n" \
  "v_mfma_f32_16x16x32_bf16 v[20:23], v[44:47], a[148:151], 0\n" \
  "v_and_b32_dpp v44, v34, v4 row_newbcast:8 row_mask:0xf bank_mask:0xf\n" \
  "v_and_b32_dpp v45, v34, v5 row_newbcast:8 row_mask:0xf bank_mask:0xf\n" \
  "ds_read_b128 a[116:119], v2 offset:11264\n" \
  "v_mfma_f32_16x16x32_bf16 v[16:19], v[48:51], a[152:155], v[16:19]\n" \
  "v_and_b32_dpp v46, v34, v6 row_newbcast:8 row_mask:0xf bank_mask:0xf\n" \
  "v_and_b32_dpp v47, v34, v7 row_newbcast:8 row_mask:0xf bank_mask:0xf\n" \
  "ds_read_b128 a[120:123], v2 offset:12288\n" \
  "v_mfma_f32_16x16x32_bf16 v[20:23], v[48:51], a[156:159], v[20:23]\n" \
  "v_and_b32_dpp v48, v34, v4 row_newbcast:12 row_mask:0xf bank_mask:0xf\n" \
  "v_and_b32_dpp v49, v34, v5 row_newbcast:12 row_mask:0xf bank_mask:0xf\n" \
  "ds_read_b128 a[124:127], v2 offset:13312\n" \
  "v_mfma_f32_16x16x32_bf16 v[16:19], v[44:47], a[160:163], v[16:19]\n" \
  "v_and_b32_dpp v50, v34, v6 row_newbcast:12 row_mask:0xf bank_mask:0xf\n" \
  "v_and_b32_dpp v51, v34, v7 row_newbcast:12 row_mask:0xf bank_mask:0xf\n" \
  "ds_read_b128 a[128:131], v2 offset:14336\n" \
  "v_mfma_f32_16x16x32_bf16 v[20:23], v[44:47], a[164:167], v[20:23]\n" \
  "v_and_b32_dpp v44, v35, v4 row_newbcast:0 row_mask:0xf bank_mask:0xf\n" \
  "v_and_b32_dpp v45, v35, v5 row_newbcast:0 row_mask:0xf bank_mask:0xf\n" \
  "ds_read_b128 a[132:135], v2 offset:15360\n" \
  "v_mfma_f32_16x16x32_bf16 v[16:19], v[48:51], a[168:171], v[16:19]\n" \
  "v_and_b32_dpp v46, v35, v6 row_newbcast:0 row_mask:0xf bank_mask:0xf\n" \
  "v_and_b32_dpp v47, v35, v7 row_newbcast:0 row_mask:0xf bank_mask:0xf\n" \
  "v_add_f32_dpp v8, v8, v8 row_ror:4 row_mask:0xf bank_mask:0xf\n" \
  "v_mfma_f32_16x16x32_bf16 v[20:23], v[48:51], a[172:175], v[20:23]\n" \
  "v_and_b32_dpp v48, v35, v4 row_newbcast:4 row_mask:0xf bank_mask:0xf\n" \
  "v_and_b32_dpp v49, v35, v5 row_newbcast:4 row_mask:0xf bank_mask:0xf\n" \
  "v_mfma_f32_16x16x32_bf16 v[16:19], v[44:47], a[176:179], v[16:19]\n" \
  "v_and_b32_dpp v50, v35, v6 row_newbcast:4 row_mask:0xf bank_mask:0xf\n" \
  "v_and_b32_dpp v51, v35, v7 row_newbcast:4 row_mask:0xf bank_mask:0xf\n" \
  "v_add_f32_dpp v8, v8, v8 row_ror:8 row_mask:0xf bank_mask:0xf\n" \
  "v_mfma_f32_16x16x32_bf16 v[20:23], v[44:47], a[180:183], v[20:23]\n" \
  "v_and_b32_dpp v44, v35, v4 row_newbcast:8 row_mask:0xf bank_mask:0xf\n" \
  "v_and_b32_dpp v45, v35, v5 row_newbcast:8 row_mask:0xf bank_mask:0xf\n" \
  "v_mfma_f32_16x16x32_bf16 v[16:19], v[48:51], a[184:187], v[16:19]\n" \
  "v_and_b32_dpp v46, v35, v6 row_newbcast:8 row_mask:0xf bank_mask:0xf\n" \
  "v_and_b32_dpp v47, v35, v7 row_newbcast:8 row_mask:0xf bank_mask:0xf\n" \
  "v_add_f32_dpp v8, v8, v8 row_bcast:15 row_mask:0xa bank_mask:0xf\n" \
  "v_mfma_f32_16x16x32_bf16 v[20:23], v[48:51], a[188:191], v[20:23]\n" \
  "v_and_b32_dpp v48, v35, v4 row_newbcast:12 row_mask:0xf bank_mask:0xf\n" \
  "v_and_b32_dpp v49, v35, v5 row_newbcast:12 row_mask:0xf bank_mask:0xf\n" \
  "v_mfma_f32_16x16x32_bf16 v[16:19], v[44:47], a[192:195], v[16:19]\n" \
  "v_and_b32_dpp v50, v35, v6 row_newbcast:12 row_mask:0xf bank_mask:0xf\n" \
  "v_and_b32_dpp v51, v35, v7 row_newbcast:12 row_mask:0xf bank_mask:0xf\n" \
  "v_add_f32_dpp v8, v8, v8 row_bcast:31 row_mask:0xc bank_mask:0xf\n" \
  "v_mfma_f32_16x16x32_bf16 v[20:23], v[44:47], a[196:199], v[20:23]\n" \
  "v_and_b32_dpp v44, v36, v4 row_newbcast:0 row_mask:0xf bank_mask:0xf\n" \
  "v_and_b32_dpp v45, v36, v5 row_newbcast:0 row_mask:0xf bank_mask:0xf\n" \
  "v_mfma_f32_16x16x32_bf16 v[16:19], v[48:51], a[200:203], v[16:19]\n" \
  "v_and_b32_dpp v46, v36, v6 row_newbcast:0 row_mask:0xf bank_mask:0xf\n" \
  "v_and_b32_dpp v47, v36, v7 row_newbcast:0 row_mask:0xf bank_mask:0xf\n" \
  "v_readlane_b32 s70, v8, 63\n" \
  "v_mfma_f32_16x16x32_bf16 v[20:23], v[48:51], a[204:207], v[20:23]\n" \
  "v_and_b32_dpp v48, v36, v4 row_newbcast:4 row_mask:0xf bank_mask:0xf\n" \
  "v_and_b32_dpp v49, v36, v5 row_newbcast:4 row_mask:0xf bank_mask:0xf\n" \
  "v_mfma_f32_16x16x32_bf16 v[16:19], v[44:47], a[208:211], v[16:19]\n" \
  "v_and_b32_dpp v50, v36, v6 row_newbcast:4 row_mask:0xf bank_mask:0xf\n" \
  "v_and_b32_dpp v51, v36, v7 row_newbcast:4 row_mask:0xf bank_mask:0xf\n" \
  "v_mfma_f32_16x16x32_bf16 v[20:23], v[44:47], a[212:215], v[20:23]\n" \
  "v_and_b32_dpp v44, v36, v4 row_newbcast:8 row_mask:0xf bank_mask:0xf\n" \
  "v_and_b32_dpp v45, v36, v5 row_newbcast:8 row_mask:0xf bank_mask:0xf\n" \
  "v_mfma_f32_16x16x32_bf16 v[16:19], v[48:51], a[216:219], v[16:19]\n" \
  "v_and_b32_dpp v46, v36, v6 row_newbcast:8 row_mask:0xf bank_mask:0xf\n" \
  "v_and_b32_dpp v47, v36, v7 row_newbcast:8 row_mask:0xf bank_mask:0xf\n" \
  "v_mfma_f32_16x16x32_bf16 v[20:23], v[48:51], a[220:223], v[20:23]\n" \
  "v_and_b32_dpp v48, v32, v4 row_newbcast:0 row_mask:0xf bank_mask:0xf\n" \
  "v_and_b32_dpp v49, v32, v5 row_newbcast:0 row_mask:0xf bank_mask:0xf\n" \
  "s_waitcnt lgkmcnt(0)\n" \
  "v_mfma_f32_16x16x32_bf16 v[16:19], v[44:47], a[136:139], v[16:19]\n" \
  "v_and_b32_dpp v50, v32, v6 row_newbcast:0 row_mask:0xf bank_mask:0xf\n" \
  "v_and_b32_dpp v51, v32, v7 row_newbcast:0 row_mask:0xf bank_mask:0xf\n" \
  "v_mfma_f32_16x16x32_bf16 v[20:23], v[44:47], a[140:143], v[20:23]\n" \
  "v_and_b32_dpp v44, v32, v4 row_newbcast:4 row_mask:0xf bank_mask:0xf\n" \
  "v_and_b32_dpp v45, v32, v5 row_newbcast:4 row_mask:0xf bank_mask:0xf\n" \
  "v_mfma_f32_16x16x32_bf16 v[16:19], v[48:51], a[72:75], v[16:19]\n" \
  "v_and_b32_dpp v46, v32, v6 row_newbcast:4 row_mask:0xf bank_mask:0xf\n" \
  "v_and_b32_dpp v47, v32, v7 row_newbcast:4 row_mask:0xf bank_mask:0xf\n" \
  "v_mfma_f32_16x16x32_bf16 v[20:23], v[48:51], a[76:79], v[20:23]\n" \
  "v_and_b32_dpp v48, v32, v4 row_newbcast:8 row_mask:0xf bank_mask:0xf\n" \
  "v_and_b32_dpp v49, v32, v5 row_newbcast:8 row_mask:0xf bank_mask:0xf\n" \
  "v_mfma_f32_16x16x32_bf16 v[16:19], v[44:47], a[80:83], v[16:19]\n" \
  "v_and_b32_dpp v50, v32, v6 row_newbcast:8 row_mask:0xf bank_mask:0xf\n" \
  "v_and_b32_dpp v51, v32, v7 row_newbcast:8 row_mask:0xf bank_mask:0xf\n" \
  "v_mfma_f32_16x16x32_bf16 v[20:23], v[44:47], a[84:87], v[20:23]\n" \
  "v_and_b32_dpp v44, v32, v4 row_newbcast:12 row_mask:0xf bank_mask:0xf\n" \
  "v_and_b32_dpp v45, v32, v5 row_newbcast:12 row_mask:0xf bank_mask:0xf\n" \
  "v_mfma_f32_16x16x32_bf16 v[16:19], v[48:51], a[88:91], v[16:19]\n" \
  "v_and_b32_dpp v46, v32, v6 row_newbcast:12 row_mask:0xf bank_mask:0xf\n" \
  "v_and_b32_dpp v47, v32, v7 row_newbcast:12 row_mask:0xf bank_mask:0xf\n" \
  "v_mfma_f32_16x16x32_bf16 v[20:23], v[48:51], a[92:95], v[20:23]\n" \
  "v_and_b32_dpp v48, v33, v4 row_newbcast:0 row_mask:0xf bank_mask:0xf\n" \
  "v_and_b32_dpp v49, v33, v5 row_newbcast:0 row_mask:0xf bank_mask:0xf\n" \
  "v_mfma_f32_16x16x32_bf16 v[16:19], v[44:47], a[96:99], v[16:19]\n" \
  "v_and_b32_dpp v50, v33, v6 row_newbcast:0 row_mask:0xf bank_mask:0xf\n" \
  "v_and_b32_dpp v51, v33, v7 row_newbcast:0 row_mask:0xf bank_mask:0xf\n" \
  "v_mfma_f32_16x16x32_bf16 v[20:23], v[44:47], a[100:103], v[20:23]\n" \
  "v_and_b32_dpp v44, v33, v4 row_newbcast:4 row_mask:0xf bank_mask:0xf\n" \
  "v_and_b32_dpp v45, v33, v5 row_newbcast:4 row_mask:0xf bank_mask:0xf\n" \
  "v_mfma_f32_16x16x32_bf16 v[16:19], v[48:51], a[104:107], v[16:19]\n" \
  "v_and_b32_dpp v46, v33, v6 row_newbcast:4 row_mask:0xf bank_mask:0xf\n" \
  "v_and_b32_dpp v47, v33, v7 row_newbcast:4 row_mask:0xf bank_mask:0xf\n" \
  "v_mfma_f32_16x16x32_bf16 v[20:23], v[48:51], a[108:111], v[20:23]\n" \
  "v_and_b32_dpp v48, v33, v4 row_newbcast:8 row_mask:0xf bank_mask:0xf\n" \
  "v_and_b32_dpp v49, v33, v5 row_newbcast:8 row_mask:0xf bank_mask:0xf\n" \
  "v_mfma_f32_16x16x32_bf16 v[16:19], v[44:47], a[112:115], v[16:19]\n" \
  "v_and_b32_dpp v50, v33, v6 row_newbcast:8 row_mask:0xf bank_mask:0xf\n" \
  "v_and_b32_dpp v51, v33, v7 row_newbcast:8 row_mask:0xf bank_mask:0xf\n" \
  "v_mfma_f32_16x16x32_bf16 v[20:23], v[44:47], a[116:119], v[20:23]\n" \
  "v_and_b32_dpp v44, v33, v4 row_newbcast:12 row_mask:0xf bank_mask:0xf\n" \
  "v_and_b32_dpp v45, v33, v5 row_newbcast:12 row_mask:0xf bank_mask:0xf\n" \
  "v_mfma_f32_16x16x32_bf16 v[16:19], v[48:51], a[120:123], v[16:19]\n" \
  "v_and_b32_dpp v46, v33, v6 row_newbcast:12 row_mask:0xf bank_mask:0xf\n" \
  "v_and_b32_dpp v47, v33, v7 row_newbcast:12 row_mask:0xf bank_mask:0xf\n" \
  "v_mfma_f32_16x16x32_bf16 v[20:23], v[48:51], a[124:127], v[20:23]\n" \
  "s_nop 0\n" \
  "v_mfma_f32_16x16x32_bf16 v[16:19], v[44:47], a[128:131], v[16:19]\n" \
  "v_mfma_f32_16x16x32_bf16 v[20:23], v[44:47], a[132:135], v[20:23]\n" \
  "s_mov_b32 s81, 155776\n" \
  "s_mov_b32 s82, s79\n" \
  "s_add_u32 s79, s79, 143360\n" \
  "s_add_u32 s59, s87, 155776\n" \
  "s_sub_u32 s85, 155776, s67\n" \
  "v_add_u32 v42, 155776, v3\n" \
  "v_and_b32 v43, 0x30, v2\n" \
  "v_add_u32 v63, s85, v2\n" \
  "v_mov_b32 v52, 1\n" \
  "v_add_u32 v43, 155776, v43\n" \
  "v_mov_b32 v48, s68\n" \
  "v_mov_b32 v49, s70\n" \
  "v_mov_b32 v50, s59\n" \
  "v_mov_b32 v51, s81\n" \
  "v_cndmask_b32 v44, v16, v20, s[88:89]\n" \
  "v_cndmask_b32 v45, v17, v21, s[88:89]\n" \
  "v_cndmask_b32 v46, v18, v22, s[88:89]\n" \
  "v_cndmask_b32 v47, v19, v23, s[88:89]\n" \
  "s_call_b64 s[94:95], .LA_MERGE\n" \
  "buffer_store_dword v42, off, s[48:51], 0\n" \
  "buffer_store_dword v42, off, s[48:51], 0\n" \
  ".LA_END_B:\n" \
  "s_add_u32 s72, s72, 1\n" \
  "s_cmp_lt_u32 s72, 2\n" \
  "s_cbranch_scc1 .LA_LOOP\n" \
  "s_mov_b32 s84, 4\n" \
  "s_waitcnt vmcnt(0)\n" \
  "s_call_b64 s[94:95], .LA_GUARD\n" \
  "v_and_b32 v42, 0xc0, v0\n" \
  "v_and_b32 v43, 0xf0, v0\n" \
  "v_add_u32 v42, v42, v43\n" \
  "buffer_load_dwordx4 v[8:11], v42, s[52:55], s76 offen sc1\n" \
  "buffer_load_dwordx4 v[12:15], v42, s[52:55], s76 offen offset:512 sc1\n" \
  "v_add_u32 v43, s80, v2\n" \
  "ds_read_b128 a[0:3], v43\n" \
  "ds_read_b128 a[4:7], v43 offset:1024\n" \
  "ds_read_b128 a[136:139], v43 offset:2048\n" \
  "ds_read_b128 a[140:143], v43 offset:3072\n" \
  "ds_read_b128 a[72:75], v2 offset:16384\n" \
  "ds_read_b128 a[76:79], v2 offset:17408\n" \
  "ds_read_b128 a[80:83], v2 offset:18432\n" \
  "ds_read_b128 a[84:87], v2 offset:19456\n" \
  "ds_read_b128 a[88:91], v2 offset:20480\n" \
  "ds_read_b128 a[92:95], v2 offset:21504\n" \
  "ds_read_b128 a[96:99], v2 offset:22528\n" \
  "ds_read_b128 a[100:103], v2 offset:23552\n" \
  "ds_read_b128 a[104:107], v2 offset:24576\n" \
  "ds_read_b128 a[108:111], v2 offset:25600\n" \
  "ds_read_b128 a[112:115], v2 offset:26624\n" \
  "ds_read_b128 a[116:119], v2 offset:27648\n" \
  "ds_read_b128 a[120:123], v2 offset:28672\n" \
  "ds_read_b128 a[124:127], v2 offset:29696\n" \
  "ds_read_b128 a[128:131], v2 offset:30720\n" \
  "ds_read_b128 a[132:135], v2 offset:31744\n" \
  "s_waitcnt vmcnt(0) lgkmcnt(0)\n" \
  "v_max3_u32 v42, v8, v9, v10\n" \
  "v_max3_u32 v43, v11, v12, v13\n" \
  "v_max3_u32 v42, v42, v14, v15\n" \
  "v_max_u32 v42, v42, v43\n" \
  "v_cmp_eq_u32 vcc, -1, v42\n" \
  "s_nop 1\n" \
  ".LA_FINAL:\n" \
  "s_mov_b64 s[92:93], vcc\n" \
  "v_mfma_f32_16x16x32_bf16 v[44:47], a[224:227], v[8:11], 0\n" \
  "v_mov_b32 v41, 0xff800000\n" \
  "v_mfma_f32_16x16x32_bf16 v[44:47], a[228:231], v[12:15], v[44:47]\n" \
  "v_mov_b32 v42, s64\n" \
  "v_mfma_f32_16x16x32_bf16 v[48:51], a[232:235], v[8:11], 0\n" \
  "v_mfma_f32_16x16x32_bf16 v[48:51], a[236:239], v[12:15], v[48:51]\n" \
  "v_mov_b32_dpp v41, v42 quad_perm:[0,1,2,3] row_mask:0xf bank_mask:0x4\n" \
  "v_mfma_f32_16x16x32_bf16 v[52:55], a[240:243], v[8:11], 0\n" \
  "s_nop 2\n" \
  "v_mov_b32_dpp v37, v44 quad_perm:[0,0,0,0] row_mask:0xf bank_mask:0x1\n" \
  "v_mov_b32_dpp v42, v45 quad_perm:[1,1,1,1] row_mask:0xf bank_mask:0x1\n" \
  "v_mfma_f32_16x16x32_bf16 v[52:55], a[244:247], v[12:15], v[52:55]\n" \
  "v_add_f32_dpp v37, v46, v37 quad_perm:[2,2,2,2] row_mask:0xf bank_mask:0x1\n" \
  "v_add_f32_dpp v42, v47, v42 quad_perm:[3,3,3,3] row_mask:0xf bank_mask:0x1\n" \
  "v_mfma_f32_16x16x32_bf16 v[44:47], a[248:251], v[8:11], 0\n" \
  "v_mov_b32_dpp v37, v48 quad_perm:[0,0,0,0] row_mask:0xf bank_mask:0x2\n" \
  "v_mov_b32_dpp v42, v49 quad_perm:[1,1,1,1] row_mask:0xf bank_mask:0x2\n" \
  "v_mfma_f32_16x16x32_bf16 v[44:47], a[252:255], v[12:15], v[44:47]\n" \
  "v_add_f32_dpp v37, v50, v37 quad_perm:[2,2,2,2] row_mask:0xf bank_mask:0x2\n" \
  "v_add_f32_dpp v42, v51, v42 quad_perm:[3,3,3,3] row_mask:0xf bank_mask:0x2\n" \
  "v_mfma_f32_16x16x32_bf16 v[48:51], v[64:67], v[8:11], 0\n" \
  "v_mov_b32_dpp v37, v52 quad_perm:[0,0,0,0] row_mask:0xf bank_mask:0x4\n" \
  "v_mov_b32_dpp v42, v53 quad_perm:[1,1,1,1] row_mask:0xf bank_mask:0x4\n" \
  "v_mfma_f32_16x16x32_bf16 v[48:51], v[68:71], v[12:15], v[48:51]\n" \
  "v_add_f32_dpp v37, v54, v37 quad_perm:[2,2,2,2] row_mask:0xf bank_mask:0x4\n" \
  "v_add_f32_dpp v42, v55, v42 quad_perm:[3,3,3,3] row_mask:0xf bank_mask:0x4\n" \
  "v_mfma_f32_16x16x32_bf16 v[52:55], v[72:75], v[8:11], 0\n" \
  "v_mov_b32_dpp v37, v44 quad_perm:[0,0,0,0] row_mask:0xf bank_mask:0x8\n" \
  "v_mov_b32_dpp v42, v45 quad_perm:[1,1,1,1] row_mask:0xf bank_mask:0x8\n" \
  "v_mfma_f32_16x16x32_bf16 v[52:55], v[76:79], v[12:15], v[52:55]\n" \
  "v_add_f32_dpp v37, v46, v37 quad_perm:[2,2,2,2] row_mask:0xf bank_mask:0x8\n" \
  "v_add_f32_dpp v42, v47, v42 quad_perm:[3,3,3,3] row_mask:0xf bank_mask:0x8\n" \
  "v_mfma_f32_16x16x32_bf16 v[44:47], v[80:83], v[8:11], 0\n" \
  "v_mov_b32_dpp v38, v48 quad_perm:[0,0,0,0] row_mask:0xf bank_mask:0x1\n" \
  "v_mov_b32_dpp v43, v49 quad_perm:[1,1,1,1] row_mask:0xf bank_mask:0x1\n" \
  "v_mfma_f32_16x16x32_bf16 v[44:47], v[84:87], v[12:15], v[44:47]\n" \
  "v_add_f32_dpp v38, v50, v38 quad_perm:[2,2,2,2] row_mask:0xf bank_mask:0x1\n" \
  "v_add_f32_dpp v43, v51, v43 quad_perm:[3,3,3,3] row_mask:0xf bank_mask:0x1\n" \
  "v_add_f32 v37, v37, v42\n" \
  "v_mfma_f32_16x16x32_bf16 v[48:51], v[88:91], v[8:11], 0\n" \
  "v_mov_b32_dpp v38, v52 quad_perm:[0,0,0,0] row_mask:0xf bank_mask:0x2\n" \
  "v_mov_b32_dpp v43, v53 quad_perm:[1,1,1,1] row_mask:0xf bank_mask:0x2\n" \
  "v_mfma_f32_16x16x32_bf16 v[48:51], v[92:95], v[12:15], v[48:51]\n" \
  "v_add_f32_dpp v38, v54, v38 quad_perm:[2,2,2,2] row_mask:0xf bank_mask:0x2\n" \
  "v_add_f32_dpp v43, v55, v43 quad_perm:[3,3,3,3] row_mask:0xf bank_mask:0x2\n" \
  "v_mfma_f32_16x16x32_bf16 v[52:55], v[96:99], v[8:11], 0\n" \
  "v_mov_b32_dpp v38, v44 quad_perm:[0,0,0,0] row_mask:0xf bank_mask:0x4\n" \
  "v_mov_b32_dpp v43, v45 quad_perm:[1,1,1,1] row_mask:0xf bank_mask:0x4\n" \
  "v_mfma_f32_16x16x32_bf16 v[52:55], v[100:103], v[12:15], v[52:55]\n" \
  "v_add_f32_dpp v38, v46, v38 quad_perm:[2,2,2,2] row_mask:0xf bank_mask:0x4\n" \
  "v_add_f32_dpp v43, v47, v43 quad_perm:[3,3,3,3] row_mask:0xf bank_mask:0x4\n" \
  "v_mfma_f32_16x16x32_bf16 v[44:47], v[104:107], v[8:11], 0\n" \
  "v_mov_b32_dpp v38, v48 quad_perm:[0,0,0,0] row_mask:0xf bank_mask:0x8\n" \
  "v_mov_b32_dpp v43, v49 quad_perm:[1,1,1,1] row_mask:0xf bank_mask:0x8\n" \
  "v_mfma_f32_16x16x32_bf16 v[44:47], v[108:111], v[12:15], v[44:47]\n" \
  "v_add_f32_dpp v38, v50, v38 quad_perm:[2,2,2,2] row_mask:0xf bank_mask:0x8\n" \
  "v_add_f32_dpp v43, v51, v43 quad_perm:[3,3,3,3] row_mask:0xf bank_mask:0x8\n" \
  "v_mfma_f32_16x16x32_bf16 v[48:51], v[112:115], v[8:11], 0\n" \
  "v_mov_b32_dpp v39, v52 quad_perm:[0,0,0,0] row_mask:0xf bank_mask:0x1\n" \
  "v_mov_b32_dpp v42, v53 quad_perm:[1,1,1,1] row_mask:0xf bank_mask:0x1\n" \
  "v_mfma_f32_16x16x32_bf16 v[48:51], v[116:119], v[12:15], v[48:51]\n" \
  "v_add_f32_dpp v39, v54, v39 quad_perm:[2,2,2,2] row_mask:0xf bank_mask:0x1\n" \
  "v_add_f32_dpp v42, v55, v42 quad_perm:[3,3,3,3] row_mask:0xf bank_mask:0x1\n" \
  "v_add_f32 v38, v38, v43\n" \
  "v_mfma_f32_16x16x32_bf16 v[52:55], v[120:123], v[8:11], 0\n" \
  "v_mov_b32_dpp v39, v44 quad_perm:[0,0,0,0] row_mask:0xf bank_mask:0x2\n" \
  "v_mov_b32_dpp v42, v45 quad_perm:[1,1,1,1] row_mask:0xf bank_mask:0x2\n" \
  "v_mfma_f32_16x16x32_bf16 v[52:55], v[124:127], v[12:15], v[52:55]\n" \
  "v_add_f32_dpp v39, v46, v39 quad_perm:[2,2,2,2] row_mask:0xf bank_mask:0x2\n" \
  "v_add_f32_dpp v42, v47, v42 quad_perm:[3,3,3,3] row_mask:0xf bank_mask:0x2\n" \
  "v_mfma_f32_16x16x32_bf16 v[44:47], v[128:131], v[8:11], 0\n" \
  "v_mov_b32_dpp v39, v48 quad_perm:[0,0,0,0] row_mask:0xf bank_mask:0x4\n" \
  "v_mov_b32_dpp v42, v49 quad_perm:[1,1,1,1] row_mask:0xf bank_mask:0x4\n" \
  "v_mfma_f32_16x16x32_bf16 v[44:47], v[132:135], v[12:15], v[44:47]\n" \
  "v_add_f32_dpp v39, v50, v39 quad_perm:[2,2,2,2] row_mask:0xf bank_mask:0x4\n" \
  "v_add_f32_dpp v42, v51, v42 quad_perm:[3,3,3,3] row_mask:0xf bank_mask:0x4\n" \
  "v_mfma_f32_16x16x32_bf16 v[48:51], v[136:139], v[8:11], 0\n" \
  "v_mov_b32_dpp v39, v52 quad_perm:[0,0,0,0] row_mask:0xf bank_mask:0x8\n" \
  "v_mov_b32_dpp v42, v53 quad_perm:[1,1,1,1] row_mask:0xf bank_mask:0x8\n" \
  "v_mfma_f32_16x16x32_bf16 v[48:51], v[140:143], v[12:15], v[48:51]\n" \
  "v_add_f32_dpp v39, v54, v39 quad_perm:[2,2,2,2] row_mask:0xf bank_mask:0x8\n" \
  "v_add_f32_dpp v42, v55, v42 quad_perm:[3,3,3,3] row_mask:0xf bank_mask:0x8\n" \
  "v_mfma_f32_16x16x32_bf16 v[52:55], v[144:147], v[8:11], 0\n" \
  "v_mov_b32_dpp v40, v44 quad_perm:[0,0,0,0] row_mask:0xf bank_mask:0x1\n" \
  "v_mov_b32_dpp v43, v45 quad_perm:[1,1,1,1] row_mask:0xf bank_mask:0x1\n" \
  "v_mfma_f32_16x16x32_bf16 v[52:55], v[148:151], v[12:15], v[52:55]\n" \
  "v_add_f32_dpp v40, v46, v40 quad_perm:[2,2,2,2] row_mask:0xf bank_mask:0x1\n" \
  "v_add_f32_dpp v43, v47, v43 quad_perm:[3,3,3,3] row_mask:0xf bank_mask:0x1\n" \
  "v_add_f32 v39, v39, v42\n" \
  "v_mfma_f32_16x16x32_bf16 v[44:47], v[152:155], v[8:11], 0\n" \
  "v_mov_b32_dpp v40, v48 quad_perm:[0,0,0,0] row_mask:0xf bank_mask:0x2\n" \
  "v_mov_b32_dpp v43, v49 quad_perm:[1,1,1,1] row_mask:0xf bank_mask:0x2\n" \
  "v_mfma_f32_16x16x32_bf16 v[44:47], v[156:159], v[12:15], v[44:47]\n" \
  "v_add_f32_dpp v40, v50, v40 quad_perm:[2,2,2,2] row_mask:0xf bank_mask:0x2\n" \
  "v_add_f32_dpp v43, v51, v43 quad_perm:[3,3,3,3] row_mask:0xf bank_mask:0x2\n" \
  "v_mfma_f32_16x16x32_bf16 v[48:51], v[160:163], v[8:11], 0\n" \
  "v_mov_b32_dpp v40, v52 quad_perm:[0,0,0,0] row_mask:0xf bank_mask:0x4\n" \
  "v_mov_b32_dpp v43, v53 quad_perm:[1,1,1,1] row_mask:0xf bank_mask:0x4\n" \
  "v_mfma_f32_16x16x32_bf16 v[48:51], v[164:167], v[12:15], v[48:51]\n" \
  "v_add_f32_dpp v40, v54, v40 quad_perm:[2,2,2,2] row_mask:0xf bank_mask:0x4\n" \
  "v_add_f32_dpp v43, v55, v43 quad_perm:[3,3,3,3] row_mask:0xf bank_mask:0x4\n" \
  "v_mfma_f32_16x16x32_bf16 v[52:55], v[168:171], v[8:11], 0\n" \
  "v_mov_b32_dpp v40, v44 quad_perm:[0,0,0,0] row_mask:0xf bank_mask:0x8\n" \
  "v_mov_b32_dpp v43, v45 quad_perm:[1,1,1,1] row_mask:0xf bank_mask:0x8\n" \
  "v_mfma_f32_16x16x32_bf16 v[52:55], v[172:175], v[12:15], v[52:55]\n" \
  "v_add_f32_dpp v40, v46, v40 quad_perm:[2,2,2,2] row_mask:0xf bank_mask:0x8\n" \
  "v_add_f32_dpp v43, v47, v43 quad_perm:[3,3,3,3] row_mask:0xf bank_mask:0x8\n" \
  "v_mfma_f32_16x16x32_bf16 v[44:47], a[0:3], v[8:11], 0\n" \
  "v_mov_b32_dpp v41, v48 quad_perm:[0,0,0,0] row_mask:0xf bank_mask:0x1\n" \
  "v_mov_b32_dpp v42, v49 quad_perm:[1,1,1,1] row_mask:0xf bank_mask:0x1\n" \
  "v_mfma_f32_16x16x32_bf16 v[44:47], a[4:7], v[12:15], v[44:47]\n" \
  "v_add_f32_dpp v41, v50, v41 quad_perm:[2,2,2,2] row_mask:0xf bank_mask:0x1\n" \
  "v_add_f32_dpp v42, v51, v42 quad_perm:[3,3,3,3] row_mask:0xf bank_mask:0x1\n" \
  "v_add_f32 v40, v40, v43\n" \
  "v_mov_b32_dpp v41, v52 quad_perm:[0,0,0,0] row_mask:0xf bank_mask:0x2\n" \
  "v_mov_b32_dpp v42, v53 quad_perm:[1,1,1,1] row_mask:0xf bank_mask:0x2\n" \
  "s_nop 0\n" \
  "v_add_f32_dpp v41, v54, v41 quad_perm:[2,2,2,2] row_mask:0xf bank_mask:0x2\n" \
  "v_add_f32_dpp v42, v55, v42 quad_perm:[3,3,3,3] row_mask:0xf bank_mask:0x2\n" \
  "s_nop 0\n" \
  "v_add_f32_dpp v41, v44, v41 quad_perm:[0,0,0,0] row_mask:0xf bank_mask:0x4\n" \
  "v_mov_b32_dpp v42, v45 quad_perm:[1,1,1,1] row_mask:0xf bank_mask:0x4\n" \
  "s_nop 0\n" \
  "v_add_f32_dpp v41, v46, v41 quad_perm:[2,2,2,2] row_mask:0xf bank_mask:0x4\n" \
  "v_add_f32_dpp v42, v47, v42 quad_perm:[3,3,3,3] row_mask:0xf bank_mask:0x4\n" \
  "v_add_f32 v41, v41, v42\n" \
  "v_max3_f32 v42, v37, v38, v39\n" \
  "v_max3_f32 v42, v42, v40, v41\n" \
  "s_nop 1\n" \
  "v_max_f32_dpp v42, v42, v42 row_ror:4 row_mask:0xf bank_mask:0xf\n" \
  "s_nop 1\n" \
  "v_max_f32_dpp v42, v42, v42 row_ror:8 row_mask:0xf bank_mask:0xf\n" \
  "s_nop 1\n" \
  "v_max_f32_dpp v42, v42, v42 row_bcast:15 row_mask:0xa bank_mask:0xf\n" \
  "s_nop 1\n" \
  "v_max_f32_dpp v42, v42, v42 row_bcast:31 row_mask:0xc bank_mask:0xf\n" \
  "s_nop 0\n" \
  "v_readlane_b32 s69, v42, 63\n" \
  "s_nop 1\n" \
  "v_subrev_f32 v37, s69, v37\n" \
  "v_subrev_f32 v38, s69, v38\n" \
  "v_subrev_f32 v39, s69, v39\n" \
  "v_subrev_f32 v40, s69, v40\n" \
  "v_subrev_f32 v41, s69, v41\n" \
  "v_exp_f32 v37, v37\n" \
  "v_exp_f32 v38, v38\n" \
  "v_exp_f32 v39, v39\n" \
  "v_exp_f32 v40, v40\n" \
  "v_exp_f32 v41, v41\n" \
  "v_add_f32 v8, v37, v38\n" \
  "v_add_f32 v9, v39, v40\n" \
  "v_cvt_pk_bf16_f32 v37, v37, v37\n" \
  "v_cvt_pk_bf16_f32 v38, v38, v38\n" \
  "v_cvt_pk_bf16_f32 v39, v39, v39\n" \
  "v_cvt_pk_bf16_f32 v40, v40, v40\n" \
  "v_add_f32 v9, v9, v41\n" \
  "v_add_f32 v8, v8, v9\n" \
  "v_cvt_pk_bf16_f32 v41, v41, v41\n" \
  "v_and_b32_dpp v44, v39, v4 row_newbcast:0 row_mask:0xf bank_mask:0xf\n" \
  "v_and_b32_dpp v45, v39, v5 row_newbcast:0 row_mask:0xf bank_mask:0xf\n" \
  "v_and_b32_dpp v46, v39, v6 row_newbcast:0 row_mask:0xf bank_mask:0xf\n" \
  "v_and_b32_dpp v47, v39, v7 row_newbcast:0 row_mask:0xf bank_mask:0xf\n" \
  "v_and_b32_dpp v48, v39, v4 row_newbcast:4 row_mask:0xf bank_mask:0xf\n" \
  "v_and_b32_dpp v49, v39, v5 row_newbcast:4 row_mask:0xf bank_mask:0xf\n" \
  "v_mfma_f32_16x16x32_bf16 v[24:27], v[44:47], v[176:179], 0\n" \
  "v_and_b32_dpp v50, v39, v6 row_newbcast:4 row_mask:0xf bank_mask:0xf\n" \
  "v_and_b32_dpp v51, v39, v7 row_newbcast:4 row_mask:0xf bank_mask:0xf\n" \
  "v_add_f32_dpp v8, v8, v8 row_ror:4 row_mask:0xf bank_mask:0xf\n" \
  "v_mfma_f32_16x16x32_bf16 v[28:31], v[44:47], v[180:183], 0\n" \
  "v_and_b32_dpp v44, v39, v4 row_newbcast:8 row_mask:0xf bank_mask:0xf\n" \
  "v_and_b32_dpp v45, v39, v5 row_newbcast:8 row_mask:0xf bank_mask:0xf\n" \
  "v_mfma_f32_16x16x32_bf16 v[24:27], v[48:51], v[184:187], v[24:27]\n" \
  "v_and_b32_dpp v46, v39, v6 row_newbcast:8 row_mask:0xf bank_mask:0xf\n" \
  "v_and_b32_dpp v47, v39, v7 row_newbcast:8 row_mask:0xf bank_mask:0xf\n" \
  "v_add_f32_dpp v8, v8, v8 row_ror:8 row_mask:0xf bank_mask:0xf\n" \
  "v_mfma_f32_16x16x32_bf16 v[28:31], v[48:51], v[188:191], v[28:31]\n" \
  "v_and_b32_dpp v48, v39, v4 row_newbcast:12 row_mask:0xf bank_mask:0xf\n" \
  "v_and_b32_dpp v49, v39, v5 row_newbcast:12 row_mask:0xf bank_mask:0xf\n" \
  "v_mfma_f32_16x16x32_bf16 v[24:27], v[44:47], v[192:195], v[24:27]\n" \
  "v_and_b32_dpp v50, v39, v6 row_newbcast:12 row_mask:0xf bank_mask:0xf\n" \
  "v_and_b32_dpp v51, v39, v7 row_newbcast:12 row_mask:0xf bank_mask:0xf\n" \
  "v_add_f32_dpp v8, v8, v8 row_bcast:15 row_mask:0xa bank_mask:0xf\n" \
  "v_mfma_f32_16x16x32_bf16 v[28:31], v[44:47], v[196:199], v[28:31]\n" \
  "v_and_b32_dpp v44, v40, v4 row_newbcast:0 row_mask:0xf bank_mask:0xf\n" \
  "v_and_b32_dpp v45, v40, v5 row_newbcast:0 row_mask:0xf bank_mask:0xf\n" \
  "v_mfma_f32_16x16x32_bf16 v[24:27], v[48:51], v[200:203], v[24:27]\n" \
  "v_and_b32_dpp v46, v40, v6 row_newbcast:0 row_mask:0xf bank_mask:0xf\n" \
  "v_and_b32_dpp v47, v40, v7 row_newbcast:0 row_mask:0xf bank_mask:0xf\n" \
  "v_add_f32_dpp v8, v8, v8 row_bcast:31 row_mask:0xc bank_mask:0xf\n" \
  "v_mfma_f32_16x16x32_bf16 v[28:31], v[48:51], v[204:207], v[28:31]\n" \
  "v_and_b32_dpp v48, v40, v4 row_newbcast:4 row_mask:0xf bank_mask:0xf\n" \
  "v_and_b32_dpp v49, v40, v5 row_newbcast:4 row_mask:0xf bank_mask:0xf\n" \
  "v_mfma_f32_16x16x32_bf16 v[24:27], v[44:47], v[208:211], v[24:27]\n" \
  "v_and_b32_dpp v50, v40, v6 row_newbcast:4 row_mask:0xf bank_mask:0xf\n" \
  "v_and_b32_dpp v51, v40, v7 row_newbcast:4 row_mask:0xf bank_mask:0xf\n" \
  "v_readlane_b32 s71, v8, 63\n" \
  "v_mfma_f32_16x16x32_bf16 v[28:31], v[44:47], v[212:215], v[28:31]\n" \
  "v_and_b32_dpp v44, v40, v4 row_newbcast:8 row_mask:0xf bank_mask:0xf\n" \
  "v_and_b32_dpp v45, v40, v5 row_newbcast:8 row_mask:0xf bank_mask:0xf\n" \
  "v_mfma_f32_16x16x32_bf16 v[24:27], v[48:51], v[216:219], v[24:27]\n" \
  "v_and_b32_dpp v46, v40, v6 row_newbcast:8 row_mask:0xf bank_mask:0xf\n" \
  "v_and_b32_dpp v47, v40, v7 row_newbcast:8 row_mask:0xf bank_mask:0xf\n" \
  "v_mfma_f32_16x16x32_bf16 v[28:31], v[48:51], v[220:223], v[28:31]\n" \
  "v_and_b32_dpp v48, v40, v4 row_newbcast:12 row_mask:0xf bank_mask:0xf\n" \
  "v_and_b32_dpp v49, v40, v5 row_newbcast:12 row_mask:0xf bank_mask:0xf\n" \
  "v_mfma_f32_16x16x32_bf16 v[24:27], v[44:47], v[224:227], v[24:27]\n" \
  "v_and_b32_dpp v50, v40, v6 row_newbcast:12 row_mask:0xf bank_mask:0xf\n" \
  "v_and_b32_dpp v51, v40, v7 row_newbcast:12 row_mask:0xf bank_mask:0xf\n" \
  "v_mfma_f32_16x16x32_bf16 v[28:31], v[44:47], v[228:231], v[28:31]\n" \
  "v_and_b32_dpp v44, v41, v4 row_newbcast:0 row_mask:0xf bank_mask:0xf\n" \
  "v_and_b32_dpp v45, v41, v5 row_newbcast:0 row_mask:0xf bank_mask:0xf\n" \
  "v_mfma_f32_16x16x32_bf16 v[24:27], v[48:51], v[232:235], v[24:27]\n" \
  "v_and_b32_dpp v46, v41, v6 row_newbcast:0 row_mask:0xf bank_mask:0xf\n" \
  "v_and_b32_dpp v47, v41, v7 row_newbcast:0 row_mask:0xf bank_mask:0xf\n" \
  "v_mfma_f32_16x16x32_bf16 v[28:31], v[48:51], v[236:239], v[28:31]\n" \
  "v_and_b32_dpp v48, v41, v4 row_newbcast:4 row_mask:0xf bank_mask:0xf\n" \
  "v_and_b32_dpp v49, v41, v5 row_newbcast:4 row_mask:0xf bank_mask:0xf\n" \
  "v_mfma_f32_16x16x32_bf16 v[24:27], v[44:47], v[240:243], v[24:27]\n" \
  "v_and_b32_dpp v50, v41, v6 row_newbcast:4 row_mask:0xf bank_mask:0xf\n" \
  "v_and_b32_dpp v51, v41, v7 row_newbcast:4 row_mask:0xf bank_mask:0xf\n" \
  "v_mfma_f32_16x16x32_bf16 v[28:31], v[44:47], v[244:247], v[28:31]\n" \
  "v_and_b32_dpp v44, v41, v4 row_newbcast:8 row_mask:0xf bank_mask:0xf\n" \
  "v_and_b32_dpp v45, v41, v5 row_newbcast:8 row_mask:0xf bank_mask:0xf\n" \
  "v_mfma_f32_16x16x32_bf16 v[24:27], v[48:51], v[248:251], v[24:27]\n" \
  "v_and_b32_dpp v46, v41, v6 row_newbcast:8 row_mask:0xf bank_mask:0xf\n" \
  "v_and_b32_dpp v47, v41, v7 row_newbcast:8 row_mask:0xf bank_mask:0xf\n" \
  "v_mfma_f32_16x16x32_bf16 v[28:31], v[48:51], v[252:255], v[28:31]\n" \
  "v_and_b32_dpp v48, v37, v4 row_newbcast:0 row_mask:0xf bank_mask:0xf\n" \
  "v_and_b32_dpp v49, v37, v5 row_newbcast:0 row_mask:0xf bank_mask:0xf\n" \
  "v_mfma_f32_16x16x32_bf16 v[24:27], v[44:47], a[136:139], v[24:27]\n" \
  "v_and_b32_dpp v50, v37, v6 row_newbcast:0 row_mask:0xf bank_mask:0xf\n" \
  "v_and_b32_dpp v51, v37, v7 row_newbcast:0 row_mask:0xf bank_mask:0xf\n" \
  "v_mfma_f32_16x16x32_bf16 v[28:31], v[44:47], a[140:143], v[28:31]\n" \
  "v_and_b32_dpp v44, v37, v4 row_newbcast:4 row_mask:0xf bank_mask:0xf\n" \
  "v_and_b32_dpp v45, v37, v5 row_newbcast:4 row_mask:0xf bank_mask:0xf\n" \
  "v_mfma_f32_16x16x32_bf16 v[24:27], v[48:51], a[72:75], v[24:27]\n" \
  "v_and_b32_dpp v46, v37, v6 row_newbcast:4 row_mask:0xf bank_mask:0xf\n" \
  "v_and_b32_dpp v47, v37, v7 row_newbcast:4 row_mask:0xf bank_mask:0xf\n" \
  "v_mfma_f32_16x16x32_bf16 v[28:31], v[48:51], a[76:79], v[28:31]\n" \
  "v_and_b32_dpp v48, v37, v4 row_newbcast:8 row_mask:0xf bank_mask:0xf\n" \
  "v_and_b32_dpp v49, v37, v5 row_newbcast:8 row_mask:0xf bank_mask:0xf\n" \
  "v_mfma_f32_16x16x32_bf16 v[24:27], v[44:47], a[80:83], v[24:27]\n" \
  "v_and_b32_dpp v50, v37, v6 row_newbcast:8 row_mask:0xf bank_mask:0xf\n" \
  "v_and_b32_dpp v51, v37, v7 row_newbcast:8 row_mask:0xf bank_mask:0xf\n" \
  "v_mfma_f32_16x16x32_bf16 v[28:31], v[44:47], a[84:87], v[28:31]\n" \
  "v_and_b32_dpp v44, v37, v4 row_newbcast:12 row_mask:0xf bank_mask:0xf\n" \
  "v_and_b32_dpp v45, v37, v5 row_newbcast:12 row_mask:0xf bank_mask:0xf\n" \
  "v_mfma_f32_16x16x32_bf16 v[24:27], v[48:51], a[88:91], v[24:27]\n" \
  "v_and_b32_dpp v46, v37, v6 row_newbcast:12 row_mask:0xf bank_mask:0xf\n" \
  "v_and_b32_dpp v47, v37, v7 row_newbcast:12 row_mask:0xf bank_mask:0xf\n" \
  "v_mfma_f32_16x16x32_bf16 v[28:31], v[48:51], a[92:95], v[28:31]\n" \
  "v_and_b32_dpp v48, v38, v4 row_newbcast:0 row_mask:0xf bank_mask:0xf\n" \
  "v_and_b32_dpp v49, v38, v5 row_newbcast:0 row_mask:0xf bank_mask:0xf\n" \
  "v_mfma_f32_16x16x32_bf16 v[24:27], v[44:47], a[96:99], v[24:27]\n" \
  "v_and_b32_dpp v50, v38, v6 row_newbcast:0 row_mask:0xf bank_mask:0xf\n" \
  "v_and_b32_dpp v51, v38, v7 row_newbcast:0 row_mask:0xf bank_mask:0xf\n" \
  "v_mfma_f32_16x16x32_bf16 v[28:31], v[44:47], a[100:103], v[28:31]\n" \
  "v_and_b32_dpp v44, v38, v4 row_newbcast:4 row_mask:0xf bank_mask:0xf\n" \
  "v_and_b32_dpp v45, v38, v5 row_newbcast:4 row_mask:0xf bank_mask:0xf\n" \
  "v_mfma_f32_16x16x32_bf16 v[24:27], v[48:51], a[104:107], v[24:27]\n" \
  "v_and_b32_dpp v46, v38, v6 row_newbcast:4 row_mask:0xf bank_mask:0xf\n" \
  "v_and_b32_dpp v47, v38, v7 row_newbcast:4 row_mask:0xf bank_mask:0xf\n" \
  "v_mfma_f32_16x16x32_bf16 v[28:31], v[48:51], a[108:111], v[28:31]\n" \
  "v_and_b32_dpp v48, v38, v4 row_newbcast:8 row_mask:0xf bank_mask:0xf\n" \
  "v_and_b32_dpp v49, v38, v5 row_newbcast:8 row_mask:0xf bank_mask:0xf\n" \
  "v_mfma_f32_16x16x32_bf16 v[24:27], v[44:47], a[112:115], v[24:27]\n" \
  "v_and_b32_dpp v50, v38, v6 row_newbcast:8 row_mask:0xf bank_mask:0xf\n" \
  "v_and_b32_dpp v51, v38, v7 row_newbcast:8 row_mask:0xf bank_mask:0xf\n" \
  "v_mfma_f32_16x16x32_bf16 v[28:31], v[44:47], a[116:119], v[28:31]\n" \
  "v_and_b32_dpp v44, v38, v4 row_newbcast:12 row_mask:0xf bank_mask:0xf\n" \
  "v_and_b32_dpp v45, v38, v5 row_newbcast:12 row_mask:0xf bank_mask:0xf\n" \
  "v_mfma_f32_16x16x32_bf16 v[24:27], v[48:51], a[120:123], v[24:27]\n" \
  "v_and_b32_dpp v46, v38, v6 row_newbcast:12 row_mask:0xf bank_mask:0xf\n" \
  "v_and_b32_dpp v47, v38, v7 row_newbcast:12 row_mask:0xf bank_mask:0xf\n" \
  "v_mfma_f32_16x16x32_bf16 v[28:31], v[48:51], a[124:127], v[28:31]\n" \
  "s_nop 0\n" \
  "v_mfma_f32_16x16x32_bf16 v[24:27], v[44:47], a[128:131], v[24:27]\n" \
  "v_mfma_f32_16x16x32_bf16 v[28:31], v[44:47], a[132:135], v[28:31]\n" \
  "s_mov_b32 s81, 155776\n" \
  "s_mov_b32 s82, s79\n" \
  "s_add_u32 s59, s87, 155776\n" \
  "s_sub_u32 s85, 155776, s67\n" \
  "v_add_u32 v42, 155776, v3\n" \
  "v_and_b32 v43, 0x30, v2\n" \
  "v_add_u32 v63, s85, v2\n" \
  "v_mov_b32 v52, 1\n" \
  "v_add_u32 v43, 155776, v43\n" \
  "v_mov_b32 v48, s69\n" \
  "v_mov_b32 v49, s71\n" \
  "v_mov_b32 v50, s59\n" \
  "v_mov_b32 v51, s81\n" \
  "v_cndmask_b32 v44, v24, v28, s[88:89]\n" \
  "v_cndmask_b32 v45, v25, v29, s[88:89]\n" \
  "v_cndmask_b32 v46, v26, v30, s[88:89]\n" \
  "v_cndmask_b32 v47, v27, v31, s[88:89]\n" \
  "s_cmp_eq_u64 s[92:93], 0\n" \
  "s_cbranch_scc0 .LA_FIN_WAIT\n" \
  ".LA_FIN_OK:\n" \
  "s_call_b64 s[94:95], .LA_MERGE\n" \
  "s_waitcnt vmcnt(0)\n" \
  "s_endpgm\n" \
  ".LA_FIN_WAIT:\n" \
  "s_cmp_eq_u32 s74, 0\n" \
  "s_cbranch_scc1 .LA_FIN_OK\n" \
  ".LA_FIN_PRE:\n" \
  "s_sleep 8\n" \
  "s_mov_b64 exec, 1\n" \
  "buffer_load_dword v42, off, s[52:55], s83 sc1\n" \
  "s_mov_b64 exec, -1\n" \
  "s_waitcnt vmcnt(0)\n" \
  "v_readfirstlane_b32 s59, v42\n" \
  "s_nop 0\n" \
  "s_cmp_lg_u32 s59, -1\n" \
  "s_nop 1\n" \
  "s_cbranch_scc1 .LA_FIN_POLL\n" \
  "s_sub_u32 s74, s74, 1\n" \
  "s_cbranch_scc0 .LA_FIN_PRE\n" \
  "s_mov_b32 s74, 0\n" \
  ".LA_FIN_POLL:\n" \
  "v_and_b32 v42, 0xc0, v0\n" \
  "v_and_b32 v43, 0xf0, v0\n" \
  "v_add_u32 v42, v42, v43\n" \
  "buffer_load_dwordx4 v[8:11], v42, s[52:55], s76 offen sc1\n" \
  "buffer_load_dwordx4 v[12:15], v42, s[52:55], s76 offen offset:512 sc1\n" \
  "s_waitcnt vmcnt(0)\n" \
  "v_max3_u32 v42, v8, v9, v10\n" \
  "v_max3_u32 v43, v11, v12, v13\n" \
  "v_max3_u32 v42, v42, v14, v15\n" \
  "v_max_u32 v42, v42, v43\n" \
  "v_cmp_eq_u32 vcc, -1, v42\n" \
  "s_nop 0\n" \
  "s_cbranch_vccz .LA_FINAL\n" \
  "s_sub_u32 s74, s74, 1\n" \
  "s_cbranch_scc0 .LA_FIN_POLL\n" \
  "s_mov_b32 s74, 0\n" \
  ".LA_FIN_GO:\n" \
  "s_branch .LA_FINAL\n" \
  ".LA_GATE:\n" \
  "s_add_u32 s33, s33, s27\n" \
  ".LA_GATE1:\n" \
  "s_memrealtime s[30:31]\n" \
  "s_waitcnt lgkmcnt(0)\n" \
  "s_lshl_b32 s30, s30, 6\n" \
  "s_sub_u32 s30, s30, s33\n" \
  "s_cmp_lt_i32 s30, 0\n" \
  "s_cbranch_scc1 .LA_GATE1\n" \
  "s_setpc_b64 s[34:35]\n" \
  ".LA_GUARD:\n" \
  "v_mov_b32 v43, 155716\n" \
  "ds_read_b32 v42, v43\n" \
  "ds_read_b32 v43, v43 offset:4224\n" \
  "s_waitcnt lgkmcnt(0)\n" \
  "v_add_u32 v43, 1, v43\n" \
  "v_min_u32 v42, v42, v43\n" \
  "v_cmp_gt_u32 vcc, s84, v42\n" \
  "s_nop 0\n" \
  "s_cbranch_vccz .LA_QW_OK\n" \
  "s_sub_u32 s74, s74, 1\n" \
  "s_cbranch_scc1 .LA_QW_OUT\n" \
  "s_sleep 4\n" \
  "s_branch .LA_GUARD\n" \
  ".LA_QWAIT:\n" \
  "v_max3_u32 v42, v8, v9, v10\n" \
  "v_max3_u32 v43, v11, v12, v13\n" \
  "v_max3_u32 v42, v42, v14, v15\n" \
  "v_max_u32 v42, v42, v43\n" \
  "v_cmp_eq_u32 vcc, -1, v42\n" \
  "s_nop 0\n" \
  "s_cbranch_vccz .LA_QW_OK\n" \
  "s_sub_u32 s74, s74, 1\n" \
  "s_cbranch_scc1 .LA_QW_OUT\n" \
  "s_sleep 8\n" \
  "v_and_b32 v42, 0xc0, v0\n" \
  "v_and_b32 v43, 0xf0, v0\n" \
  "v_add_u32 v42, v42, v43\n" \
  "buffer_load_dwordx4 v[8:11], v42, s[52:55], s86 offen sc1\n" \
  "buffer_load_dwordx4 v[12:15], v42, s[52:55], s86 offen offset:512 sc1\n" \
  "s_waitcnt vmcnt(0)\n" \
  "s_branch .LA_QWAIT\n" \
  ".LA_QW_OUT:\n" \
  "s_mov_b32 s74, 0\n" \
  ".LA_QW_OK:\n" \
  "s_setpc_b64 s[94:95]\n" \
  ".LA_MERGE:\n" \
  "ds_write_b128 v42, v[44:47]\n" \
  "s_mov_b64 exec, 1\n" \
  "ds_write_b64 v50, v[48:49] offset:4096\n" \
  "ds_add_rtn_u32 v62, v51, v52 offset:4160\n" \
  "s_mov_b64 exec, -1\n" \
  "ds_read_b64 v[60:61], v43 offset:4096\n" \
  "ds_read_b128 v[44:47], v63 offset:0\n" \
  "ds_read_b128 v[48:51], v63 offset:1024\n" \
  "ds_read_b128 v[52:55], v63 offset:2048\n" \
  "ds_read_b128 v[56:59], v63 offset:3072\n" \
  "v_subrev_u32 v42, s67, v2\n" \
  "v_lshrrev_b32 v42, 1, v42\n" \
  "s_waitcnt lgkmcnt(4)\n" \
  "v_readfirstlane_b32 s59, v62\n" \
  "v_max_f32_dpp v62, v60, v60 quad_perm:[1,0,3,2] row_mask:0xf bank_mask:0xf\n" \
  "s_and_b32 s59, s59, 3\n" \
  "s_cmp_eq_u32 s59, 3\n" \
  "s_cbranch_scc0 .LA_MERGE_NO\n" \
  "v_max_f32_dpp v62, v62, v62 quad_perm:[2,3,0,1] row_mask:0xf bank_mask:0xf\n" \
  "v_sub_f32 v60, v60, v62\n" \
  "v_exp_f32 v60, v60\n" \
  "v_mov_b32 v43, s81\n" \
  "v_mul_f32 v63, v60, v61\n" \
  "s_waitcnt lgkmcnt(0)\n" \
  "v_mul_f32_dpp v44, v60, v44 quad_perm:[0,0,0,0] row_mask:0xf bank_mask:0xf\n" \
  "v_mul_f32_dpp v45, v60, v45 quad_perm:[0,0,0,0] row_mask:0xf bank_mask:0xf\n" \
  "v_mul_f32_dpp v46, v60, v46 quad_perm:[0,0,0,0] row_mask:0xf bank_mask:0xf\n" \
  "v_mul_f32_dpp v47, v60, v47 quad_perm:[0,0,0,0] row_mask:0xf bank_mask:0xf\n" \
  "v_fmac_f32_dpp v44, v60, v48 quad_perm:[1,1,1,1] row_mask:0xf bank_mask:0xf\n" \
  "v_fmac_f32_dpp v45, v60, v49 quad_perm:[1,1,1,1] row_mask:0xf bank_mask:0xf\n" \
  "v_fmac_f32_dpp v46, v60, v50 quad_perm:[1,1,1,1] row_mask:0xf bank_mask:0xf\n" \
  "v_fmac_f32_dpp v47, v60, v51 quad_perm:[1,1,1,1] row_mask:0xf bank_mask:0xf\n" \
  "v_fmac_f32_dpp v44, v60, v52 quad_perm:[2,2,2,2] row_mask:0xf bank_mask:0xf\n" \
  "v_fmac_f32_dpp v45, v60, v53 quad_perm:[2,2,2,2] row_mask:0xf bank_mask:0xf\n" \
  "v_fmac_f32_dpp v46, v60, v54 quad_perm:[2,2,2,2] row_mask:0xf bank_mask:0xf\n" \
  "v_fmac_f32_dpp v47, v60, v55 quad_perm:[2,2,2,2] row_mask:0xf bank_mask:0xf\n" \
  "v_fmac_f32_dpp v44, v60, v56 quad_perm:[3,3,3,3] row_mask:0xf bank_mask:0xf\n" \
  "v_fmac_f32_dpp v45, v60, v57 quad_perm:[3,3,3,3] row_mask:0xf bank_mask:0xf\n" \
  "v_fmac_f32_dpp v46, v60, v58 quad_perm:[3,3,3,3] row_mask:0xf bank_mask:0xf\n" \
  "v_fmac_f32_dpp v47, v60, v59 quad_perm:[3,3,3,3] row_mask:0xf bank_mask:0xf\n" \
  "v_cvt_pk_bf16_f32 v44, v44, v45\n" \
  "v_cvt_pk_bf16_f32 v45, v46, v47\n" \
  "v_add_f32_dpp v63, v63, v63 quad_perm:[1,0,3,2] row_mask:0xf bank_mask:0xf\n" \
  "v_min_u32 v44, -2, v44\n" \
  "v_min_u32 v45, -2, v45\n" \
  "buffer_store_dwordx2 v[44:45], v42, s[52:55], s82 offen sc1\n" \
  "v_add_f32_dpp v63, v63, v63 quad_perm:[2,3,0,1] row_mask:0xf bank_mask:0xf\n" \
  "v_min_u32 v62, -2, v62\n" \
  "v_mov_b32 v61, 1\n" \
  "v_min_u32 v63, -2, v63\n" \
  "s_mov_b64 exec, 1\n" \
  "buffer_store_dwordx2 v[62:63], off, s[52:55], s82 offset:512 sc1\n" \
  "ds_add_u32 v43, v61 offset:4164\n" \
  "s_mov_b64 exec, -1\n" \
  "s_setpc_b64 s[94:95]\n" \
  ".LA_MERGE_NO:\n" \
  "buffer_store_dword v42, off, s[48:51], 0\n" \
  "buffer_store_dword v42, off, s[48:51], 0\n" \
  "s_waitcnt lgkmcnt(0)\n" \
  "s_setpc_b64 s[94:95]\n" \
  ".LS_START:\n" \
  "s_getpc_b64 s[0:1]\n" \
  ".LS_HERE:\n" \
  "s_sub_u32 s0, s0, .LS_HERE-.LA_START\n" \
  "s_subb_u32 s1, s1, 0\n" \
  "s_mov_b32 s3, .LS_END-.LA_START\n" \
  "s_lshl_b32 s27, s25, 2\n" \
  "s_add_u32 s27, s27, s26\n" \
  "s_mul_i32 s27, s27, 2560\n" \
  "v_mbcnt_lo_u32_b32 v31, -1, 0\n" \
  "v_lshl_add_u32 v31, v31, 7, s27\n" \
  "v_min_u32 v31, s3, v31\n" \
  "s_mov_b64 exec, 0xfffff\n" \
  "global_load_dword v30, v31, s[0:1]\n" \
  "s_mov_b64 exec, -1\n" \
  "v_mov_b32 v0, v2\n" \
  "v_and_b32 v1, 15, v0\n" \
  "v_lshrrev_b32 v2, 4, v0\n" \
  "s_lshl_b32 s64, s24, 10\n" \
  "s_lshl_b32 s54, s25, 8\n" \
  "s_add_u32 s64, s64, s54\n" \
  "v_lshlrev_b32 v24, 2, v2\n" \
  "v_xor_b32 v24, v24, v1\n" \
  "s_and_b32 s54, s26, 2\n" \
  "v_xor_b32 v25, s54, v24\n" \
  "v_lshlrev_b32 v26, 13, v2\n" \
  "v_lshl_add_u32 v26, v25, 4, v26\n" \
  "v_lshlrev_b32 v28, 4, v0\n" \
  "s_mul_i32 s54, s26, 896\n" \
  "s_waitcnt lgkmcnt(0)\n" \
  "s_load_dwordx2 s[96:97], s[22:23], 0x0\n" \
  "s_mov_b64 s[40:41], s[10:11]\n" \
  "s_mov_b32 s42, 3670016\n" \
  "s_mov_b32 s43, 0x20000\n" \
  "s_mov_b64 s[44:45], s[4:5]\n" \
  "s_mov_b32 s46, 3584\n" \
  "s_mov_b32 s47, 0x20000\n" \
  "buffer_load_dwordx4 v[32:35], v28, s[44:47], s54 offen\n" \
  "s_mov_b64 s[36:37], s[22:23]\n" \
  "s_mov_b32 s38, 1773568\n" \
  "s_mov_b32 s39, 0x20000\n" \
  "s_lshl_b32 s62, s24, 2\n" \
  "s_add_u32 s62, s62, s25\n" \
  "s_min_u32 s63, s62, 27\n" \
  "v_mov_b32 v40, -1\n" \
  "v_mov_b32 v41, -1\n" \
  "v_mov_b32 v42, -1\n" \
  "v_mov_b32 v43, -1\n" \
  "v_lshlrev_b32 v3, 4, v1\n" \
  "v_mul_u32_u24 v4, 28672, v2\n" \
  "v_add_u32 v4, v4, v3\n" \
  "s_lshl_b32 s54, s63, 10\n" \
  "s_lshl_b32 s55, s26, 8\n" \
  "s_add_u32 s54, s54, s55\n" \
  "s_add_u32 s54, s54, 1368064\n" \
  "s_cmp_lt_u32 s62, 28\n" \
  "s_cselect_b32 s54, s54, 1597440\n" \
  "s_cselect_b32 s55, 114688, 0\n" \
  "s_cselect_b64 vcc, -1, 0\n" \
  "v_cndmask_b32 v4, v3, v4, vcc\n" \
  "buffer_store_dwordx4 v[40:43], v4, s[36:39], s54 offen sc1\n" \
  "s_add_u32 s54, s54, s55\n" \
  "buffer_store_dwordx4 v[40:43], v4, s[36:39], s54 offen sc1\n" \
  "s_lshl_b32 s54, s26, 14\n" \
  "s_add_u32 s54, s54, s64\n" \
  "s_add_u32 s54, s54, 1294336\n" \
  "v_lshl_add_u32 v4, v2, 13, v3\n" \
  "v_add_u32 v4, s54, v4\n" \
  "s_lshl_b32 s54, s64, 1\n" \
  "s_lshl_b32 s55, s26, 7\n" \
  "s_add_u32 s54, s54, s55\n" \
  "s_add_u32 s60, s54, 1626112\n" \
  "v_bfe_u32 v5, v0, 2, 3\n" \
  "v_lshlrev_b32 v5, 14, v5\n" \
  "v_and_b32 v6, 0x30, v3\n" \
  "v_add3_u32 v5, v5, v6, s60\n" \
  "v_cmp_gt_u32 vcc, 32, v0\n" \
  "s_nop 1\n" \
  "v_cndmask_b32 v4, v5, v4, vcc\n" \
  "buffer_store_dwordx4 v[40:43], v4, s[36:39], 0 offen sc1\n" \
  "s_lshl_b32 s56, s26, 15\n" \
  "s_add_u32 s56, s56, s64\n" \
  "s_lshl_b32 m0, s26, 10\n" \
  "s_call_b64 s[88:89], .LS_LDQ\n" \
  "s_mov_b64 s[48:49], s[20:21]\n" \
  "s_mov_b32 s50, 3584\n" \
  "s_mov_b32 s51, 0x20000\n" \
  "s_mov_b32 s72, 0x0\n" \
  "s_mov_b32 s73, 0xffffffff\n" \
  "s_mov_b32 s74, 0xf000f\n" \
  "s_mov_b32 s75, 0x3000f\n" \
  "s_mov_b32 s76, 0x3fff3fff\n" \
  "s_mov_b32 s77, 0x3fff3fff\n" \
  "s_mov_b32 s78, 0xaaaaaaaa\n" \
  "s_mov_b32 s79, 0xaaaaaaaa\n" \
  "s_mov_b32 s80, 0x30003\n" \
  "s_mov_b32 s81, 0x30003\n" \
  "s_mov_b32 s82, 0x30003\n" \
  "s_mov_b32 s83, 0x0\n" \
  "s_mov_b32 s86, 0x3fff3fff\n" \
  "s_mov_b32 s87, 0x0\n" \
  "s_mov_b32 s61, s26\n" \
  "v_bfe_u32 v3, v0, 2, 2\n" \
  "v_lshl_add_u32 v29, v2, 3, v3\n" \
  "v_lshlrev_b32 v29, 8, v29\n" \
  "v_and_b32 v4, 1, v0\n" \
  "v_lshl_add_u32 v29, v4, 3, v29\n" \
  "v_and_b32 v5, 1, v2\n" \
  "v_lshlrev_b32 v5, 1, v5\n" \
  "v_lshl_add_u32 v6, v3, 2, v5\n" \
  "v_bfe_u32 v7, v0, 1, 1\n" \
  "v_or_b32 v6, v6, v7\n" \
  "v_lshl_add_u32 v8, v6, 4, v29\n" \
  "v_lshl_add_u32 v9, v2, 3, v3\n" \
  "v_lshlrev_b32 v9, 7, v9\n" \
  "v_lshl_add_u32 v9, v4, 3, v9\n" \
  "v_lshl_add_u32 v10, v3, 1, v7\n" \
  "s_lshl_b32 s54, s26, 1\n" \
  "v_xor_b32 v10, s54, v10\n" \
  "v_lshl_add_u32 v9, v10, 4, v9\n" \
  "s_add_u32 m0, m0, 8192\n" \
  "s_call_b64 s[88:89], .LS_LDQ\n" \
  "v_xor_b32 v25, 2, v24\n" \
  "v_min_u32 v24, 13, v24\n" \
  "v_min_u32 v25, 13, v25\n" \
  "v_mul_u32_u24 v18, 896, v2\n" \
  "v_lshl_add_u32 v19, v25, 4, v18\n" \
  "v_lshl_add_u32 v18, v24, 4, v18\n" \
  "v_lshrrev_b32 v22, 3, v0\n" \
  "v_and_b32 v25, 3, v22\n" \
  "v_lshlrev_b32 v25, 1, v25\n" \
  "v_and_b32 v24, 7, v0\n" \
  "v_xor_b32 v25, v25, v24\n" \
  "v_mul_u32_u24 v22, 3584, v22\n" \
  "v_lshl_add_u32 v22, v25, 4, v22\n" \
  "v_mov_b32 v40, 0x3f803f80\n" \
  "v_mov_b32 v41, v40\n" \
  "v_mov_b32 v42, v40\n" \
  "v_mov_b32 v43, v40\n" \
  "v_mov_b32 v44, 0\n" \
  "v_add_u32 v11, 134720, v28\n" \
  "ds_write_b128 v11, v[40:43]\n" \
  "v_lshlrev_b32 v11, 2, v0\n" \
  "v_add_u32 v11, 136448, v11\n" \
  "ds_write_b32 v11, v44\n" \
  "s_mul_i32 s54, s26, 912\n" \
  "s_add_u32 s54, s54, 131072\n" \
  "v_add_u32 v11, s54, v28\n" \
  "s_lshl_b32 s54, s26, 11\n" \
  "s_add_u32 s54, s54, 143360\n" \
  "v_lshl_add_u32 v12, v0, 5, s54\n" \
  "s_lshl_b32 s54, s26, 6\n" \
  "v_xor_b32 v13, s54, v8\n" \
  "v_xor_b32 v14, 32, v13\n" \
  "s_waitcnt vmcnt(14)\n" \
  "v_lshlrev_b32 v48, 16, v32\n" \
  "v_and_b32 v49, 0xffff0000, v32\n" \
  "v_lshlrev_b32 v50, 16, v33\n" \
  "v_and_b32 v51, 0xffff0000, v33\n" \
  "v_lshlrev_b32 v52, 16, v34\n" \
  "v_and_b32 v53, 0xffff0000, v34\n" \
  "v_lshlrev_b32 v54, 16, v35\n" \
  "v_and_b32 v55, 0xffff0000, v35\n" \
  "s_mov_b32 exec_hi, 0x00ffffff\n" \
  "ds_write_b128 v11, v[32:35]\n" \
  "ds_write_b128 v12, v[48:51]\n" \
  "ds_write_b128 v12, v[52:55] offset:16\n" \
  "s_mov_b64 exec, -1\n" \
  "s_waitcnt lgkmcnt(0)\n" \
  "s_barrier\n" \
  "s_add_u32 s96, s96, 1\n" \
  "s_addc_u32 s97, s97, 0\n" \
  "v_mov_b32 v40, s96\n" \
  "v_mov_b32 v41, s97\n" \
  "s_lshl_b32 s54, s2, 3\n" \
  "s_mov_b64 exec, 1\n" \
  "buffer_store_dwordx2 v[40:41], off, s[36:39], s54 offset:256 sc1\n" \
  "s_mov_b64 exec, -1\n" \
  "s_or_b32 s54, s25, s26\n" \
  "s_cmp_lg_u32 s54, 0\n" \
  "s_cbranch_scc1 .LS_NOGT\n" \
  "v_mov_b32 v42, s30\n" \
  "v_mov_b32 v43, s30\n" \
  "s_lshl_b32 s54, s24, 4\n" \
  "s_mov_b64 exec, 1\n" \
  "buffer_store_dwordx4 v[40:43], off, s[36:39], s54 offset:128 sc1\n" \
  "s_mov_b64 exec, -1\n" \
  ".LS_NOGT:\n" \
  "ds_read_b64_tr_b16 a[0:1], v13 offset:0\n" \
  "ds_read_b64_tr_b16 a[2:3], v13 offset:1024\n" \
  "ds_read_b64_tr_b16 a[56:57], v14 offset:0\n" \
  "ds_read_b64_tr_b16 a[58:59], v14 offset:1024\n" \
  "ds_read_b64_tr_b16 a[4:5], v13 offset:8192\n" \
  "ds_read_b64_tr_b16 a[6:7], v13 offset:9216\n" \
  "ds_read_b64_tr_b16 a[60:61], v14 offset:8192\n" \
  "ds_read_b64_tr_b16 a[62:63], v14 offset:9216\n" \
  "ds_read_b64_tr_b16 a[8:9], v13 offset:16384\n" \
  "ds_read_b64_tr_b16 a[10:11], v13 offset:17408\n" \
  "ds_read_b64_tr_b16 a[64:65], v14 offset:16384\n" \
  "ds_read_b64_tr_b16 a[66:67], v14 offset:17408\n" \
  "ds_read_b64_tr_b16 a[12:13], v13 offset:24576\n" \
  "ds_read_b64_tr_b16 a[14:15], v13 offset:25600\n" \
  "ds_read_b64_tr_b16 a[68:69], v14 offset:24576\n" \
  "ds_read_b64_tr_b16 a[70:71], v14 offset:25600\n" \
  "ds_read_b64_tr_b16 a[16:17], v13 offset:32768\n" \
  "ds_read_b64_tr_b16 a[18:19], v13 offset:33792\n" \
  "ds_read_b64_tr_b16 a[72:73], v14 offset:32768\n" \
  "ds_read_b64_tr_b16 a[74:75], v14 offset:33792\n" \
  "ds_read_b64_tr_b16 a[20:21], v13 offset:40960\n" \
  "ds_read_b64_tr_b16 a[22:23], v13 offset:41984\n" \
  "ds_read_b64_tr_b16 a[76:77], v14 offset:40960\n" \
  "ds_read_b64_tr_b16 a[78:79], v14 offset:41984\n" \
  "ds_read_b64_tr_b16 a[24:25], v13 offset:49152\n" \
  "ds_read_b64_tr_b16 a[26:27], v13 offset:50176\n" \
  "ds_read_b64_tr_b16 a[80:81], v14 offset:49152\n" \
  "ds_read_b64_tr_b16 a[82:83], v14 offset:50176\n" \
  "v_add_u32 v13, 0x10000, v13\n" \
  "v_add_u32 v14, 0x10000, v14\n" \
  "s_mov_b64 s[40:41], s[12:13]\n" \
  "s_mov_b32 s42, 3670016\n" \
  "s_mov_b32 s43, 0x20000\n" \
  "s_lshl_b32 s54, s24, 2\n" \
  "s_add_u32 s54, s54, s26\n" \
  "s_mul_i32 s56, s54, 114688\n" \
  "s_mul_i32 s54, s25, 224\n" \
  "s_add_u32 s56, s56, s54\n" \
  "s_lshl_b32 s57, s26, 14\n" \
  "s_mov_b32 m0, s57\n" \
  "v_bfe_u32 v15, v0, 1, 2\n" \
  "v_cmp_gt_u32 vcc, 8, v1\n" \
  "s_nop 1\n" \
  "v_cndmask_b32 v16, 4, v15, vcc\n" \
  "v_mul_u32_u24 v16, 912, v16\n" \
  "v_lshl_add_u32 v16, v2, 4, v16\n" \
  "v_add_u32 v16, 131072, v16\n" \
  "v_lshrrev_b32 v17, 3, v0\n" \
  "v_lshl_add_u32 v17, v17, 1, v4\n" \
  "v_mul_u32_u24 v21, 912, v15\n" \
  "v_lshl_add_u32 v21, v17, 6, v21\n" \
  "v_add_u32 v21, 131072, v21\n" \
  "v_cmp_gt_u32 vcc, 14, v17\n" \
  "v_mov_b32 v17, 136448\n" \
  "s_nop 0\n" \
  "v_cndmask_b32 v17, v17, v21, vcc\n" \
  "v_lshl_add_u32 v20, v2, 3, s60\n" \
  "v_lshl_add_u32 v20, v4, 5, v20\n" \
  "v_lshl_add_u32 v21, v15, 14, v20\n" \
  "s_waitcnt lgkmcnt(0)\n" \
  "s_barrier\n" \
  "v_mbcnt_lo_u32_b32 v31, -1, 0\n" \
  "v_lshl_add_u32 v31, v31, 7, s27\n" \
  "v_min_u32 v31, s3, v31\n" \
  "s_mov_b64 exec, 0xfffff\n" \
  "global_load_dword v30, v31, s[0:1]\n" \
  "s_mov_b64 exec, -1\n" \
  "s_call_b64 s[88:89], .LS_LDP\n" \
  "s_waitcnt vmcnt(17)\n" \
  "s_barrier\n" \
  "ds_read_b64_tr_b16 a[28:29], v13 offset:0\n" \
  "ds_read_b64_tr_b16 a[30:31], v13 offset:1024\n" \
  "ds_read_b64_tr_b16 a[84:85], v14 offset:0\n" \
  "ds_read_b64_tr_b16 a[86:87], v14 offset:1024\n" \
  "ds_read_b64_tr_b16 a[32:33], v13 offset:8192\n" \
  "ds_read_b64_tr_b16 a[34:35], v13 offset:9216\n" \
  "ds_read_b64_tr_b16 a[88:89], v14 offset:8192\n" \
  "ds_read_b64_tr_b16 a[90:91], v14 offset:9216\n" \
  "ds_read_b64_tr_b16 a[36:37], v13 offset:16384\n" \
  "ds_read_b64_tr_b16 a[38:39], v13 offset:17408\n" \
  "ds_read_b64_tr_b16 a[92:93], v14 offset:16384\n" \
  "ds_read_b64_tr_b16 a[94:95], v14 offset:17408\n" \
  "ds_read_b64_tr_b16 a[40:41], v13 offset:24576\n" \
  "ds_read_b64_tr_b16 a[42:43], v13 offset:25600\n" \
  "ds_read_b64_tr_b16 a[96:97], v14 offset:24576\n" \
  "ds_read_b64_tr_b16 a[98:99], v14 offset:25600\n" \
  "ds_read_b64_tr_b16 a[44:45], v13 offset:32768\n" \
  "ds_read_b64_tr_b16 a[46:47], v13 offset:33792\n" \
  "ds_read_b64_tr_b16 a[100:101], v14 offset:32768\n" \
  "ds_read_b64_tr_b16 a[102:103], v14 offset:33792\n" \
  "ds_read_b64_tr_b16 a[48:49], v13 offset:40960\n" \
  "ds_read_b64_tr_b16 a[50:51], v13 offset:41984\n" \
  "ds_read_b64_tr_b16 a[104:105], v14 offset:40960\n" \
  "ds_read_b64_tr_b16 a[106:107], v14 offset:41984\n" \
  "ds_read_b64_tr_b16 a[52:53], v13 offset:49152\n" \
  "ds_read_b64_tr_b16 a[54:55], v13 offset:50176\n" \
  "ds_read_b64_tr_b16 a[108:109], v14 offset:49152\n" \
  "ds_read_b64_tr_b16 a[110:111], v14 offset:50176\n" \
  "s_add_u32 m0, m0, 49152\n" \
  "s_waitcnt lgkmcnt(0)\n" \
  "s_barrier\n" \
  "s_call_b64 s[88:89], .LS_LDP\n" \
  "ds_read_b128 v[32:35], v16 offset:0\n" \
  "ds_read_b128 v[36:39], v16 offset:64\n" \
  "ds_read_b128 v[40:43], v16 offset:128\n" \
  "ds_read_b128 v[44:47], v16 offset:192\n" \
  "ds_read_b128 v[48:51], v16 offset:256\n" \
  "ds_read_b128 v[52:55], v16 offset:320\n" \
  "ds_read_b128 v[56:59], v16 offset:384\n" \
  "ds_read_b128 v[60:63], v16 offset:448\n" \
  "ds_read_b128 v[64:67], v16 offset:512\n" \
  "ds_read_b128 v[68:71], v16 offset:576\n" \
  "ds_read_b128 v[72:75], v16 offset:640\n" \
  "ds_read_b128 v[76:79], v16 offset:704\n" \
  "ds_read_b128 v[80:83], v16 offset:768\n" \
  "ds_read_b128 v[84:87], v16 offset:832\n" \
  "s_waitcnt lgkmcnt(0)\n" \
  "ds_read_b128 v[104:107], v17 offset:0\n" \
  "ds_read_b128 v[108:111], v17 offset:16\n" \
  "ds_read_b128 v[112:115], v17 offset:32\n" \
  "ds_read_b128 v[116:119], v17 offset:48\n" \
  "v_mfma_f32_16x16x32_bf16 v[88:91], a[0:3], v[32:35], 0\n" \
  "v_mfma_f32_16x16x32_bf16 v[92:95], a[56:59], v[32:35], 0\n" \
  "v_mfma_f32_16x16x32_bf16 v[88:91], a[4:7], v[36:39], v[88:91]\n" \
  "v_mfma_f32_16x16x32_bf16 v[92:95], a[60:63], v[36:39], v[92:95]\n" \
  "s_waitcnt lgkmcnt(0)\n" \
  "v_mfma_f32_16x16x32_bf16 v[88:91], a[8:11], v[40:43], v[88:91]\n" \
  "v_mov_b32 v24, 0\n" \
  "v_mov_b32 v26, 0\n" \
  "v_dot2c_f32_bf16 v24, 0x3f803f80, v104\n" \
  "v_mfma_f32_16x16x32_bf16 v[92:95], a[64:67], v[40:43], v[92:95]\n" \
  "v_dot2c_f32_bf16 v26, v104, v104\n" \
  "v_dot2c_f32_bf16 v24, 0x3f803f80, v105\n" \
  "v_dot2c_f32_bf16 v26, v105, v105\n" \
  "v_mfma_f32_16x16x32_bf16 v[88:91], a[12:15], v[44:47], v[88:91]\n" \
  "v_dot2c_f32_bf16 v24, 0x3f803f80, v106\n" \
  "v_dot2c_f32_bf16 v26, v106, v106\n" \
  "v_dot2c_f32_bf16 v24, 0x3f803f80, v107\n" \
  "v_mfma_f32_16x16x32_bf16 v[92:95], a[68:71], v[44:47], v[92:95]\n" \
  "v_dot2c_f32_bf16 v26, v107, v107\n" \
  "v_dot2c_f32_bf16 v24, 0x3f803f80, v108\n" \
  "v_dot2c_f32_bf16 v26, v108, v108\n" \
  "v_mfma_f32_16x16x32_bf16 v[88:91], a[16:19], v[48:51], v[88:91]\n" \
  "v_dot2c_f32_bf16 v24, 0x3f803f80, v109\n" \
  "v_dot2c_f32_bf16 v26, v109, v109\n" \
  "v_dot2c_f32_bf16 v24, 0x3f803f80, v110\n" \
  "v_mfma_f32_16x16x32_bf16 v[92:95], a[72:75], v[48:51], v[92:95]\n" \
  "v_dot2c_f32_bf16 v26, v110, v110\n" \
  "v_dot2c_f32_bf16 v24, 0x3f803f80, v111\n" \
  "v_dot2c_f32_bf16 v26, v111, v111\n" \
  "v_mfma_f32_16x16x32_bf16 v[88:91], a[20:23], v[52:55], v[88:91]\n" \
  "v_dot2c_f32_bf16 v24, 0x3f803f80, v112\n" \
  "v_dot2c_f32_bf16 v26, v112, v112\n" \
  "v_dot2c_f32_bf16 v24, 0x3f803f80, v113\n" \
  "v_mfma_f32_16x16x32_bf16 v[92:95], a[76:79], v[52:55], v[92:95]\n" \
  "v_dot2c_f32_bf16 v26, v113, v113\n" \
  "v_dot2c_f32_bf16 v24, 0x3f803f80, v114\n" \
  "v_dot2c_f32_bf16 v26, v114, v114\n" \
  "v_mfma_f32_16x16x32_bf16 v[88:91], a[24:27], v[56:59], v[88:91]\n" \
  "v_dot2c_f32_bf16 v24, 0x3f803f80, v115\n" \
  "v_dot2c_f32_bf16 v26, v115, v115\n" \
  "v_dot2c_f32_bf16 v24, 0x3f803f80, v116\n" \
  "v_mfma_f32_16x16x32_bf16 v[92:95], a[80:83], v[56:59], v[92:95]\n" \
  "v_dot2c_f32_bf16 v26, v116, v116\n" \
  "v_dot2c_f32_bf16 v24, 0x3f803f80, v117\n" \
  "v_dot2c_f32_bf16 v26, v117, v117\n" \
  "v_mfma_f32_16x16x32_bf16 v[88:91], a[28:31], v[60:63], v[88:91]\n" \
  "v_dot2c_f32_bf16 v24, 0x3f803f80, v118\n" \
  "v_dot2c_f32_bf16 v26, v118, v118\n" \
  "v_dot2c_f32_bf16 v24, 0x3f803f80, v119\n" \
  "v_mfma_f32_16x16x32_bf16 v[92:95], a[84:87], v[60:63], v[92:95]\n" \
  "v_dot2c_f32_bf16 v26, v119, v119\n" \
  "s_nop 0\n" \
  "v_add_f32_dpp v24, v24, v24 quad_perm:[1,0,3,2] row_mask:0xf bank_mask:0xf\n" \
  "s_nop 0\n" \
  "v_add_f32_dpp v26, v26, v26 quad_perm:[1,0,3,2] row_mask:0xf bank_mask:0xf\n" \
  "v_mfma_f32_16x16x32_bf16 v[88:91], a[32:35], v[64:67], v[88:91]\n" \
  "v_add_f32_dpp v24, v24, v24 row_ror:8 row_mask:0xf bank_mask:0xf\n" \
  "v_add_f32_dpp v26, v26, v26 row_ror:8 row_mask:0xf bank_mask:0xf\n" \
  "v_mov_b32 v28, v24\n" \
  "v_mfma_f32_16x16x32_bf16 v[92:95], a[88:91], v[64:67], v[92:95]\n" \
  "v_mov_b32 v29, v26\n" \
  "v_permlane16_swap_b32 v24, v28\n" \
  "s_nop 0\n" \
  "v_permlane16_swap_b32 v26, v29\n" \
  "v_mfma_f32_16x16x32_bf16 v[88:91], a[36:39], v[68:71], v[88:91]\n" \
  "v_add_f32 v24, v24, v28\n" \
  "v_add_f32 v26, v26, v29\n" \
  "v_mov_b32 v28, v24\n" \
  "v_mfma_f32_16x16x32_bf16 v[92:95], a[92:95], v[68:71], v[92:95]\n" \
  "v_mov_b32 v29, v26\n" \
  "v_permlane32_swap_b32 v24, v28\n" \
  "s_nop 0\n" \
  "v_permlane32_swap_b32 v26, v29\n" \
  "v_mfma_f32_16x16x32_bf16 v[88:91], a[40:43], v[72:75], v[88:91]\n" \
  "v_add_f32 v24, v24, v28\n" \
  "v_add_f32 v26, v26, v29\n" \
  "v_mul_f32 v24, 0x3b124925, v24\n" \
  "v_mfma_f32_16x16x32_bf16 v[92:95], a[96:99], v[72:75], v[92:95]\n" \
  "v_mul_f32 v26, 0x3b124925, v26\n" \
  "v_fma_f32 v26, -v24, v24, v26\n" \
  "v_max_f32 v26, 0, v26\n" \
  "v_mfma_f32_16x16x32_bf16 v[88:91], a[44:47], v[76:79], v[88:91]\n" \
  "v_add_f32 v26, 0x3727c5ac, v26\n" \
  "v_rsq_f32 v26, v26\n" \
  "v_xor_b32 v24, 0x80000000, v24\n" \
  "v_mfma_f32_16x16x32_bf16 v[92:95], a[100:103], v[76:79], v[92:95]\n" \
  "v_mul_f32 v26, 0x3db8aa3b, v26\n" \
  "v_mfma_f32_16x16x32_bf16 v[88:91], a[48:51], v[80:83], v[88:91]\n" \
  "v_mfma_f32_16x16x32_bf16 v[92:95], a[104:107], v[80:83], v[92:95]\n" \
  "v_mfma_f32_16x16x32_bf16 v[88:91], a[52:55], v[84:87], v[88:91]\n" \
  "v_mfma_f32_16x16x32_bf16 v[92:95], a[108:111], v[84:87], v[92:95]\n" \
  "s_mov_b32 s84, 0xff00ff\n" \
  "s_mov_b32 s85, 0xff00ff\n" \
  "s_nop 5\n" \
  "v_cndmask_b32 v88, v88, v92, s[78:79]\n" \
  "v_cndmask_b32 v89, v89, v93, s[78:79]\n" \
  "v_cndmask_b32 v90, v90, v94, s[78:79]\n" \
  "v_cndmask_b32 v91, v91, v95, s[78:79]\n" \
  "v_mov_b32_dpp v104, v88 row_shl:8 row_mask:0xf bank_mask:0xf\n" \
  "v_mov_b32_dpp v105, v89 row_shl:8 row_mask:0xf bank_mask:0xf\n" \
  "v_mov_b32_dpp v106, v90 row_shl:8 row_mask:0xf bank_mask:0xf\n" \
  "v_mov_b32_dpp v107, v91 row_shl:8 row_mask:0xf bank_mask:0xf\n" \
  "v_fmac_f32 v88, v24, v104\n" \
  "v_fmac_f32 v89, v24, v105\n" \
  "v_fmac_f32 v90, v24, v106\n" \
  "v_fmac_f32 v91, v24, v107\n" \
  "v_mul_f32 v88, v26, v88\n" \
  "v_mul_f32 v89, v26, v89\n" \
  "v_mul_f32 v90, v26, v90\n" \
  "v_mul_f32 v91, v26, v91\n" \
  "v_cvt_pk_bf16_f32 v88, v88, v89\n" \
  "v_cvt_pk_bf16_f32 v89, v90, v91\n" \
  "v_min_u32 v88, -2, v88\n" \
  "v_min_u32 v89, -2, v89\n" \
  "s_mov_b64 exec, s[84:85]\n" \
  "buffer_store_dwordx2 v[88:89], v21, s[36:39], 0 offen sc1\n" \
  "s_mov_b64 exec, -1\n" \
  "v_add_u32 v8, s57, v8\n" \
  "s_mov_b64 s[40:41], s[14:15]\n" \
  "s_mov_b32 s42, 1605632\n" \
  "s_mov_b32 s43, 0x20000\n" \
  "s_mov_b64 s[44:45], s[16:17]\n" \
  "s_mov_b32 s46, 1605632\n" \
  "s_mov_b32 s47, 0x20000\n" \
  "s_cmp_lt_u32 s62, 28\n" \
  "s_cselect_b32 s42, s42, 0\n" \
  "s_cselect_b32 s46, s46, 0\n" \
  "s_mul_i32 s56, s26, 200704\n" \
  "s_lshl_b32 s54, s63, 7\n" \
  "s_add_u32 s56, s56, s54\n" \
  "s_mul_i32 m0, s26, 7168\n" \
  "s_waitcnt vmcnt(17)\n" \
  "v_xor_b32 v13, 0, v8\n" \
  "ds_read_b64_tr_b16 a[112:113], v13 offset:0\n" \
  "ds_read_b64_tr_b16 a[114:115], v13 offset:1024\n" \
  "ds_read_b64_tr_b16 a[116:117], v13 offset:8192\n" \
  "ds_read_b64_tr_b16 a[118:119], v13 offset:9216\n" \
  "v_xor_b32 v13, 32, v8\n" \
  "ds_read_b64_tr_b16 a[128:129], v13 offset:0\n" \
  "ds_read_b64_tr_b16 a[130:131], v13 offset:1024\n" \
  "ds_read_b64_tr_b16 a[132:133], v13 offset:8192\n" \
  "ds_read_b64_tr_b16 a[134:135], v13 offset:9216\n" \
  "v_xor_b32 v13, 64, v8\n" \
  "ds_read_b64_tr_b16 a[144:145], v13 offset:0\n" \
  "ds_read_b64_tr_b16 a[146:147], v13 offset:1024\n" \
  "ds_read_b64_tr_b16 a[148:149], v13 offset:8192\n" \
  "ds_read_b64_tr_b16 a[150:151], v13 offset:9216\n" \
  "v_xor_b32 v13, 96, v8\n" \
  "ds_read_b64_tr_b16 a[160:161], v13 offset:0\n" \
  "ds_read_b64_tr_b16 a[162:163], v13 offset:1024\n" \
  "ds_read_b64_tr_b16 a[164:165], v13 offset:8192\n" \
  "ds_read_b64_tr_b16 a[166:167], v13 offset:9216\n" \
  "v_xor_b32 v13, 128, v8\n" \
  "ds_read_b64_tr_b16 a[176:177], v13 offset:0\n" \
  "ds_read_b64_tr_b16 a[178:179], v13 offset:1024\n" \
  "ds_read_b64_tr_b16 a[180:181], v13 offset:8192\n" \
  "ds_read_b64_tr_b16 a[182:183], v13 offset:9216\n" \
  "v_xor_b32 v13, 160, v8\n" \
  "ds_read_b64_tr_b16 a[192:193], v13 offset:0\n" \
  "ds_read_b64_tr_b16 a[194:195], v13 offset:1024\n" \
  "ds_read_b64_tr_b16 a[196:197], v13 offset:8192\n" \
  "ds_read_b64_tr_b16 a[198:199], v13 offset:9216\n" \
  "v_xor_b32 v13, 192, v8\n" \
  "ds_read_b64_tr_b16 a[208:209], v13 offset:0\n" \
  "ds_read_b64_tr_b16 a[210:211], v13 offset:1024\n" \
  "ds_read_b64_tr_b16 a[212:213], v13 offset:8192\n" \
  "ds_read_b64_tr_b16 a[214:215], v13 offset:9216\n" \
  "s_waitcnt lgkmcnt(0)\n" \
  "s_barrier\n" \
  "v_mbcnt_lo_u32_b32 v31, -1, 0\n" \
  "v_lshl_add_u32 v31, v31, 7, s27\n" \
  "v_min_u32 v31, s3, v31\n" \
  "s_mov_b64 exec, 0xfffff\n" \
  "global_load_dword v30, v31, s[0:1]\n" \
  "s_mov_b64 exec, -1\n" \
  "s_call_b64 s[88:89], .LS_LDG\n" \
  "s_waitcnt vmcnt(15)\n" \
  "v_add_u32 v14, 0x10000, v8\n" \
  "v_xor_b32 v13, 0, v14\n" \
  "ds_read_b64_tr_b16 a[120:121], v13 offset:0\n" \
  "ds_read_b64_tr_b16 a[122:123], v13 offset:1024\n" \
  "ds_read_b64_tr_b16 a[124:125], v13 offset:8192\n" \
  "ds_read_b64_tr_b16 a[126:127], v13 offset:9216\n" \
  "v_xor_b32 v13, 32, v14\n" \
  "ds_read_b64_tr_b16 a[136:137], v13 offset:0\n" \
  "ds_read_b64_tr_b16 a[138:139], v13 offset:1024\n" \
  "ds_read_b64_tr_b16 a[140:141], v13 offset:8192\n" \
  "ds_read_b64_tr_b16 a[142:143], v13 offset:9216\n" \
  "v_xor_b32 v13, 64, v14\n" \
  "ds_read_b64_tr_b16 a[152:153], v13 offset:0\n" \
  "ds_read_b64_tr_b16 a[154:155], v13 offset:1024\n" \
  "ds_read_b64_tr_b16 a[156:157], v13 offset:8192\n" \
  "ds_read_b64_tr_b16 a[158:159], v13 offset:9216\n" \
  "v_xor_b32 v13, 96, v14\n" \
  "ds_read_b64_tr_b16 a[168:169], v13 offset:0\n" \
  "ds_read_b64_tr_b16 a[170:171], v13 offset:1024\n" \
  "ds_read_b64_tr_b16 a[172:173], v13 offset:8192\n" \
  "ds_read_b64_tr_b16 a[174:175], v13 offset:9216\n" \
  "v_xor_b32 v13, 128, v14\n" \
  "ds_read_b64_tr_b16 a[184:185], v13 offset:0\n" \
  "ds_read_b64_tr_b16 a[186:187], v13 offset:1024\n" \
  "ds_read_b64_tr_b16 a[188:189], v13 offset:8192\n" \
  "ds_read_b64_tr_b16 a[190:191], v13 offset:9216\n" \
  "v_xor_b32 v13, 160, v14\n" \
  "ds_read_b64_tr_b16 a[200:201], v13 offset:0\n" \
  "ds_read_b64_tr_b16 a[202:203], v13 offset:1024\n" \
  "ds_read_b64_tr_b16 a[204:205], v13 offset:8192\n" \
  "ds_read_b64_tr_b16 a[206:207], v13 offset:9216\n" \
  "v_xor_b32 v13, 192, v14\n" \
  "ds_read_b64_tr_b16 a[216:217], v13 offset:0\n" \
  "ds_read_b64_tr_b16 a[218:219], v13 offset:1024\n" \
  "ds_read_b64_tr_b16 a[220:221], v13 offset:8192\n" \
  "ds_read_b64_tr_b16 a[222:223], v13 offset:9216\n" \
  "s_add_u32 m0, m0, 58368\n" \
  "s_add_u32 s56, s56, 602112\n" \
  "s_waitcnt lgkmcnt(0)\n" \
  "s_barrier\n" \
  "s_call_b64 s[88:89], .LS_LDG\n" \
  "s_mov_b64 s[40:41], s[18:19]\n" \
  "s_mul_i32 s56, s63, 57344\n" \
  "s_mul_i32 s54, s26, 224\n" \
  "s_add_u32 s56, s56, s54\n" \
  "s_mov_b32 m0, s57\n" \
  "s_waitcnt vmcnt(14)\n" \
  "s_barrier\n" \
  "ds_read_b64_tr_b16 v[120:121], v9 offset:0\n" \
  "ds_read_b64_tr_b16 v[122:123], v9 offset:512\n" \
  "ds_read_b64_tr_b16 v[176:177], v9 offset:28672\n" \
  "ds_read_b64_tr_b16 v[178:179], v9 offset:29184\n" \
  "ds_read_b64_tr_b16 v[124:125], v9 offset:4096\n" \
  "ds_read_b64_tr_b16 v[126:127], v9 offset:4608\n" \
  "ds_read_b64_tr_b16 v[180:181], v9 offset:32768\n" \
  "ds_read_b64_tr_b16 v[182:183], v9 offset:33280\n" \
  "ds_read_b64_tr_b16 v[128:129], v9 offset:8192\n" \
  "ds_read_b64_tr_b16 v[130:131], v9 offset:8704\n" \
  "ds_read_b64_tr_b16 v[184:185], v9 offset:36864\n" \
  "ds_read_b64_tr_b16 v[186:187], v9 offset:37376\n" \
  "ds_read_b64_tr_b16 v[132:133], v9 offset:12288\n" \
  "ds_read_b64_tr_b16 v[134:135], v9 offset:12800\n" \
  "ds_read_b64_tr_b16 v[188:189], v9 offset:40960\n" \
  "ds_read_b64_tr_b16 v[190:191], v9 offset:41472\n" \
  "ds_read_b64_tr_b16 v[136:137], v9 offset:16384\n" \
  "ds_read_b64_tr_b16 v[138:139], v9 offset:16896\n" \
  "ds_read_b64_tr_b16 v[192:193], v9 offset:45056\n" \
  "ds_read_b64_tr_b16 v[194:195], v9 offset:45568\n" \
  "ds_read_b64_tr_b16 v[140:141], v9 offset:20480\n" \
  "ds_read_b64_tr_b16 v[142:143], v9 offset:20992\n" \
  "ds_read_b64_tr_b16 v[196:197], v9 offset:49152\n" \
  "ds_read_b64_tr_b16 v[198:199], v9 offset:49664\n" \
  "ds_read_b64_tr_b16 v[144:145], v9 offset:24576\n" \
  "ds_read_b64_tr_b16 v[146:147], v9 offset:25088\n" \
  "ds_read_b64_tr_b16 v[200:201], v9 offset:53248\n" \
  "ds_read_b64_tr_b16 v[202:203], v9 offset:53760\n" \
  "v_add_u32 v9, 0x10000, v9\n" \
  "s_waitcnt lgkmcnt(0)\n" \
  "s_barrier\n" \
  "s_call_b64 s[88:89], .LS_LDP\n" \
  "s_waitcnt vmcnt(16)\n" \
  "s_barrier\n" \
  "ds_read_b64_tr_b16 v[148:149], v9 offset:0\n" \
  "ds_read_b64_tr_b16 v[150:151], v9 offset:512\n" \
  "ds_read_b64_tr_b16 v[204:205], v9 offset:28672\n" \
  "ds_read_b64_tr_b16 v[206:207], v9 offset:29184\n" \
  "ds_read_b64_tr_b16 v[152:153], v9 offset:4096\n" \
  "ds_read_b64_tr_b16 v[154:155], v9 offset:4608\n" \
  "ds_read_b64_tr_b16 v[208:209], v9 offset:32768\n" \
  "ds_read_b64_tr_b16 v[210:211], v9 offset:33280\n" \
  "ds_read_b64_tr_b16 v[156:157], v9 offset:8192\n" \
  "ds_read_b64_tr_b16 v[158:159], v9 offset:8704\n" \
  "ds_read_b64_tr_b16 v[212:213], v9 offset:36864\n" \
  "ds_read_b64_tr_b16 v[214:215], v9 offset:37376\n" \
  "ds_read_b64_tr_b16 v[160:161], v9 offset:12288\n" \
  "ds_read_b64_tr_b16 v[162:163], v9 offset:12800\n" \
  "ds_read_b64_tr_b16 v[216:217], v9 offset:40960\n" \
  "ds_read_b64_tr_b16 v[218:219], v9 offset:41472\n" \
  "ds_read_b64_tr_b16 v[164:165], v9 offset:16384\n" \
  "ds_read_b64_tr_b16 v[166:167], v9 offset:16896\n" \
  "ds_read_b64_tr_b16 v[220:221], v9 offset:45056\n" \
  "ds_read_b64_tr_b16 v[222:223], v9 offset:45568\n" \
  "ds_read_b64_tr_b16 v[168:169], v9 offset:20480\n" \
  "ds_read_b64_tr_b16 v[170:171], v9 offset:20992\n" \
  "ds_read_b64_tr_b16 v[224:225], v9 offset:49152\n" \
  "ds_read_b64_tr_b16 v[226:227], v9 offset:49664\n" \
  "ds_read_b64_tr_b16 v[172:173], v9 offset:24576\n" \
  "ds_read_b64_tr_b16 v[174:175], v9 offset:25088\n" \
  "ds_read_b64_tr_b16 v[228:229], v9 offset:53248\n" \
  "ds_read_b64_tr_b16 v[230:231], v9 offset:53760\n" \
  "v_min_u32 v24, 13, v1\n" \
  "s_lshr_b32 s54, s26, 1\n" \
  "s_mul_i32 s54, s54, 112\n" \
  "s_add_u32 s54, s54, s24\n" \
  "s_add_u32 s54, s54, 32\n" \
  "v_lshl_add_u32 v29, v24, 3, s54\n" \
  "v_cndmask_b32 v29, v0, v29, s[72:73]\n" \
  "v_lshlrev_b32 v44, 3, v29\n" \
  "buffer_load_dwordx2 v[46:47], v44, s[36:39], 0 offen offset:256 sc1\n" \
  "v_mbcnt_lo_u32_b32 v31, -1, 0\n" \
  "v_lshl_add_u32 v31, v31, 7, s27\n" \
  "v_min_u32 v31, s3, v31\n" \
  "s_mov_b64 exec, 0xfffff\n" \
  "global_load_dword v30, v31, s[0:1]\n" \
  "s_mov_b64 exec, -1\n" \
  "v_mov_b32 v40, 0x3f803f80\n" \
  "v_mov_b32 v41, v40\n" \
  "v_mov_b32 v42, v40\n" \
  "v_mov_b32 v43, v40\n" \
  "s_waitcnt lgkmcnt(0)\n" \
  "s_nop 0\n" \
  "v_mfma_f32_16x16x32_bf16 v[112:115], v[120:123], v[40:43], 0\n" \
  "v_mfma_f32_16x16x32_bf16 v[116:119], v[176:179], v[40:43], 0\n" \
  "v_mfma_f32_16x16x32_bf16 v[112:115], v[124:127], v[40:43], v[112:115]\n" \
  "v_mfma_f32_16x16x32_bf16 v[116:119], v[180:183], v[40:43], v[116:119]\n" \
  "v_mfma_f32_16x16x32_bf16 v[112:115], v[128:131], v[40:43], v[112:115]\n" \
  "v_mfma_f32_16x16x32_bf16 v[116:119], v[184:187], v[40:43], v[116:119]\n" \
  "v_mfma_f32_16x16x32_bf16 v[112:115], v[132:135], v[40:43], v[112:115]\n" \
  "v_mfma_f32_16x16x32_bf16 v[116:119], v[188:191], v[40:43], v[116:119]\n" \
  "v_mfma_f32_16x16x32_bf16 v[112:115], v[136:139], v[40:43], v[112:115]\n" \
  "v_mfma_f32_16x16x32_bf16 v[116:119], v[192:195], v[40:43], v[116:119]\n" \
  "v_mfma_f32_16x16x32_bf16 v[112:115], v[140:143], v[40:43], v[112:115]\n" \
  "v_mfma_f32_16x16x32_bf16 v[116:119], v[196:199], v[40:43], v[116:119]\n" \
  "v_mfma_f32_16x16x32_bf16 v[112:115], v[144:147], v[40:43], v[112:115]\n" \
  "v_mfma_f32_16x16x32_bf16 v[116:119], v[200:203], v[40:43], v[116:119]\n" \
  "v_mfma_f32_16x16x32_bf16 v[112:115], v[148:151], v[40:43], v[112:115]\n" \
  "v_mfma_f32_16x16x32_bf16 v[116:119], v[204:207], v[40:43], v[116:119]\n" \
  "v_mfma_f32_16x16x32_bf16 v[112:115], v[152:155], v[40:43], v[112:115]\n" \
  "v_mfma_f32_16x16x32_bf16 v[116:119], v[208:211], v[40:43], v[116:119]\n" \
  "v_mfma_f32_16x16x32_bf16 v[112:115], v[156:159], v[40:43], v[112:115]\n" \
  "v_mfma_f32_16x16x32_bf16 v[116:119], v[212:215], v[40:43], v[116:119]\n" \
  "v_mfma_f32_16x16x32_bf16 v[112:115], v[160:163], v[40:43], v[112:115]\n" \
  "v_mfma_f32_16x16x32_bf16 v[116:119], v[216:219], v[40:43], v[116:119]\n" \
  "v_mfma_f32_16x16x32_bf16 v[112:115], v[164:167], v[40:43], v[112:115]\n" \
  "v_mfma_f32_16x16x32_bf16 v[116:119], v[220:223], v[40:43], v[116:119]\n" \
  "v_mfma_f32_16x16x32_bf16 v[112:115], v[168:171], v[40:43], v[112:115]\n" \
  "v_mfma_f32_16x16x32_bf16 v[116:119], v[224:227], v[40:43], v[116:119]\n" \
  "v_mfma_f32_16x16x32_bf16 v[112:115], v[172:175], v[40:43], v[112:115]\n" \
  "v_mfma_f32_16x16x32_bf16 v[116:119], v[228:231], v[40:43], v[116:119]\n" \
  "s_waitcnt vmcnt(0)\n" \
  "v_xor_b32 v13, 0, v8\n" \
  "ds_read_b64_tr_b16 a[224:225], v13 offset:0\n" \
  "ds_read_b64_tr_b16 a[226:227], v13 offset:1024\n" \
  "ds_read_b64_tr_b16 a[228:229], v13 offset:8192\n" \
  "ds_read_b64_tr_b16 a[230:231], v13 offset:9216\n" \
  "v_xor_b32 v13, 32, v8\n" \
  "ds_read_b64_tr_b16 a[232:233], v13 offset:0\n" \
  "ds_read_b64_tr_b16 a[234:235], v13 offset:1024\n" \
  "ds_read_b64_tr_b16 a[236:237], v13 offset:8192\n" \
  "ds_read_b64_tr_b16 a[238:239], v13 offset:9216\n" \
  "v_xor_b32 v13, 64, v8\n" \
  "ds_read_b64_tr_b16 a[240:241], v13 offset:0\n" \
  "ds_read_b64_tr_b16 a[242:243], v13 offset:1024\n" \
  "ds_read_b64_tr_b16 a[244:245], v13 offset:8192\n" \
  "ds_read_b64_tr_b16 a[246:247], v13 offset:9216\n" \
  "v_xor_b32 v13, 96, v8\n" \
  "ds_read_b64_tr_b16 a[248:249], v13 offset:0\n" \
  "ds_read_b64_tr_b16 a[250:251], v13 offset:1024\n" \
  "ds_read_b64_tr_b16 a[252:253], v13 offset:8192\n" \
  "ds_read_b64_tr_b16 a[254:255], v13 offset:9216\n" \
  "v_xor_b32 v13, 128, v8\n" \
  "ds_read_b64_tr_b16 v[232:233], v13 offset:0\n" \
  "ds_read_b64_tr_b16 v[234:235], v13 offset:1024\n" \
  "ds_read_b64_tr_b16 v[236:237], v13 offset:8192\n" \
  "ds_read_b64_tr_b16 v[238:239], v13 offset:9216\n" \
  "v_xor_b32 v13, 160, v8\n" \
  "ds_read_b64_tr_b16 v[240:241], v13 offset:0\n" \
  "ds_read_b64_tr_b16 v[242:243], v13 offset:1024\n" \
  "ds_read_b64_tr_b16 v[244:245], v13 offset:8192\n" \
  "ds_read_b64_tr_b16 v[246:247], v13 offset:9216\n" \
  "v_xor_b32 v13, 192, v8\n" \
  "ds_read_b64_tr_b16 v[248:249], v13 offset:0\n" \
  "ds_read_b64_tr_b16 v[250:251], v13 offset:1024\n" \
  "ds_read_b64_tr_b16 v[252:253], v13 offset:8192\n" \
  "ds_read_b64_tr_b16 v[254:255], v13 offset:9216\n" \
  "v_mov_b32 v14, v20\n" \
  "v_min_u32 v24, 13, v1\n" \
  "v_and_b32 v25, 1, v2\n" \
  "v_lshrrev_b32 v26, 1, v2\n" \
  "v_and_b32 v27, 1, v0\n" \
  "v_lshrrev_b32 v28, 1, v1\n" \
  "v_lshlrev_b32 v31, 4, v2\n" \
  "s_lshl_b32 s54, s26, 8\n" \
  "v_lshlrev_b32 v3, 10, v2\n" \
  "v_lshl_add_u32 v3, v24, 4, v3\n" \
  "v_add_u32 v3, s54, v3\n" \
  "v_lshl_add_u32 v29, v2, 2, v1\n" \
  "v_and_b32 v29, 15, v29\n" \
  "v_min_u32 v29, 13, v29\n" \
  "v_mul_u32_u24 v5, 640, v29\n" \
  "s_and_b32 s55, s26, 1\n" \
  "s_lshl_b32 s55, s55, 8\n" \
  "v_mul_u32_u24 v4, 640, v2\n" \
  "v_lshl_add_u32 v4, v1, 4, v4\n" \
  "v_add_u32 v4, s55, v4\n" \
  "v_mov_b32 v29, 2560\n" \
  "v_mov_b32 v30, 1280\n" \
  "v_cndmask_b32 v29, v29, v30, s[72:73]\n" \
  "v_add_u32 v111, v29, v4\n" \
  "s_mul_i32 s55, s26, 4352\n" \
  "v_mul_u32_u24 v15, 272, v2\n" \
  "v_lshl_add_u32 v15, v1, 4, v15\n" \
  "v_add_u32 v15, s55, v15\n" \
  "v_mul_u32_u24 v45, 272, v1\n" \
  "v_add_u32 v45, v45, v31\n" \
  "v_add_u32 v45, s55, v45\n" \
  "s_mul_i32 s54, s26, 224\n" \
  "v_lshlrev_b32 v29, 4, v1\n" \
  "v_lshl_add_u32 v29, v25, 3, v29\n" \
  "v_add_u32 v29, s54, v29\n" \
  "v_mul_u32_u24 v6, 912, v26\n" \
  "v_add_u32 v6, v6, v29\n" \
  "v_add_u32 v6, 131072, v6\n" \
  "v_mul_u32_u24 v7, 912, v27\n" \
  "v_add_u32 v7, 131072, v7\n" \
  "v_lshrrev_b32 v29, 1, v0\n" \
  "v_lshl_add_u32 v8, v29, 5, v7\n" \
  "v_cmp_gt_u32 vcc, 28, v29\n" \
  "v_mov_b32 v30, 136448\n" \
  "s_nop 0\n" \
  "v_cndmask_b32 v8, v30, v8, vcc\n" \
  "v_add_u32 v7, v7, v31\n" \
  "s_lshl_b32 s55, s26, 5\n" \
  "s_add_u32 s55, s55, 136192\n" \
  "v_lshl_add_u32 v9, v27, 7, s55\n" \
  "v_lshl_add_u32 v9, v2, 3, v9\n" \
  "v_lshlrev_b32 v29, 7, v27\n" \
  "v_add_u32 v29, 136192, v29\n" \
  "v_add_u32 v29, v29, v31\n" \
  "v_add_u32 v30, 136512, v31\n" \
  "v_cmp_eq_u32 vcc, 0, v28\n" \
  "s_nop 1\n" \
  "v_cndmask_b32 v17, v30, v29, vcc\n" \
  "v_cmp_eq_u32 vcc, 1, v28\n" \
  "s_nop 1\n" \
  "v_cndmask_b32 v18, v30, v29, vcc\n" \
  "v_cmp_eq_u32 vcc, 2, v28\n" \
  "s_nop 1\n" \
  "v_cndmask_b32 v19, v30, v29, vcc\n" \
  "v_cmp_eq_u32 vcc, 3, v28\n" \
  "s_nop 1\n" \
  "v_cndmask_b32 v20, v30, v29, vcc\n" \
  "v_cmp_eq_u32 vcc, 4, v28\n" \
  "s_nop 1\n" \
  "v_cndmask_b32 v21, v30, v29, vcc\n" \
  "v_cmp_eq_u32 vcc, 5, v28\n" \
  "s_nop 1\n" \
  "v_cndmask_b32 v22, v30, v29, vcc\n" \
  "v_cmp_eq_u32 vcc, 6, v28\n" \
  "s_nop 1\n" \
  "v_cndmask_b32 v23, v30, v29, vcc\n" \
  "s_lshl_b32 s55, s26, 9\n" \
  "s_add_u32 s55, s55, 139264\n" \
  "v_lshl_add_u32 v10, v2, 7, s55\n" \
  "v_lshl_add_u32 v10, v1, 2, v10\n" \
  "v_lshlrev_b32 v11, 3, v0\n" \
  "v_add_u32 v11, 139264, v11\n" \
  "s_mul_i32 s55, s26, 448\n" \
  "s_add_u32 s55, s55, 143360\n" \
  "v_lshl_add_u32 v12, v24, 5, s55\n" \
  "v_lshl_add_u32 v12, v25, 4, v12\n" \
  "s_lshl_b32 s55, s63, 10\n" \
  "s_lshl_b32 s54, s26, 8\n" \
  "s_add_u32 s55, s55, s54\n" \
  "s_add_u32 s55, s55, 1368064\n" \
  "v_lshl_add_u32 v13, v28, 5, s55\n" \
  "v_lshl_add_u32 v13, v2, 3, v13\n" \
  "s_mul_i32 s54, s63, 37\n" \
  "s_lshr_b32 s54, s54, 8\n" \
  "s_lshl_b32 s54, s54, 5\n" \
  "s_lshl_b32 s55, s63, 5\n" \
  "s_add_u32 s54, s54, s55\n" \
  "v_min_u32 v29, 6, v28\n" \
  "v_lshl_add_u32 v29, v29, 2, v2\n" \
  "v_lshlrev_b32 v108, 10, v29\n" \
  "v_lshl_add_u32 v108, v27, 4, v108\n" \
  "v_add_u32 v108, s54, v108\n" \
  "v_lshlrev_b32 v110, 4, v27\n" \
  "v_lshl_add_u32 v110, v25, 3, v110\n" \
  "v_add_u32 v110, s55, v110\n" \
  "s_lshl_b32 s54, s26, 11\n" \
  "s_add_u32 s54, s54, 151552\n" \
  "v_lshl_add_u32 v109, v110, 1, s54\n" \
  "v_lshlrev_b32 v29, 4, v27\n" \
  "v_mov_b32 v30, 0x3f80\n" \
  "v_lshlrev_b32 v30, v29, v30\n" \
  "v_cmp_eq_u32 vcc, 0, v28\n" \
  "s_nop 1\n" \
  "v_cndmask_b32 v96, 0, v30, vcc\n" \
  "v_cmp_eq_u32 vcc, 4, v28\n" \
  "s_nop 1\n" \
  "v_cndmask_b32 v100, 0, v30, vcc\n" \
  "v_cmp_eq_u32 vcc, 1, v28\n" \
  "s_nop 1\n" \
  "v_cndmask_b32 v97, 0, v30, vcc\n" \
  "v_cmp_eq_u32 vcc, 5, v28\n" \
  "s_nop 1\n" \
  "v_cndmask_b32 v101, 0, v30, vcc\n" \
  "v_cmp_eq_u32 vcc, 2, v28\n" \
  "s_nop 1\n" \
  "v_cndmask_b32 v98, 0, v30, vcc\n" \
  "v_cmp_eq_u32 vcc, 6, v28\n" \
  "s_nop 1\n" \
  "v_cndmask_b32 v102, 0, v30, vcc\n" \
  "v_cmp_eq_u32 vcc, 3, v28\n" \
  "s_nop 1\n" \
  "v_cndmask_b32 v99, 0, v30, vcc\n" \
  "v_cmp_eq_u32 vcc, 7, v28\n" \
  "s_nop 1\n" \
  "v_cndmask_b32 v103, 0, v30, vcc\n" \
  "v_cmp_gt_u32 vcc, 56, v0\n" \
  "v_lshlrev_b32 v29, 4, v0\n" \
  "v_add_u32 v29, 131072, v29\n" \
  "v_mov_b32 v30, 136448\n" \
  "v_cndmask_b32 v2, v30, v29, vcc\n" \
  "v_add_u32 v1, 131072, v31\n" \
  "v_mov_b32 v27, v45\n" \
  "s_lshr_b32 s54, s26, 1\n" \
  "s_lshl_b32 s55, s24, 1\n" \
  "s_add_u32 s54, s54, s55\n" \
  "s_mul_i32 s69, s54, 8960\n" \
  "s_add_u32 s69, s69, 4096\n" \
  "s_add_u32 s71, s64, 1294336\n" \
  "s_mov_b32 s52, -1\n" \
  "s_mov_b32 s53, 0x10000\n" \
  "s_waitcnt lgkmcnt(0)\n" \
  "s_barrier\n" \
  "s_branch .LS_TAGC\n" \
  ".LS_TAG:\n" \
  "buffer_load_dwordx2 v[46:47], v44, s[36:39], 0 offen offset:256 sc1\n" \
  "s_waitcnt vmcnt(0)\n" \
  ".LS_TAGC:\n" \
  "v_cmp_ne_u64 vcc, s[96:97], v[46:47]\n" \
  "s_nop 0\n" \
  "s_cbranch_vccz .LS_OK1\n" \
  "s_sleep 8\n" \
  "s_sub_u32 s53, s53, 1\n" \
  "s_cbranch_scc0 .LS_TAG\n" \
  "s_mov_b32 s53, 0\n" \
  ".LS_OK1:\n" \
  "s_mov_b32 s31, s38\n" \
  "s_mov_b64 s[40:41], s[36:37]\n" \
  "s_mov_b32 s38, 0\n" \
  "s_mov_b32 s50, 0\n" \
  ".LS_ROUND:\n" \
  "s_min_u32 s67, s52, 3\n" \
  "s_cmp_lt_u32 s52, 4\n" \
  "s_cselect_b32 s65, s52, 7\n" \
  "s_cselect_b32 s70, 13, 16\n" \
  "s_min_u32 s99, s52, 4\n" \
  "s_add_u32 s98, s99, 4\n" \
  "s_max_i32 s68, s52, 2\n" \
  "s_sub_u32 s68, s68, 2\n" \
  "s_add_u32 s66, s68, 4\n" \
  "s_lshl_b32 s54, s67, 11\n" \
  "s_lshl_b32 s55, s68, 11\n" \
  "v_mov_b32 v24, s54\n" \
  "v_mov_b32 v25, s55\n" \
  "v_cndmask_b32 v24, v24, v25, s[72:73]\n" \
  "v_add_u32 v16, v24, v12\n" \
  "ds_read_b128 v[92:95], v16\n" \
  "s_mul_i32 s54, s65, 28672\n" \
  "s_mul_i32 s55, s66, 28672\n" \
  "v_mov_b32 v25, s54\n" \
  "v_mov_b32 v26, s55\n" \
  "v_cndmask_b32 v25, v25, v26, s[78:79]\n" \
  "v_add_u32 v0, v25, v13\n" \
  "s_andn2_b64 s[84:85], s[76:77], s[78:79]\n" \
  "s_mov_b32 s42, 0\n" \
  "s_waitcnt lgkmcnt(0)\n" \
  "s_cmp_lt_i32 s52, 2\n" \
  "s_cbranch_scc1 .LS_NOLAG\n" \
  "s_mov_b64 s[84:85], s[76:77]\n" \
  "s_mov_b32 s42, s38\n" \
  "s_mul_i32 s58, s66, 143360\n" \
  "s_add_u32 s58, s58, s69\n" \
  "s_lshl_b32 s60, s66, 13\n" \
  "s_add_u32 s60, s60, s71\n" \
  "s_call_b64 s[88:89], .LS_WO\n" \
  ".LS_NOLAG:\n" \
  "s_mul_i32 s58, s65, 143360\n" \
  "s_add_u32 s58, s58, s69\n" \
  "s_lshl_b32 s60, s65, 13\n" \
  "s_add_u32 s60, s60, s71\n" \
  "s_call_b64 s[88:89], .LS_WO\n" \
  "s_lshl_b32 s58, s66, 13\n" \
  "s_add_u32 s58, s58, 1294336\n" \
  "s_add_u32 s59, s58, 4096\n" \
  "s_lshl_b32 s54, s65, 13\n" \
  "s_add_u32 s54, s54, 1294336\n" \
  "s_add_u32 s55, s54, 4096\n" \
  "buffer_load_dwordx4 v[40:43], v3, s[40:43], s58 offen sc1\n" \
  "buffer_load_dwordx4 v[44:47], v3, s[40:43], s59 offen sc1\n" \
  "s_cmp_lt_i32 s52, 0\n" \
  "s_cbranch_scc1 .LS_GY0\n" \
  "s_cmp_lt_u32 s52, 3\n" \
  "s_cbranch_scc1 .LS_DYL\n" \
  "s_cmp_eq_u32 s52, 3\n" \
  "s_cbranch_scc1 .LS_DY3\n" \
  "s_sleep 12\n" \
  "s_branch .LS_GY0\n" \
  ".LS_DYL:\n" \
  "s_sleep 14\n" \
  "s_branch .LS_GY0\n" \
  ".LS_DY3:\n" \
  "s_sleep 10\n" \
  "s_branch .LS_GY0\n" \
  ".LS_GY1:\n" \
  "s_mov_b64 exec, s[94:95]\n" \
  "buffer_load_dwordx4 v[40:43], v3, s[40:43], s58 offen sc1\n" \
  "buffer_load_dwordx4 v[44:47], v3, s[40:43], s59 offen sc1\n" \
  ".LS_GY0:\n" \
  "buffer_load_dwordx4 v[32:35], v3, s[36:39], s54 offen sc1\n" \
  "buffer_load_dwordx4 v[36:39], v3, s[36:39], s55 offen sc1\n" \
  "s_mov_b64 exec, -1\n" \
  "s_waitcnt vmcnt(3)\n" \
  "v_mfma_f32_16x16x32_bf16 v[88:91], v[100:103], v[40:43], v[92:95]\n" \
  "v_max_u32 v24, v40, v41\n" \
  "v_max3_u32 v24, v24, v42, v43\n" \
  "s_waitcnt vmcnt(2)\n" \
  "v_mfma_f32_16x16x32_bf16 v[88:91], v[100:103], v[44:47], v[88:91]\n" \
  "v_max3_u32 v24, v24, v44, v45\n" \
  "v_max3_u32 v24, v24, v46, v47\n" \
  "s_waitcnt vmcnt(1)\n" \
  "v_mfma_f32_16x16x32_bf16 v[88:91], v[96:99], v[32:35], v[88:91]\n" \
  "v_max3_u32 v24, v24, v32, v33\n" \
  "v_max3_u32 v24, v24, v34, v35\n" \
  "s_waitcnt vmcnt(0)\n" \
  "v_mfma_f32_16x16x32_bf16 v[88:91], v[96:99], v[36:39], v[88:91]\n" \
  "v_max3_u32 v24, v24, v36, v37\n" \
  "v_max3_u32 v24, v24, v38, v39\n" \
  "v_cmp_eq_u32 vcc, -1, v24\n" \
  "s_nop 3\n" \
  "s_cbranch_vccz .LS_OK2\n" \
  "s_mov_b64 s[94:95], vcc\n" \
  "s_sub_u32 s53, s53, 1\n" \
  "s_cbranch_scc0 .LS_GY1\n" \
  "s_mov_b32 s53, 0\n" \
  ".LS_OK2:\n" \
  "s_cmp_ge_u32 s62, 28\n" \
  "s_cbranch_scc1 .LS_NOMLP\n" \
  "v_cvt_pk_bf16_f32 v24, v88, v89\n" \
  "v_cvt_pk_bf16_f32 v25, v90, v91\n" \
  "s_mov_b64 exec, s[76:77]\n" \
  "ds_write_b64 v6, v[24:25]\n" \
  "s_mov_b64 exec, -1\n" \
  "s_waitcnt lgkmcnt(0)\n" \
  "s_barrier\n" \
  "ds_read_b128 v[80:83], v8 offset:0\n" \
  "ds_read_b128 v[84:87], v8 offset:16\n" \
  "ds_read_b128 v[32:35], v7 offset:0\n" \
  "ds_read_b128 v[36:39], v7 offset:64\n" \
  "ds_read_b128 v[40:43], v7 offset:128\n" \
  "v_mov_b32 v24, 0\n" \
  "v_mov_b32 v26, 0\n" \
  "s_waitcnt lgkmcnt(4)\n" \
  "v_dot2c_f32_bf16 v24, 0x3f803f80, v80\n" \
  "v_dot2c_f32_bf16 v26, v80, v80\n" \
  "v_dot2c_f32_bf16 v24, 0x3f803f80, v81\n" \
  "v_dot2c_f32_bf16 v26, v81, v81\n" \
  "v_dot2c_f32_bf16 v24, 0x3f803f80, v82\n" \
  "v_dot2c_f32_bf16 v26, v82, v82\n" \
  "v_dot2c_f32_bf16 v24, 0x3f803f80, v83\n" \
  "v_dot2c_f32_bf16 v26, v83, v83\n" \
  "s_waitcnt lgkmcnt(3)\n" \
  "v_dot2c_f32_bf16 v24, 0x3f803f80, v84\n" \
  "v_dot2c_f32_bf16 v26, v84, v84\n" \
  "v_dot2c_f32_bf16 v24, 0x3f803f80, v85\n" \
  "v_dot2c_f32_bf16 v26, v85, v85\n" \
  "v_dot2c_f32_bf16 v24, 0x3f803f80, v86\n" \
  "v_dot2c_f32_bf16 v26, v86, v86\n" \
  "v_dot2c_f32_bf16 v24, 0x3f803f80, v87\n" \
  "v_dot2c_f32_bf16 v26, v87, v87\n" \
  "s_waitcnt lgkmcnt(2)\n" \
  "v_mfma_f32_16x16x32_bf16 v[72:75], v[120:123], v[32:35], 0\n" \
  "ds_read_b128 v[44:47], v7 offset:192\n" \
  "v_add_f32_dpp v24, v24, v24 quad_perm:[2,3,0,1] row_mask:0xf bank_mask:0xf\n" \
  "v_add_f32_dpp v26, v26, v26 quad_perm:[2,3,0,1] row_mask:0xf bank_mask:0xf\n" \
  "v_mfma_f32_16x16x32_bf16 v[76:79], v[176:179], v[32:35], 0\n" \
  "ds_read_b128 v[48:51], v7 offset:256\n" \
  "v_add_f32_dpp v24, v24, v24 row_ror:4 row_mask:0xf bank_mask:0xf\n" \
  "v_add_f32_dpp v26, v26, v26 row_ror:4 row_mask:0xf bank_mask:0xf\n" \
  "s_waitcnt lgkmcnt(3)\n" \
  "v_mfma_f32_16x16x32_bf16 v[72:75], v[124:127], v[36:39], v[72:75]\n" \
  "ds_read_b128 v[52:55], v7 offset:320\n" \
  "v_add_f32_dpp v24, v24, v24 row_ror:8 row_mask:0xf bank_mask:0xf\n" \
  "v_add_f32_dpp v26, v26, v26 row_ror:8 row_mask:0xf bank_mask:0xf\n" \
  "v_mfma_f32_16x16x32_bf16 v[76:79], v[180:183], v[36:39], v[76:79]\n" \
  "ds_read_b128 v[56:59], v7 offset:384\n" \
  "v_mov_b32 v28, v24\n" \
  "v_mov_b32 v29, v26\n" \
  "s_waitcnt lgkmcnt(4)\n" \
  "v_mfma_f32_16x16x32_bf16 v[72:75], v[128:131], v[40:43], v[72:75]\n" \
  "ds_read_b128 v[60:63], v7 offset:448\n" \
  "v_permlane16_swap_b32 v24, v28\n" \
  "v_mfma_f32_16x16x32_bf16 v[76:79], v[184:187], v[40:43], v[76:79]\n" \
  "ds_read_b128 v[64:67], v7 offset:512\n" \
  "v_permlane16_swap_b32 v26, v29\n" \
  "s_waitcnt lgkmcnt(5)\n" \
  "v_mfma_f32_16x16x32_bf16 v[72:75], v[132:135], v[44:47], v[72:75]\n" \
  "ds_read_b128 v[68:71], v7 offset:576\n" \
  "v_add_f32 v24, v24, v28\n" \
  "v_add_f32 v26, v26, v29\n" \
  "v_mfma_f32_16x16x32_bf16 v[76:79], v[188:191], v[44:47], v[76:79]\n" \
  "v_mov_b32 v28, v24\n" \
  "v_mov_b32 v29, v26\n" \
  "s_waitcnt lgkmcnt(5)\n" \
  "v_mfma_f32_16x16x32_bf16 v[72:75], v[136:139], v[48:51], v[72:75]\n" \
  "ds_read_b128 v[32:35], v7 offset:640\n" \
  "v_permlane32_swap_b32 v24, v28\n" \
  "v_mfma_f32_16x16x32_bf16 v[76:79], v[192:195], v[48:51], v[76:79]\n" \
  "v_permlane32_swap_b32 v26, v29\n" \
  "s_waitcnt lgkmcnt(5)\n" \
  "v_mfma_f32_16x16x32_bf16 v[72:75], v[140:143], v[52:55], v[72:75]\n" \
  "ds_read_b128 v[36:39], v7 offset:704\n" \
  "v_add_f32 v24, v24, v28\n" \
  "v_add_f32 v26, v26, v29\n" \
  "v_mfma_f32_16x16x32_bf16 v[76:79], v[196:199], v[52:55], v[76:79]\n" \
  "v_mul_f32 v24, 0x3b124925, v24\n" \
  "v_mul_f32 v26, 0x3b124925, v26\n" \
  "s_waitcnt lgkmcnt(5)\n" \
  "v_mfma_f32_16x16x32_bf16 v[72:75], v[144:147], v[56:59], v[72:75]\n" \
  "ds_read_b128 v[40:43], v7 offset:768\n" \
  "v_fma_f32 v26, -v24, v24, v26\n" \
  "v_max_f32 v26, 0, v26\n" \
  "v_mfma_f32_16x16x32_bf16 v[76:79], v[200:203], v[56:59], v[76:79]\n" \
  "v_add_f32 v26, 0x3727c5ac, v26\n" \
  "s_waitcnt lgkmcnt(5)\n" \
  "v_mfma_f32_16x16x32_bf16 v[72:75], v[148:151], v[60:63], v[72:75]\n" \
  "ds_read_b128 v[44:47], v7 offset:832\n" \
  "v_rsq_f32 v26, v26\n" \
  "v_mfma_f32_16x16x32_bf16 v[76:79], v[204:207], v[60:63], v[76:79]\n" \
  "v_xor_b32 v24, 0x80000000, v24\n" \
  "v_mul_f32 v26, v26, v26\n" \
  "s_waitcnt lgkmcnt(5)\n" \
  "v_mfma_f32_16x16x32_bf16 v[72:75], v[152:155], v[64:67], v[72:75]\n" \
  "v_mfma_f32_16x16x32_bf16 v[76:79], v[208:211], v[64:67], v[76:79]\n" \
  "s_waitcnt lgkmcnt(0)\n" \
  "v_mfma_f32_16x16x32_bf16 v[72:75], v[156:159], v[68:71], v[72:75]\n" \
  "s_nop 1\n" \
  "v_mfma_f32_16x16x32_bf16 v[72:75], v[160:163], v[32:35], v[72:75]\n" \
  "s_nop 1\n" \
  "v_mfma_f32_16x16x32_bf16 v[72:75], v[164:167], v[36:39], v[72:75]\n" \
  "s_nop 1\n" \
  "v_mfma_f32_16x16x32_bf16 v[72:75], v[168:171], v[40:43], v[72:75]\n" \
  "s_nop 1\n" \
  "v_mfma_f32_16x16x32_bf16 v[72:75], v[172:175], v[44:47], v[72:75]\n" \
  "s_nop 1\n" \
  "v_mfma_f32_16x16x32_bf16 v[76:79], v[212:215], v[68:71], v[76:79]\n" \
  "s_nop 1\n" \
  "v_mfma_f32_16x16x32_bf16 v[76:79], v[216:219], v[32:35], v[76:79]\n" \
  "s_nop 1\n" \
  "v_mfma_f32_16x16x32_bf16 v[76:79], v[220:223], v[36:39], v[76:79]\n" \
  "v_fmac_f32 v72, v24, v112\n" \
  "v_fmac_f32 v73, v24, v113\n" \
  "v_mfma_f32_16x16x32_bf16 v[76:79], v[224:227], v[40:43], v[76:79]\n" \
  "v_fmac_f32 v74, v24, v114\n" \
  "v_fmac_f32 v75, v24, v115\n" \
  "v_mfma_f32_16x16x32_bf16 v[76:79], v[228:231], v[44:47], v[76:79]\n" \
  "v_max_f32 v72, 0, v72\n" \
  "v_max_f32 v73, 0, v73\n" \
  "v_max_f32 v74, 0, v74\n" \
  "v_max_f32 v75, 0, v75\n" \
  "v_mul_f32 v72, v26, v72\n" \
  "v_mul_f32 v73, v26, v73\n" \
  "v_mul_f32 v74, v26, v74\n" \
  "v_mul_f32 v75, v26, v75\n" \
  "v_fmac_f32 v76, v24, v116\n" \
  "v_fmac_f32 v77, v24, v117\n" \
  "v_fmac_f32 v78, v24, v118\n" \
  "v_fmac_f32 v79, v24, v119\n" \
  "v_mul_f32 v72, v72, v76\n" \
  "v_mul_f32 v73, v73, v77\n" \
  "v_mul_f32 v74, v74, v78\n" \
  "v_mul_f32 v75, v75, v79\n" \
  "v_cvt_pk_bf16_f32 v24, v72, v73\n" \
  "v_cvt_pk_bf16_f32 v25, v74, v75\n" \
  "s_mov_b64 exec, s[80:81]\n" \
  "ds_write_b64 v9, v[24:25]\n" \
  "s_mov_b64 exec, -1\n" \
  "s_waitcnt lgkmcnt(0)\n" \
  "s_barrier\n" \
  "ds_read_b128 v[32:35], v17 offset:0\n" \
  "ds_read_b128 v[36:39], v17 offset:64\n" \
  "ds_read_b128 v[40:43], v18 offset:0\n" \
  "ds_read_b128 v[44:47], v18 offset:64\n" \
  "s_waitcnt lgkmcnt(3)\n" \
  "v_mfma_f32_16x16x32_bf16 v[92:95], a[224:227], v[32:35], 0\n" \
  "ds_read_b128 v[48:51], v19 offset:0\n" \
  "s_waitcnt lgkmcnt(3)\n" \
  "v_mfma_f32_16x16x32_bf16 v[92:95], a[228:231], v[36:39], v[92:95]\n" \
  "ds_read_b128 v[52:55], v19 offset:64\n" \
  "s_waitcnt lgkmcnt(3)\n" \
  "v_mfma_f32_16x16x32_bf16 v[92:95], a[232:235], v[40:43], v[92:95]\n" \
  "ds_read_b128 v[56:59], v20 offset:0\n" \
  "s_waitcnt lgkmcnt(3)\n" \
  "v_mfma_f32_16x16x32_bf16 v[92:95], a[236:239], v[44:47], v[92:95]\n" \
  "ds_read_b128 v[60:63], v20 offset:64\n" \
  "s_waitcnt lgkmcnt(3)\n" \
  "v_mfma_f32_16x16x32_bf16 v[92:95], a[240:243], v[48:51], v[92:95]\n" \
  "ds_read_b128 v[64:67], v21 offset:0\n" \
  "s_waitcnt lgkmcnt(3)\n" \
  "v_mfma_f32_16x16x32_bf16 v[92:95], a[244:247], v[52:55], v[92:95]\n" \
  "ds_read_b128 v[68:71], v21 offset:64\n" \
  "s_waitcnt lgkmcnt(3)\n" \
  "v_mfma_f32_16x16x32_bf16 v[92:95], a[248:251], v[56:59], v[92:95]\n" \
  "ds_read_b128 v[72:75], v22 offset:0\n" \
  "s_waitcnt lgkmcnt(3)\n" \
  "v_mfma_f32_16x16x32_bf16 v[92:95], a[252:255], v[60:63], v[92:95]\n" \
  "ds_read_b128 v[76:79], v22 offset:64\n" \
  "s_waitcnt lgkmcnt(3)\n" \
  "v_mfma_f32_16x16x32_bf16 v[92:95], v[232:235], v[64:67], v[92:95]\n" \
  "ds_read_b128 v[80:83], v23 offset:0\n" \
  "s_waitcnt lgkmcnt(3)\n" \
  "v_mfma_f32_16x16x32_bf16 v[92:95], v[236:239], v[68:71], v[92:95]\n" \
  "ds_read_b128 v[84:87], v23 offset:64\n" \
  "s_waitcnt lgkmcnt(3)\n" \
  "v_mfma_f32_16x16x32_bf16 v[92:95], v[240:243], v[72:75], v[92:95]\n" \
  "s_waitcnt lgkmcnt(2)\n" \
  "v_mfma_f32_16x16x32_bf16 v[92:95], v[244:247], v[76:79], v[92:95]\n" \
  "s_waitcnt lgkmcnt(1)\n" \
  "v_mfma_f32_16x16x32_bf16 v[92:95], v[248:251], v[80:83], v[92:95]\n" \
  "s_waitcnt lgkmcnt(0)\n" \
  "v_mfma_f32_16x16x32_bf16 v[92:95], v[252:255], v[84:87], v[92:95]\n" \
  "s_mov_b64 exec, s[76:77]\n" \
  "ds_write_b128 v16, v[88:91] offset:8192\n" \
  "s_mov_b64 exec, -1\n" \
  "s_nop 4\n" \
  "v_cvt_pk_bf16_f32 v24, v92, v93\n" \
  "v_cvt_pk_bf16_f32 v25, v94, v95\n" \
  "v_min_u32 v24, -2, v24\n" \
  "v_min_u32 v25, -2, v25\n" \
  "s_mov_b64 exec, s[84:85]\n" \
  "buffer_store_dwordx2 v[24:25], v0, s[36:39], 0 offen sc1\n" \
  "s_mov_b64 exec, -1\n" \
  "s_waitcnt lgkmcnt(0)\n" \
  "s_barrier\n" \
  "s_branch .LS_DLY\n" \
  ".LS_NOMLP:\n" \
  "s_sleep 16\n" \
  ".LS_DLY:\n" \
  "s_cmp_lt_i32 s52, 0\n" \
  "s_cbranch_scc1 .LS_AFTER\n" \
  "s_cmp_lt_u32 s52, 3\n" \
  "s_cbranch_scc1 .LS_DDL\n" \
  "s_cmp_eq_u32 s52, 3\n" \
  "s_cbranch_scc1 .LS_DD3\n" \
  "s_sleep 10\n" \
  "s_branch .LS_AFTER\n" \
  ".LS_DDL:\n" \
  "s_sleep 16\n" \
  "s_branch .LS_AFTER\n" \
  ".LS_DD3:\n" \
  "s_sleep 12\n" \
  "s_branch .LS_AFTER\n" \
  ".LS_AFTER:\n" \
  "s_cmp_eq_u32 s52, 4\n" \
  "s_cbranch_scc1 .LS_OUT\n" \
  "s_mul_i32 s54, s65, 28672\n" \
  "s_add_u32 s54, s54, 1368064\n" \
  "s_mov_b64 s[94:95], -1\n" \
  ".LS_GD0:\n" \
  "s_mov_b32 s55, s54\n" \
  "s_mov_b64 exec, s[94:95]\n" \
  "buffer_load_dwordx4 v[32:35], v3, s[36:39], s55 offen sc1\n" \
  "s_add_u32 s55, s55, 4096\n" \
  "buffer_load_dwordx4 v[36:39], v3, s[36:39], s55 offen sc1\n" \
  "s_add_u32 s55, s55, 4096\n" \
  "buffer_load_dwordx4 v[40:43], v3, s[36:39], s55 offen sc1\n" \
  "s_add_u32 s55, s55, 4096\n" \
  "buffer_load_dwordx4 v[44:47], v3, s[36:39], s55 offen sc1\n" \
  "s_add_u32 s55, s55, 4096\n" \
  "buffer_load_dwordx4 v[48:51], v3, s[36:39], s55 offen sc1\n" \
  "s_add_u32 s55, s55, 4096\n" \
  "buffer_load_dwordx4 v[52:55], v3, s[36:39], s55 offen sc1\n" \
  "s_add_u32 s55, s55, 4096\n" \
  "buffer_load_dwordx4 v[56:59], v3, s[36:39], s55 offen sc1\n" \
  "s_mov_b64 exec, -1\n" \
  "s_waitcnt vmcnt(6)\n" \
  "v_mfma_f32_16x16x32_bf16 v[92:95], v[96:99], v[32:35], v[88:91]\n" \
  "v_max_u32 v24, v32, v33\n" \
  "v_max3_u32 v24, v24, v34, v35\n" \
  "s_waitcnt vmcnt(5)\n" \
  "v_mfma_f32_16x16x32_bf16 v[92:95], v[96:99], v[36:39], v[92:95]\n" \
  "v_max3_u32 v24, v24, v36, v37\n" \
  "v_max3_u32 v24, v24, v38, v39\n" \
  "s_waitcnt vmcnt(4)\n" \
  "v_mfma_f32_16x16x32_bf16 v[92:95], v[96:99], v[40:43], v[92:95]\n" \
  "v_max3_u32 v24, v24, v40, v41\n" \
  "v_max3_u32 v24, v24, v42, v43\n" \
  "s_waitcnt vmcnt(3)\n" \
  "v_mfma_f32_16x16x32_bf16 v[92:95], v[96:99], v[44:47], v[92:95]\n" \
  "v_max3_u32 v24, v24, v44, v45\n" \
  "v_max3_u32 v24, v24, v46, v47\n" \
  "s_waitcnt vmcnt(2)\n" \
  "v_mfma_f32_16x16x32_bf16 v[92:95], v[96:99], v[48:51], v[92:95]\n" \
  "v_max3_u32 v24, v24, v48, v49\n" \
  "v_max3_u32 v24, v24, v50, v51\n" \
  "s_waitcnt vmcnt(1)\n" \
  "v_mfma_f32_16x16x32_bf16 v[92:95], v[96:99], v[52:55], v[92:95]\n" \
  "v_max3_u32 v24, v24, v52, v53\n" \
  "v_max3_u32 v24, v24, v54, v55\n" \
  "s_waitcnt vmcnt(0)\n" \
  "v_mfma_f32_16x16x32_bf16 v[92:95], v[96:99], v[56:59], v[92:95]\n" \
  "v_max3_u32 v24, v24, v56, v57\n" \
  "v_max3_u32 v24, v24, v58, v59\n" \
  "v_cmp_eq_u32 vcc, -1, v24\n" \
  "s_nop 3\n" \
  "s_cbranch_vccz .LS_OK3\n" \
  "s_mov_b64 s[94:95], vcc\n" \
  "s_sub_u32 s53, s53, 1\n" \
  "s_cbranch_scc0 .LS_GD0\n" \
  "s_mov_b32 s53, 0\n" \
  ".LS_OK3:\n" \
  "v_cvt_pk_bf16_f32 v24, v92, v93\n" \
  "v_cvt_pk_bf16_f32 v25, v94, v95\n" \
  "s_mov_b64 exec, s[86:87]\n" \
  "ds_write_b64 v6, v[24:25]\n" \
  "s_mov_b64 exec, -1\n" \
  "s_waitcnt lgkmcnt(0)\n" \
  "s_barrier\n" \
  "s_lshl_b32 s54, s98, 14\n" \
  "ds_read_b128 v[80:83], v2 offset:0\n" \
  "ds_read_b128 v[32:35], v1 offset:0\n" \
  "ds_read_b128 v[36:39], v1 offset:64\n" \
  "ds_read_b128 v[40:43], v1 offset:128\n" \
  "v_mov_b32 v24, 0\n" \
  "v_mov_b32 v26, 0\n" \
  "s_waitcnt lgkmcnt(3)\n" \
  "v_dot2c_f32_bf16 v24, 0x3f803f80, v80\n" \
  "v_dot2c_f32_bf16 v26, v80, v80\n" \
  "v_dot2c_f32_bf16 v24, 0x3f803f80, v81\n" \
  "v_dot2c_f32_bf16 v26, v81, v81\n" \
  "v_dot2c_f32_bf16 v24, 0x3f803f80, v82\n" \
  "v_dot2c_f32_bf16 v26, v82, v82\n" \
  "v_dot2c_f32_bf16 v24, 0x3f803f80, v83\n" \
  "v_dot2c_f32_bf16 v26, v83, v83\n" \
  "s_waitcnt lgkmcnt(2)\n" \
  "v_mfma_f32_16x16x32_bf16 v[72:75], a[0:3], v[32:35], 0\n" \
  "ds_read_b128 v[44:47], v1 offset:192\n" \
  "v_add_f32_dpp v24, v24, v24 quad_perm:[1,0,3,2] row_mask:0xf bank_mask:0xf\n" \
  "v_add_f32_dpp v26, v26, v26 quad_perm:[1,0,3,2] row_mask:0xf bank_mask:0xf\n" \
  "v_mfma_f32_16x16x32_bf16 v[76:79], a[56:59], v[32:35], 0\n" \
  "ds_read_b128 v[48:51], v1 offset:256\n" \
  "v_add_f32_dpp v24, v24, v24 quad_perm:[2,3,0,1] row_mask:0xf bank_mask:0xf\n" \
  "v_add_f32_dpp v26, v26, v26 quad_perm:[2,3,0,1] row_mask:0xf bank_mask:0xf\n" \
  "s_waitcnt lgkmcnt(3)\n" \
  "v_mfma_f32_16x16x32_bf16 v[72:75], a[4:7], v[36:39], v[72:75]\n" \
  "ds_read_b128 v[52:55], v1 offset:320\n" \
  "v_add_f32_dpp v24, v24, v24 row_ror:4 row_mask:0xf bank_mask:0xf\n" \
  "v_add_f32_dpp v26, v26, v26 row_ror:4 row_mask:0xf bank_mask:0xf\n" \
  "v_mfma_f32_16x16x32_bf16 v[76:79], a[60:63], v[36:39], v[76:79]\n" \
  "ds_read_b128 v[56:59], v1 offset:384\n" \
  "v_add_f32_dpp v24, v24, v24 row_ror:8 row_mask:0xf bank_mask:0xf\n" \
  "v_add_f32_dpp v26, v26, v26 row_ror:8 row_mask:0xf bank_mask:0xf\n" \
  "s_waitcnt lgkmcnt(4)\n" \
  "v_mfma_f32_16x16x32_bf16 v[72:75], a[8:11], v[40:43], v[72:75]\n" \
  "ds_read_b128 v[60:63], v1 offset:448\n" \
  "v_mov_b32 v28, v24\n" \
  "v_mov_b32 v29, v26\n" \
  "v_mfma_f32_16x16x32_bf16 v[76:79], a[64:67], v[40:43], v[76:79]\n" \
  "ds_read_b128 v[64:67], v1 offset:512\n" \
  "v_permlane16_swap_b32 v24, v28\n" \
  "s_waitcnt lgkmcnt(5)\n" \
  "v_mfma_f32_16x16x32_bf16 v[72:75], a[12:15], v[44:47], v[72:75]\n" \
  "ds_read_b128 v[68:71], v1 offset:576\n" \
  "v_permlane16_swap_b32 v26, v29\n" \
  "v_mfma_f32_16x16x32_bf16 v[76:79], a[68:71], v[44:47], v[76:79]\n" \
  "v_add_f32 v24, v24, v28\n" \
  "v_add_f32 v26, v26, v29\n" \
  "s_waitcnt lgkmcnt(5)\n" \
  "v_mfma_f32_16x16x32_bf16 v[72:75], a[16:19], v[48:51], v[72:75]\n" \
  "ds_read_b128 v[32:35], v1 offset:640\n" \
  "v_mov_b32 v28, v24\n" \
  "v_mov_b32 v29, v26\n" \
  "v_mfma_f32_16x16x32_bf16 v[76:79], a[72:75], v[48:51], v[76:79]\n" \
  "v_permlane32_swap_b32 v24, v28\n" \
  "s_waitcnt lgkmcnt(5)\n" \
  "v_mfma_f32_16x16x32_bf16 v[72:75], a[20:23], v[52:55], v[72:75]\n" \
  "ds_read_b128 v[36:39], v1 offset:704\n" \
  "v_permlane32_swap_b32 v26, v29\n" \
  "v_mfma_f32_16x16x32_bf16 v[76:79], a[76:79], v[52:55], v[76:79]\n" \
  "v_add_f32 v24, v24, v28\n" \
  "v_add_f32 v26, v26, v29\n" \
  "s_waitcnt lgkmcnt(5)\n" \
  "v_mfma_f32_16x16x32_bf16 v[72:75], a[24:27], v[56:59], v[72:75]\n" \
  "ds_read_b128 v[40:43], v1 offset:768\n" \
  "v_mul_f32 v24, 0x3b124925, v24\n" \
  "v_mul_f32 v26, 0x3b124925, v26\n" \
  "v_mfma_f32_16x16x32_bf16 v[76:79], a[80:83], v[56:59], v[76:79]\n" \
  "v_fma_f32 v26, -v24, v24, v26\n" \
  "v_max_f32 v26, 0, v26\n" \
  "s_waitcnt lgkmcnt(5)\n" \
  "v_mfma_f32_16x16x32_bf16 v[72:75], a[28:31], v[60:63], v[72:75]\n" \
  "ds_read_b128 v[44:47], v1 offset:832\n" \
  "v_add_f32 v26, 0x3727c5ac, v26\n" \
  "v_mfma_f32_16x16x32_bf16 v[76:79], a[84:87], v[60:63], v[76:79]\n" \
  "v_rsq_f32 v26, v26\n" \
  "s_waitcnt lgkmcnt(5)\n" \
  "v_mfma_f32_16x16x32_bf16 v[72:75], a[32:35], v[64:67], v[72:75]\n" \
  "v_xor_b32 v24, 0x80000000, v24\n" \
  "v_mul_f32 v26, 0x3db8aa3b, v26\n" \
  "v_mfma_f32_16x16x32_bf16 v[76:79], a[88:91], v[64:67], v[76:79]\n" \
  "v_mul_f32 v24, v26, v24\n" \
  "v_mul_f32 v80, v24, v104\n" \
  "s_waitcnt lgkmcnt(0)\n" \
  "v_mfma_f32_16x16x32_bf16 v[72:75], a[36:39], v[68:71], v[72:75]\n" \
  "v_mul_f32 v81, v24, v105\n" \
  "v_mul_f32 v82, v24, v106\n" \
  "v_mfma_f32_16x16x32_bf16 v[72:75], a[40:43], v[32:35], v[72:75]\n" \
  "v_mul_f32 v83, v24, v107\n" \
  "s_nop 0\n" \
  "v_mfma_f32_16x16x32_bf16 v[72:75], a[44:47], v[36:39], v[72:75]\n" \
  "s_nop 1\n" \
  "v_mfma_f32_16x16x32_bf16 v[72:75], a[48:51], v[40:43], v[72:75]\n" \
  "s_nop 1\n" \
  "v_mfma_f32_16x16x32_bf16 v[72:75], a[52:55], v[44:47], v[72:75]\n" \
  "s_nop 1\n" \
  "v_mfma_f32_16x16x32_bf16 v[76:79], a[92:95], v[68:71], v[76:79]\n" \
  "s_nop 1\n" \
  "v_mfma_f32_16x16x32_bf16 v[76:79], a[96:99], v[32:35], v[76:79]\n" \
  "s_nop 1\n" \
  "v_mfma_f32_16x16x32_bf16 v[76:79], a[100:103], v[36:39], v[76:79]\n" \
  "s_nop 1\n" \
  "v_mfma_f32_16x16x32_bf16 v[76:79], a[104:107], v[40:43], v[76:79]\n" \
  "s_nop 1\n" \
  "v_mfma_f32_16x16x32_bf16 v[76:79], a[108:111], v[44:47], v[76:79]\n" \
  "s_nop 7\n" \
  "v_cndmask_b32 v72, v72, v76, s[78:79]\n" \
  "v_cndmask_b32 v73, v73, v77, s[78:79]\n" \
  "v_cndmask_b32 v74, v74, v78, s[78:79]\n" \
  "v_cndmask_b32 v75, v75, v79, s[78:79]\n" \
  "v_fmac_f32 v80, v26, v72\n" \
  "v_fmac_f32 v81, v26, v73\n" \
  "v_fmac_f32 v82, v26, v74\n" \
  "v_fmac_f32 v83, v26, v75\n" \
  "v_cvt_pk_bf16_f32 v24, v80, v81\n" \
  "v_cvt_pk_bf16_f32 v25, v82, v83\n" \
  "v_min_u32 v24, -2, v24\n" \
  "v_min_u32 v25, -2, v25\n" \
  "s_mov_b64 exec, s[80:81]\n" \
  "buffer_store_dwordx2 v[24:25], v14, s[36:39], s54 offen sc1\n" \
  "s_mov_b64 exec, -1\n" \
  "s_lshl_b32 s54, s99, 11\n" \
  "v_add_u32 v26, s54, v12\n" \
  "s_mov_b64 exec, s[86:87]\n" \
  "ds_write_b128 v26, v[92:95]\n" \
  "s_mov_b64 exec, -1\n" \
  "s_cmp_ge_i32 s52, 0\n" \
  "s_cbranch_scc1 .LS_NEXT\n" \
  ".LS_OUT:\n" \
  "s_cmp_ge_u32 s62, 28\n" \
  "s_cbranch_scc1 .LS_NEXT\n" \
  "s_mul_i32 s54, s61, 28672\n" \
  "s_add_u32 s54, s54, 1482752\n" \
  "s_mul_i32 s59, s61, 896\n" \
  "ds_read_b128 v[40:43], v109\n" \
  ".LS_GO:\n" \
  "buffer_load_dwordx4 v[32:35], v108, s[36:39], s54 offen sc1\n" \
  "s_waitcnt vmcnt(0)\n" \
  "v_mfma_f32_16x16x32_bf16 v[36:39], v[96:99], v[32:35], 0\n" \
  "v_max_u32 v24, v32, v33\n" \
  "v_max3_u32 v24, v24, v34, v35\n" \
  "v_cmp_eq_u32 vcc, -1, v24\n" \
  "s_nop 3\n" \
  "s_cbranch_vccz .LS_OK4\n" \
  "s_sub_u32 s53, s53, 1\n" \
  "s_cbranch_scc0 .LS_GO\n" \
  "s_mov_b32 s53, 0\n" \
  ".LS_OK4:\n" \
  "v_cndmask_b32 v36, 0, v36, s[76:77]\n" \
  "v_cndmask_b32 v37, 0, v37, s[76:77]\n" \
  "v_cndmask_b32 v38, 0, v38, s[76:77]\n" \
  "v_cndmask_b32 v39, 0, v39, s[76:77]\n" \
  "v_add_f32_dpp v36, v36, v36 quad_perm:[2,3,0,1] row_mask:0xf bank_mask:0xf\n" \
  "v_add_f32_dpp v37, v37, v37 quad_perm:[2,3,0,1] row_mask:0xf bank_mask:0xf\n" \
  "v_add_f32_dpp v38, v38, v38 quad_perm:[2,3,0,1] row_mask:0xf bank_mask:0xf\n" \
  "v_add_f32_dpp v39, v39, v39 quad_perm:[2,3,0,1] row_mask:0xf bank_mask:0xf\n" \
  "v_add_f32_dpp v36, v36, v36 row_ror:4 row_mask:0xf bank_mask:0xf\n" \
  "v_add_f32_dpp v37, v37, v37 row_ror:4 row_mask:0xf bank_mask:0xf\n" \
  "v_add_f32_dpp v38, v38, v38 row_ror:4 row_mask:0xf bank_mask:0xf\n" \
  "v_add_f32_dpp v39, v39, v39 row_ror:4 row_mask:0xf bank_mask:0xf\n" \
  "v_add_f32_dpp v36, v36, v36 row_ror:8 row_mask:0xf bank_mask:0xf\n" \
  "v_add_f32_dpp v37, v37, v37 row_ror:8 row_mask:0xf bank_mask:0xf\n" \
  "v_add_f32_dpp v38, v38, v38 row_ror:8 row_mask:0xf bank_mask:0xf\n" \
  "v_add_f32_dpp v39, v39, v39 row_ror:8 row_mask:0xf bank_mask:0xf\n" \
  "s_waitcnt lgkmcnt(0)\n" \
  "v_add_f32 v36, v36, v40\n" \
  "v_add_f32 v37, v37, v41\n" \
  "v_add_f32 v38, v38, v42\n" \
  "v_add_f32 v39, v39, v43\n" \
  "v_cvt_pk_bf16_f32 v24, v36, v37\n" \
  "v_cvt_pk_bf16_f32 v25, v38, v39\n" \
  "s_mov_b64 exec, s[82:83]\n" \
  "buffer_store_dwordx2 v[24:25], v110, s[48:51], s59 offen sc1\n" \
  "s_mov_b64 exec, -1\n" \
  "s_xor_b32 s54, s61, 3\n" \
  "s_or_b32 s54, s62, s54\n" \
  "s_cmp_lg_u32 s54, 0\n" \
  "s_cbranch_scc1 .LS_NEXT\n" \
  "v_mov_b32 v24, s96\n" \
  "v_mov_b32 v25, s97\n" \
  "s_mov_b64 exec, 1\n" \
  "buffer_store_dwordx2 v[24:25], off, s[36:39], 0 sc1\n" \
  "s_mov_b64 exec, -1\n" \
  ".LS_NEXT:\n" \
  "v_mbcnt_lo_u32_b32 v31, -1, 0\n" \
  "v_lshl_add_u32 v31, v31, 7, s27\n" \
  "v_min_u32 v31, s3, v31\n" \
  "s_mov_b64 exec, 0xfffff\n" \
  "global_load_dword v30, v31, s[0:1]\n" \
  "s_mov_b64 exec, -1\n" \
  "s_add_u32 s52, s52, 1\n" \
  "s_mov_b32 s38, s31\n" \
  "s_mov_b32 s50, 3584\n" \
  "s_waitcnt lgkmcnt(0)\n" \
  "s_cmp_lt_u32 s52, 5\n" \
  "s_cbranch_scc1 .LS_ROUND\n" \
  "s_waitcnt vmcnt(0)\n" \
  "s_endpgm\n" \
  ".LS_LDQ:\n" \
  "s_mov_b32 s54, 14\n" \
  ".LS_LDQL:\n" \
  "buffer_load_dwordx4 v26, s[40:43], s56 offen lds\n" \
  "s_add_u32 m0, m0, 4096\n" \
  "s_add_u32 s56, s56, 131072\n" \
  "s_sub_u32 s54, s54, 1\n" \
  "s_cmp_lg_u32 s54, 0\n" \
  "s_cbranch_scc1 .LS_LDQL\n" \
  "s_setpc_b64 s[88:89]\n" \
  ".LS_LDP:\n" \
  "s_mov_b32 s54, 4\n" \
  ".LS_LDPL:\n" \
  "buffer_load_dwordx4 v18, s[40:43], s56 offen lds\n" \
  "s_add_u32 m0, m0, 1024\n" \
  "s_add_u32 s56, s56, 3584\n" \
  "buffer_load_dwordx4 v18, s[40:43], s56 offen lds\n" \
  "s_add_u32 m0, m0, 1024\n" \
  "s_add_u32 s56, s56, 3584\n" \
  "buffer_load_dwordx4 v19, s[40:43], s56 offen lds\n" \
  "s_add_u32 m0, m0, 1024\n" \
  "s_add_u32 s56, s56, 3584\n" \
  "buffer_load_dwordx4 v19, s[40:43], s56 offen lds\n" \
  "s_add_u32 m0, m0, 1024\n" \
  "s_add_u32 s56, s56, 3584\n" \
  "s_sub_u32 s54, s54, 1\n" \
  "s_cmp_lg_u32 s54, 0\n" \
  "s_cbranch_scc1 .LS_LDPL\n" \
  "s_setpc_b64 s[88:89]\n" \
  ".LS_LDG:\n" \
  "s_mov_b32 s54, 7\n" \
  ".LS_LDGL:\n" \
  "buffer_load_dwordx4 v22, s[40:43], s56 offen lds\n" \
  "s_add_u32 m0, m0, 28672\n" \
  "s_nop 0\n" \
  "buffer_load_dwordx4 v22, s[44:47], s56 offen lds\n" \
  "s_add_u32 m0, m0, 4294939648\n" \
  "s_add_u32 s56, s56, 28672\n" \
  "s_sub_u32 s54, s54, 1\n" \
  "s_cmp_lg_u32 s54, 0\n" \
  "s_cbranch_scc1 .LS_LDGL\n" \
  "s_setpc_b64 s[88:89]\n" \
  ".LS_WO:\n" \
  "s_mov_b64 s[94:95], -1\n" \
  "v_mov_b32 v48, 0xff800000\n" \
  "v_mov_b32 v49, 0\n" \
  "s_add_u32 s59, s58, 5120\n" \
  "s_nop 0\n" \
  ".LS_WOP:\n" \
  "s_mov_b64 exec, s[74:75]\n" \
  "buffer_load_dwordx2 v[48:49], v5, s[36:39], s58 offen offset:512 sc1\n" \
  "s_mov_b64 exec, s[94:95]\n" \
  "buffer_load_dwordx4 v[52:55], v4, s[36:39], s58 offen sc1\n" \
  "buffer_load_dwordx4 v[56:59], v4, s[36:39], s58 offen offset:2560 sc1\n" \
  "buffer_load_dwordx4 v[60:63], v4, s[36:39], s59 offen sc1\n" \
  "buffer_load_dwordx4 v[64:67], v111, s[36:39], s59 offen sc1\n" \
  "s_mov_b64 exec, -1\n" \
  "s_waitcnt vmcnt(3)\n" \
  "ds_write_b128 v15, v[52:55] offset:0\n" \
  "v_max_u32 v24, v48, v49\n" \
  "v_max3_u32 v24, v24, v52, v53\n" \
  "v_max3_u32 v24, v24, v54, v55\n" \
  "s_waitcnt vmcnt(2)\n" \
  "ds_write_b128 v15, v[56:59] offset:1088\n" \
  "v_max3_u32 v24, v24, v56, v57\n" \
  "v_max3_u32 v24, v24, v58, v59\n" \
  "s_waitcnt vmcnt(1)\n" \
  "ds_write_b128 v15, v[60:63] offset:2176\n" \
  "v_max3_u32 v24, v24, v60, v61\n" \
  "v_max3_u32 v24, v24, v62, v63\n" \
  "s_waitcnt vmcnt(0)\n" \
  "ds_write_b128 v15, v[64:67] offset:3264\n" \
  "ds_read_b128 v[32:35], v27 offset:0\n" \
  "ds_read_b128 v[36:39], v27 offset:64\n" \
  "ds_read_b128 v[40:43], v27 offset:128\n" \
  "ds_read_b128 v[44:47], v27 offset:192\n" \
  "v_max3_u32 v24, v24, v64, v65\n" \
  "v_max3_u32 v24, v24, v66, v67\n" \
  "v_cmp_eq_u32 vcc, -1, v24\n" \
  "v_max_f32_dpp v25, v48, v48 quad_perm:[1,0,3,2] row_mask:0xf bank_mask:0xf\n" \
  "s_nop 0\n" \
  "s_cbranch_vccz .LS_WOK\n" \
  "s_mov_b64 s[94:95], vcc\n" \
  "v_cmp_eq_u32 vcc, -1, v48\n" \
  "s_nop 1\n" \
  ".LS_WOC:\n" \
  "s_sub_u32 s53, s53, 1\n" \
  "s_cbranch_scc1 .LS_WOX\n" \
  "s_bcnt1_i32_b64 s54, vcc\n" \
  "s_cmp_le_u32 s54, s70\n" \
  "s_cbranch_scc1 .LS_WOP\n" \
  "s_sleep 6\n" \
  "v_mbcnt_lo_u32_b32 v31, -1, 0\n" \
  "v_lshl_add_u32 v31, v31, 7, s27\n" \
  "v_min_u32 v31, s3, v31\n" \
  "s_mov_b64 exec, 0xfffff\n" \
  "global_load_dword v30, v31, s[0:1]\n" \
  "s_mov_b64 exec, -1\n" \
  "s_mov_b64 exec, s[74:75]\n" \
  "buffer_load_dword v48, v5, s[36:39], s58 offen offset:512 sc1\n" \
  "s_mov_b64 exec, -1\n" \
  "s_waitcnt vmcnt(0)\n" \
  "v_cmp_eq_u32 vcc, -1, v48\n" \
  "s_nop 1\n" \
  "s_branch .LS_WOC\n" \
  ".LS_WOX:\n" \
  "s_mov_b32 s53, 0\n" \
  ".LS_WOK:\n" \
  "v_max_f32_dpp v24, v25, v25 quad_perm:[2,3,0,1] row_mask:0xf bank_mask:0xf\n" \
  "v_mov_b32 v25, v24\n" \
  "s_nop 1\n" \
  "v_permlane16_swap_b32 v24, v25\n" \
  "v_max_f32 v24, v24, v25\n" \
  "v_mov_b32 v25, v24\n" \
  "s_nop 1\n" \
  "v_permlane32_swap_b32 v24, v25\n" \
  "v_max_f32 v24, v24, v25\n" \
  "s_waitcnt lgkmcnt(0)\n" \
  "v_mfma_f32_16x16x32_bf16 v[52:55], v[32:35], a[112:115], 0\n" \
  "v_sub_f32 v48, v48, v24\n" \
  "v_exp_f32 v48, v48\n" \
  "v_mfma_f32_16x16x32_bf16 v[52:55], v[36:39], a[116:119], v[52:55]\n" \
  "v_mul_f32 v49, v48, v49\n" \
  "v_mov_b32_dpp v28, v48 row_newbcast:0 row_mask:0xf bank_mask:0xf\n" \
  "v_mfma_f32_16x16x32_bf16 v[52:55], v[40:43], a[120:123], v[52:55]\n" \
  "v_add_f32_dpp v49, v49, v49 quad_perm:[1,0,3,2] row_mask:0xf bank_mask:0xf\n" \
  "v_mov_b32_dpp v29, v48 row_newbcast:1 row_mask:0xf bank_mask:0xf\n" \
  "v_mfma_f32_16x16x32_bf16 v[52:55], v[44:47], a[124:127], v[52:55]\n" \
  "v_add_f32_dpp v49, v49, v49 quad_perm:[2,3,0,1] row_mask:0xf bank_mask:0xf\n" \
  "v_mov_b32_dpp v50, v48 row_newbcast:2 row_mask:0xf bank_mask:0xf\n" \
  "v_mfma_f32_16x16x32_bf16 v[68:71], v[32:35], a[176:179], 0\n" \
  "v_mov_b32_dpp v26, v49 row_newbcast:0 row_mask:0xf bank_mask:0xf\n" \
  "v_mov_b32 v25, v26\n" \
  "v_mfma_f32_16x16x32_bf16 v[68:71], v[36:39], a[180:183], v[68:71]\n" \
  "v_mov_b32_dpp v51, v48 row_newbcast:3 row_mask:0xf bank_mask:0xf\n" \
  "v_permlane16_swap_b32 v26, v25\n" \
  "v_mfma_f32_16x16x32_bf16 v[68:71], v[40:43], a[184:187], v[68:71]\n" \
  "v_mul_f32 v80, v28, v52\n" \
  "v_fmac_f32 v80, v29, v53\n" \
  "v_mfma_f32_16x16x32_bf16 v[68:71], v[44:47], a[188:191], v[68:71]\n" \
  "v_fmac_f32 v80, v50, v54\n" \
  "v_mov_b32 v87, 0\n" \
  "v_mfma_f32_16x16x32_bf16 v[56:59], v[32:35], a[128:131], 0\n" \
  "v_add_f32 v26, v26, v25\n" \
  "v_fmac_f32 v80, v51, v55\n" \
  "v_mfma_f32_16x16x32_bf16 v[56:59], v[36:39], a[132:135], v[56:59]\n" \
  "v_mov_b32 v25, v26\n" \
  "s_nop 0\n" \
  "v_mfma_f32_16x16x32_bf16 v[56:59], v[40:43], a[136:139], v[56:59]\n" \
  "v_mul_f32 v84, v28, v68\n" \
  "v_fmac_f32 v84, v29, v69\n" \
  "v_mfma_f32_16x16x32_bf16 v[56:59], v[44:47], a[140:143], v[56:59]\n" \
  "v_fmac_f32 v84, v50, v70\n" \
  "v_fmac_f32 v84, v51, v71\n" \
  "v_mfma_f32_16x16x32_bf16 v[72:75], v[32:35], a[192:195], 0\n" \
  "v_permlane32_swap_b32 v26, v25\n" \
  "v_mfma_f32_16x16x32_bf16 v[72:75], v[36:39], a[196:199], v[72:75]\n" \
  "v_permlane32_swap_b32 v80, v84\n" \
  "v_mfma_f32_16x16x32_bf16 v[72:75], v[40:43], a[200:203], v[72:75]\n" \
  "v_add_f32 v80, v80, v84\n" \
  "v_mul_f32 v81, v28, v56\n" \
  "v_mfma_f32_16x16x32_bf16 v[72:75], v[44:47], a[204:207], v[72:75]\n" \
  "v_fmac_f32 v81, v29, v57\n" \
  "v_fmac_f32 v81, v50, v58\n" \
  "v_mfma_f32_16x16x32_bf16 v[64:67], v[32:35], a[160:163], 0\n" \
  "v_fmac_f32 v81, v51, v59\n" \
  "v_add_f32 v26, v26, v25\n" \
  "v_mfma_f32_16x16x32_bf16 v[64:67], v[36:39], a[164:167], v[64:67]\n" \
  "v_rcp_f32 v26, v26\n" \
  "v_mfma_f32_16x16x32_bf16 v[64:67], v[40:43], a[168:171], v[64:67]\n" \
  "v_mul_f32 v85, v28, v72\n" \
  "v_fmac_f32 v85, v29, v73\n" \
  "v_mfma_f32_16x16x32_bf16 v[64:67], v[44:47], a[172:175], v[64:67]\n" \
  "v_fmac_f32 v85, v50, v74\n" \
  "v_fmac_f32 v85, v51, v75\n" \
  "v_mfma_f32_16x16x32_bf16 v[60:63], v[32:35], a[144:147], 0\n" \
  "s_nop 0\n" \
  "v_permlane32_swap_b32 v81, v85\n" \
  "v_mfma_f32_16x16x32_bf16 v[60:63], v[36:39], a[148:151], v[60:63]\n" \
  "v_add_f32 v81, v81, v85\n" \
  "s_nop 0\n" \
  "v_mfma_f32_16x16x32_bf16 v[60:63], v[40:43], a[152:155], v[60:63]\n" \
  "v_mul_f32 v83, v28, v64\n" \
  "v_fmac_f32 v83, v29, v65\n" \
  "v_mfma_f32_16x16x32_bf16 v[60:63], v[44:47], a[156:159], v[60:63]\n" \
  "v_fmac_f32 v83, v50, v66\n" \
  "v_fmac_f32 v83, v51, v67\n" \
  "v_mfma_f32_16x16x32_bf16 v[76:79], v[32:35], a[208:211], 0\n" \
  "s_nop 0\n" \
  "v_permlane32_swap_b32 v83, v87\n" \
  "v_mfma_f32_16x16x32_bf16 v[76:79], v[36:39], a[212:215], v[76:79]\n" \
  "v_add_f32 v83, v83, v87\n" \
  "s_nop 0\n" \
  "v_mfma_f32_16x16x32_bf16 v[76:79], v[40:43], a[216:219], v[76:79]\n" \
  "v_mul_f32 v82, v28, v60\n" \
  "v_fmac_f32 v82, v29, v61\n" \
  "v_mfma_f32_16x16x32_bf16 v[76:79], v[44:47], a[220:223], v[76:79]\n" \
  "v_fmac_f32 v82, v50, v62\n" \
  "v_fmac_f32 v82, v51, v63\n" \
  "v_permlane16_swap_b32 v81, v83\n" \
  "v_add_f32 v81, v81, v83\n" \
  "v_mul_f32 v81, v26, v81\n" \
  "ds_write_b32 v10, v81 offset:64\n" \
  "s_nop 1\n" \
  "v_mul_f32 v86, v28, v76\n" \
  "v_fmac_f32 v86, v29, v77\n" \
  "v_fmac_f32 v86, v50, v78\n" \
  "v_fmac_f32 v86, v51, v79\n" \
  "s_nop 1\n" \
  "v_permlane32_swap_b32 v82, v86\n" \
  "v_add_f32 v82, v82, v86\n" \
  "s_nop 1\n" \
  "v_permlane16_swap_b32 v80, v82\n" \
  "v_add_f32 v80, v80, v82\n" \
  "v_mul_f32 v80, v26, v80\n" \
  "ds_write_b32 v10, v80 offset:0\n" \
  "s_waitcnt lgkmcnt(0)\n" \
  "s_barrier\n" \
  "s_cmp_lg_u32 s61, 0\n" \
  "s_cbranch_scc1 .LS_WO_RET\n" \
  "ds_read_b64 v[32:33], v11 offset:0\n" \
  "ds_read_b64 v[34:35], v11 offset:512\n" \
  "ds_read_b64 v[36:37], v11 offset:1024\n" \
  "ds_read_b64 v[38:39], v11 offset:1536\n" \
  "v_bfe_u32 v25, v11, 1, 8\n" \
  "s_waitcnt lgkmcnt(2)\n" \
  "v_add_f32 v32, v32, v34\n" \
  "v_add_f32 v33, v33, v35\n" \
  "s_waitcnt lgkmcnt(0)\n" \
  "v_add_f32 v36, v36, v38\n" \
  "v_add_f32 v37, v37, v39\n" \
  "v_add_f32 v32, v32, v36\n" \
  "v_add_f32 v33, v33, v37\n" \
  "v_cvt_pk_bf16_f32 v24, v32, v33\n" \
  "v_min_u32 v24, -2, v24\n" \
  "s_mov_b32 exec_hi, 0x00ffffff\n" \
  "buffer_store_dword v24, v25, s[36:39], s60 offen sc1\n" \
  "s_mov_b64 exec, -1\n" \
  ".LS_WO_RET:\n" \
  "v_xor_b32 v10, 0x800, v10\n" \
  "v_xor_b32 v11, 0x800, v11\n" \
  "s_nop 0\n" \
  "s_setpc_b64 s[88:89]\n" \
  ".LS_END:\n"
#define KERNEL_CLOBBERS "v255", "a255", "s3", "s4", "s5", "s6", "s7", "s8", "s9", "s10", "s11", "s12", "s13", "s14", "s15", "s16", "s17", "s18", "s19", "s20", "s21", "s22", "s23", "s24", "s25", "s26", "s27", "s28", "s29", "s30", "s31", "s33", "s34", "s35", "s36", "s37", "s38", "s39", "s40", "s41", "s42", "s43", "s44", "s45", "s46", "s47", "s48", "s49", "s50", "s51", "s52", "s53", "s54", "s55", "s56", "s57", "s58", "s59", "s60", "s61", "s62", "s63", "s64", "s65", "s66", "s67", "s68", "s69", "s70", "s71", "s72", "s73", "s74", "s75", "s76", "s77", "s78", "s79", "s80", "s81", "s82", "s83", "s84", "s85", "s86", "s87", "s88", "s89", "s90", "s91", "s92", "s93", "s94", "s95", "s96", "s97", "s98", "s99", "vcc", "scc", "memory"
__global__ void __launch_bounds__(256, 1) fused_kernel(const u16* xin, const u16* K, const u16* V, const u16* wq, const u16* wo,
                                                       const u16* wg, const u16* wu, const u16* wd, u16* out, unsigned char* ws) {
  asm volatile(KERNEL_ASM : : "s"(__builtin_amdgcn_kernarg_segment_ptr()), "s"(blockIdx.x), "v"(threadIdx.x) : KERNEL_CLOBBERS);
}

extern "C" void kernel_launch(void* const* d_in, const int* in_sizes, int n_in, void* d_out, int out_size, void* d_ws, size_t ws_size,
                              hipStream_t stream) {
  (void)hipFuncSetAttribute((const void*)fused_kernel, hipFuncAttributeMaxDynamicSharedMemorySize, LDS_BYTES);
  fused_kernel<<<256, 256, LDS_BYTES, stream>>>((const u16*)d_in[0], (const u16*)d_in[1], (const u16*)d_in[2], (const u16*)d_in[3],
                                                (const u16*)d_in[4], (const u16*)d_in[6], (const u16*)d_in[7], (const u16*)d_in[8],
                                                (u16*)d_out, (unsigned char*)d_ws);
}
```
